```python
import torch

def _softmax(x, axis=-1):
    return torch.softmax(x, dim=axis)
def _gelu(x):
    return torch.nn.functional.gelu(x, approximate="tanh")
def _silu(x):
    return torch.nn.functional.silu(x)
def _sigmoid(x):
    return torch.sigmoid(x)
def _layernorm(x, axis=-1, eps=1e-5):
    m = x.mean(dim=axis, keepdim=True)
    v = x.var(dim=axis, keepdim=True, unbiased=False)
    return (x - m) / torch.sqrt(v + eps)
def _scatter_add(tgt, idx, src, axis=0):
    return tgt.clone().index_add_(axis, idx, src)
def _segment_sum(src, seg, n):
    out = torch.zeros((n,) + src.shape[1:], dtype=src.dtype)
    return out.index_add_(0, seg, src)
def _pad(x, pw):
    flat = tuple(v for pair in reversed(pw) for v in pair)
    return torch.nn.functional.pad(x, flat)

kernel_name = 'standalone__gqa_softmax__near_ridge'

def setup_inputs():

    _w = (lambda a: (lambda t: t.to(torch.bfloat16) if t.is_floating_point() else t)(torch.as_tensor(a)))
    B, Hq, Hkv, Sq, Sk, D = 16, 32, 32, 384, 384, 128
    return {
        "q": _w((lambda *s: torch.randn(*s, dtype=torch.bfloat16) * 0.8)(B, Hq, Sq, D)),
        "k": _w((lambda *s: torch.randn(*s, dtype=torch.bfloat16) * 0.8)(B, Hkv, Sk, D)),
        "v": _w((lambda *s: torch.randn(*s, dtype=torch.bfloat16) * 0.05)(B, Hkv, Sk, D)),
    }

def reference(q, k, v):
    B, Hq, Sq, D = q.shape
    Hkv = k.shape[1]
    rep = Hq // Hkv
    k = (lambda x, n, axis: torch.repeat_interleave(x, n, dim=axis))(k, rep, axis=1)
    v = (lambda x, n, axis: torch.repeat_interleave(x, n, dim=axis))(v, rep, axis=1)
    scale = D ** -0.5
    scores = torch.einsum("bhqd,bhkd->bhqk", q, k) * scale
    p = _softmax(scores, axis=-1)
    return torch.einsum("bhqk,bhkd->bhqd", p, v)
```

```cpp
#include <hip/hip_runtime.h>
#include <hip/hip_bf16.h>

using bf16 = __hip_bfloat16;
constexpr int D = 128, NW = 4, QBLK = 32, KVBLK = 64, S = 384, NH = 512;
constexpr float SCALE = 0.088388347648318440f;
constexpr float THR = 8.f;
constexpr size_t SHM_V = KVBLK * D * 2, SHM_K = KVBLK * D * 2, SHM_ATTN = 2 * SHM_V + 2 * SHM_K + NW * 64 * 4;
using bf16x8 = __attribute__((ext_vector_type(8))) short;
using s16x4  = __attribute__((ext_vector_type(4))) short;
using f32x16 = __attribute__((ext_vector_type(16))) float;
using u32x4  = __attribute__((ext_vector_type(4))) unsigned;
#define KSWZ(row, colB) ((row) * 256 + ((colB) ^ (((row) & 7) << 4)))
#define SBAR() __builtin_amdgcn_sched_barrier(0)
__device__ __forceinline__ int crow(int r, int hi) { return (r & 3) + 8 * (r >> 2) + 4 * hi; }
__device__ __forceinline__ unsigned cvtpk(float lo, float hi) {
  unsigned r; asm volatile("v_cvt_pk_bf16_f32 %0, %1, %2" : "=v"(r) : "v"(lo), "v"(hi)); return r;
}
__device__ __forceinline__ void partialSM(f32x16& p0, f32x16& p1, float& m_reg, float& mn, float& alpha) {
  constexpr float C = SCALE * 1.4426950408889634f;
  float pmax = p0[0]; for (int r = 1; r < 16; ++r) pmax = fmaxf(pmax, p0[r]); for (int r = 0; r < 16; ++r) pmax = fmaxf(pmax, p1[r]);
  { auto rr = __builtin_amdgcn_permlane32_swap(__float_as_uint(pmax), __float_as_uint(pmax), false, false);
    pmax = fmaxf(__uint_as_float(rr[0]), __uint_as_float(rr[1])); }
  if (__builtin_expect(__all(pmax - m_reg <= THR / SCALE), 1)) { mn = m_reg; alpha = 1.f; }
  else { mn = fmaxf(m_reg, pmax); alpha = __builtin_amdgcn_exp2f((m_reg - mn) * C); m_reg = mn; }
  float mnC = -mn * C;
  for (int r = 0; r < 16; ++r) p0[r] = fmaf(p0[r], C, mnC); for (int r = 0; r < 16; ++r) p1[r] = fmaf(p1[r], C, mnC);
  for (int r = 0; r < 16; ++r) p0[r] = __builtin_amdgcn_exp2f(p0[r]);
}
__device__ __forceinline__ void finishSM(f32x16& p0, f32x16& p1, float alpha, float& l_reg, bf16x8& pa0, bf16x8& pa1, bf16x8& pa2, bf16x8& pa3) {
  for (int r = 0; r < 16; ++r) p1[r] = __builtin_amdgcn_exp2f(p1[r]);
  float ps = 0; for (int r = 0; r < 16; ++r) ps += p0[r]; for (int r = 0; r < 16; ++r) ps += p1[r];
  { auto rr = __builtin_amdgcn_permlane32_swap(__float_as_uint(ps), __float_as_uint(ps), false, false);
    ps = __uint_as_float(rr[0]) + __uint_as_float(rr[1]); }
  l_reg = l_reg * alpha + ps;
#define PK4(P, BASE, OUT) do { unsigned a0 = cvtpk(P[BASE + 0], P[BASE + 1]), a1 = cvtpk(P[BASE + 2], P[BASE + 3]);   \
    unsigned b0 = cvtpk(P[BASE + 4], P[BASE + 5]), b1 = cvtpk(P[BASE + 6], P[BASE + 7]);                              \
    auto r0 = __builtin_amdgcn_permlane32_swap(a0, b0, false, false); auto r1 = __builtin_amdgcn_permlane32_swap(a1, b1, false, false); \
    u32x4 w = {r0[0], r1[0], r0[1], r1[1]}; OUT = *reinterpret_cast<bf16x8*>(&w); } while (0)
  PK4(p0, 0, pa0); PK4(p0, 8, pa1); PK4(p1, 0, pa2); PK4(p1, 8, pa3);
#undef PK4
}
__device__ __forceinline__ void qkt(f32x16& p0, f32x16& p1, const bf16* Ks, const bf16x8* qr, int r32, int hi) {
  p0 = f32x16{}; p1 = f32x16{};
  for (int d0 = 0; d0 < 8; ++d0) { int cb = (d0 * 16 + hi * 8) * 2;
    bf16x8 b0 = *reinterpret_cast<const bf16x8*>((const char*)Ks + KSWZ(r32, cb));
    bf16x8 b1 = *reinterpret_cast<const bf16x8*>((const char*)Ks + KSWZ(32 + r32, cb));
    p0 = __builtin_amdgcn_mfma_f32_32x32x16_bf16(b0, qr[d0], p0, 0, 0, 0);
    p1 = __builtin_amdgcn_mfma_f32_32x32x16_bf16(b1, qr[d0], p1, 0, 0, 0); }
}
__device__ __forceinline__ int v_st(int k, int c) { const int kk = (k & ~0xC) | ((k & 4) << 1) | ((k & 8) >> 1); return ((kk >> 3) * 4 + (c >> 5)) * 512 + ((kk & 7) * 32 + (c & 31)) * 2; }
__device__ __forceinline__ int v_rd_base(int lane) { return ((lane & 3) << 3) | (((lane >> 2) & 3) << 6) | (((lane >> 4) & 1) << 5) | (((lane >> 5) & 1) << 8); }
constexpr int v_rd_off(int d0, int ks, int half) { return d0 * 512 + ks * 4096 + half * 2048; }
template <int OFF> __device__ __forceinline__ s16x4 tr_read(int vb) {
  s16x4 r; asm volatile("ds_read_b64_tr_b16 %0, %1 offset:%2" : "=&v"(r) : "v"(vb), "i"(OFF) : "memory"); return r;
}
template <int D0> __device__ __forceinline__ void pv_one(f32x16& od, int vb, bf16x8 pa0, bf16x8 pa1, bf16x8 pa2, bf16x8 pa3) {
  const s16x4 l0 = tr_read<v_rd_off(D0, 0, 0)>(vb), h0 = tr_read<v_rd_off(D0, 0, 1)>(vb), l1 = tr_read<v_rd_off(D0, 1, 0)>(vb), h1 = tr_read<v_rd_off(D0, 1, 1)>(vb);
  const s16x4 l2 = tr_read<v_rd_off(D0, 2, 0)>(vb), h2 = tr_read<v_rd_off(D0, 2, 1)>(vb), l3 = tr_read<v_rd_off(D0, 3, 0)>(vb), h3 = tr_read<v_rd_off(D0, 3, 1)>(vb);
  asm volatile("s_waitcnt lgkmcnt(0)" ::: "memory"); SBAR();
#define PK(L, H) (bf16x8){L[0], L[1], L[2], L[3], H[0], H[1], H[2], H[3]}
  od = __builtin_amdgcn_mfma_f32_32x32x16_bf16(pa0, PK(l0, h0), od, 0, 0, 0);
  od = __builtin_amdgcn_mfma_f32_32x32x16_bf16(pa1, PK(l1, h1), od, 0, 0, 0);
  od = __builtin_amdgcn_mfma_f32_32x32x16_bf16(pa2, PK(l2, h2), od, 0, 0, 0);
  od = __builtin_amdgcn_mfma_f32_32x32x16_bf16(pa3, PK(l3, h3), od, 0, 0, 0);
#undef PK
}
__device__ __forceinline__ void pv_d0(f32x16* o, int vb, bf16x8 pa0, bf16x8 pa1, bf16x8 pa2, bf16x8 pa3) {
  pv_one<0>(o[0], vb, pa0, pa1, pa2, pa3); pv_one<1>(o[1], vb, pa0, pa1, pa2, pa3); pv_one<2>(o[2], vb, pa0, pa1, pa2, pa3); pv_one<3>(o[3], vb, pa0, pa1, pa2, pa3);
}
__global__ __launch_bounds__(64 * NW, 2)
void attn_fwd(const bf16* __restrict__ Q, const bf16* __restrict__ K, const bf16* __restrict__ V, bf16* __restrict__ O) {
  extern __shared__ __attribute__((aligned(16))) char lds[];
  const int bid = blockIdx.x;
  const int xcd = bid & 7, j = bid >> 3;
  const int head = xcd * 64 + j / 3, qb = j % 3;
  const bf16* Qb = Q + ((long)head * S + qb * 128) * D;
  const bf16* Kh = K + (long)head * S * D;
  const bf16* Vh = V + (long)head * S * D;
  bf16* Ob = O + ((long)head * S + qb * 128) * D;
  const int seq = S;
  const int tid = threadIdx.x, wid = tid >> 6, lane = tid & 63, r32 = lane & 31, hi = lane >> 5;
  bf16* V_lds = (bf16*)lds; bf16* K_lds = (bf16*)(lds + 2 * SHM_V);
  float* ws = (float*)(lds + 2 * SHM_V + 2 * SHM_K) + wid * 64; float* li_l = ws; float* al_l = ws + 32;
  float m_reg = -1e30f, l_reg = 0; f32x16 o[4] = {}; bf16x8 qr[8];
  const bf16* Qw = Qb + (long)(wid * QBLK + r32) * D + hi * 8;
#pragma unroll
  for (int d0 = 0; d0 < 8; ++d0) qr[d0] = *reinterpret_cast<const bf16x8*>(Qw + d0 * 16);
  const int sr = tid >> 4, sc = (tid & 15) * 8;
  int vst[4];
#pragma unroll
  for (int i = 0; i < 4; ++i) vst[i] = v_st(16 * i + sr, sc);
  const int vb0 = (int)(uintptr_t)V_lds + v_rd_base(lane);
  bf16x8 vs[4], ks[4];
#define SLOAD(k0) do { _Pragma("unroll") for (int i = 0; i < 4; ++i) { vs[i] = *reinterpret_cast<const bf16x8*>(&Vh[(long)((k0) + 16 * i + sr) * D + sc]); \
    ks[i] = *reinterpret_cast<const bf16x8*>(&Kh[(long)((k0) + 16 * i + sr) * D + sc]); } } while (0)
#define SWRITE(b) do { _Pragma("unroll") for (int i = 0; i < 4; ++i) { *(bf16x8*)((char*)V_lds + (b) * SHM_V + vst[i]) = vs[i]; \
    *(bf16x8*)((char*)K_lds + (b) * SHM_K + KSWZ(16 * i + sr, sc * 2)) = ks[i]; } } while (0)
#define RESC(a) do { if (__any((a) < 1.f)) { if (hi == 0) al_l[r32] = (a); asm volatile("s_waitcnt lgkmcnt(0)" ::: "memory"); \
    for (int d = 0; d < 4; ++d) for (int r = 0; r < 16; ++r) o[d][r] *= al_l[crow(r, hi)]; } } while (0)
  f32x16 p0, p1; float mn, al; bf16x8 pa0, pa1, pa2, pa3; const int NT = seq / KVBLK;
  SLOAD(0); SWRITE(0); __syncthreads();
  for (int t = 0; t < NT; ++t) {
    const int b = t & 1;
    if (t + 1 < NT) SLOAD((t + 1) * KVBLK);
    qkt(p0, p1, (bf16*)((char*)K_lds + b * SHM_K), qr, r32, hi);
    partialSM(p0, p1, m_reg, mn, al);
    RESC(al);
    finishSM(p0, p1, al, l_reg, pa0, pa1, pa2, pa3); SBAR();
    pv_d0(o, vb0 + b * (int)SHM_V, pa0, pa1, pa2, pa3);
    if (t + 1 < NT) SWRITE(b ^ 1);
    __syncthreads();
  }
  if (hi == 0) li_l[r32] = l_reg; asm volatile("s_waitcnt lgkmcnt(0)" ::: "memory");
  float rli[16];
#pragma unroll
  for (int r = 0; r < 16; ++r) rli[r] = __builtin_amdgcn_rcpf(li_l[crow(r, hi)]);
  bf16* Ow = Ob + (long)(wid * QBLK) * D;
#pragma unroll
  for (int r = 0; r < 16; ++r) { int orow = crow(r, hi);
    for (int d0 = 0; d0 < 4; ++d0) Ow[(long)orow * D + d0 * 32 + r32] = __float2bfloat16(o[d0][r] * rli[r]); }
}

extern "C" void kernel_launch(void* const* d_in, const int* in_sizes, int n_in, void* d_out, int out_size, void* d_ws, size_t ws_size, hipStream_t stream) {
  hipFuncSetAttribute((const void*)attn_fwd, hipFuncAttributeMaxDynamicSharedMemorySize, (int)SHM_ATTN);
  hipLaunchKernelGGL(attn_fwd, dim3(1536), dim3(64 * NW), SHM_ATTN, stream,
                     (const bf16*)d_in[0], (const bf16*)d_in[1], (const bf16*)d_in[2], (bf16*)d_out);
}
```

```cpp
#include <hip/hip_runtime.h>
#include <hip/hip_bf16.h>

namespace {

constexpr int S = 384, D = 128, NWG = 256;
constexpr unsigned LDS_BYTES = 163840;
using bf16x8 = __attribute__((ext_vector_type(8))) short;
using f32x16 = __attribute__((ext_vector_type(16))) float;
using u32x2 = __attribute__((ext_vector_type(2))) unsigned;
using f32x2 = __attribute__((ext_vector_type(2))) float;
using b16x2 = __attribute__((ext_vector_type(2))) __bf16;

__device__ __forceinline__ unsigned pack_bf16(float lo, float hi) {
  f32x2 v = {lo, hi};
  return __builtin_bit_cast(unsigned, __builtin_convertvector(v, b16x2));
}
__device__ __forceinline__ float other_half(float x, int h) {
  auto r = __builtin_amdgcn_permlane32_swap(__float_as_uint(x), __float_as_uint(x), false, false);
  return __uint_as_float(h ? r[0] : r[1]);
}
__device__ __forceinline__ f32x16 scores(const unsigned short* __restrict__ kt, const bf16x8* qf, int r, int h) {
  f32x16 s = {};
#pragma unroll
  for (int d = 0; d < 8; ++d) {
    const bf16x8 kf = *reinterpret_cast<const bf16x8*>(kt + r * D + 16 * d + 8 * h);
    s = __builtin_amdgcn_mfma_f32_32x32x16_bf16(kf, qf[d], s, 0, 0, 0);
  }
  return s;
}

__device__ __forceinline__ void attn_true_max(const unsigned short* __restrict__ q, const unsigned short* __restrict__ k,
                                           const unsigned short* __restrict__ v, unsigned short* __restrict__ o, int lane) {
  constexpr float C = 0.08838834764831845f * 1.4426950408889634f;
  const int r = lane & 31, h = lane >> 5;
#pragma unroll 1
  for (int qb = 0; qb < 3; ++qb) {
    bf16x8 qf[8];
#pragma unroll
    for (int d = 0; d < 8; ++d) qf[d] = *reinterpret_cast<const bf16x8*>(q + (32 * qb + r) * D + 16 * d + 8 * h);
    float m = -INFINITY;
#pragma unroll 1
    for (int t = 0; t < S / 32; ++t) {
      const f32x16 s = scores(k + t * 32 * D, qf, r, h);
#pragma unroll
      for (int e = 0; e < 16; ++e) m = fmaxf(m, s[e]);
    }
    m = fmaxf(m, other_half(m, h));
    float l = 0.f;
    f32x16 acc[4] = {};
#pragma unroll 1
    for (int t = 0; t < S / 32; ++t) {
      const f32x16 s = scores(k + t * 32 * D, qf, r, h);
      float p[16];
#pragma unroll
      for (int e = 0; e < 16; ++e) { p[e] = __builtin_amdgcn_exp2f((s[e] - m) * C); l += p[e]; }
#pragma unroll
      for (int ks = 0; ks < 2; ++ks) {
        bf16x8 pf;
#pragma unroll
        for (int j = 0; j < 4; ++j) {
          const unsigned w = pack_bf16(p[8 * ks + 2 * j], p[8 * ks + 2 * j + 1]);
          pf[2 * j] = (short)(w & 0xffffu); pf[2 * j + 1] = (short)(w >> 16);
        }
#pragma unroll
        for (int db = 0; db < 4; ++db) {
          bf16x8 vf;
#pragma unroll
          for (int j = 0; j < 8; ++j) vf[j] = (short)v[(t * 32 + 16 * ks + 8 * (j >> 2) + 4 * h + (j & 3)) * D + 32 * db + r];
          acc[db] = __builtin_amdgcn_mfma_f32_32x32x16_bf16(vf, pf, acc[db], 0, 0, 0);
        }
      }
    }
    l += other_half(l, h);
    const float inv = 1.0f / l;
#pragma unroll
    for (int db = 0; db < 4; ++db)
#pragma unroll
      for (int g = 0; g < 4; ++g) {
        u32x2 w = {pack_bf16(acc[db][4 * g] * inv, acc[db][4 * g + 1] * inv), pack_bf16(acc[db][4 * g + 2] * inv, acc[db][4 * g + 3] * inv)};
        *reinterpret_cast<u32x2*>(o + (32 * qb + r) * D + 32 * db + 8 * g + 4 * h) = w;
      }
  }
}

__global__ __launch_bounds__(256, 1) void attn_fwd_kernel(const unsigned short* __restrict__ q, const unsigned short* __restrict__ k,
                                                          const unsigned short* __restrict__ v, unsigned short* __restrict__ o) {
  extern __shared__ __attribute__((aligned(16))) char lds[];
  const unsigned wid = __builtin_amdgcn_readfirstlane(threadIdx.x >> 6);
  const unsigned bid = blockIdx.x;
  const unsigned pid = bid;
  const unsigned ldsb = (unsigned)(size_t)lds;
  const size_t qa = (size_t)q, ka = (size_t)k, va = (size_t)v, oa = (size_t)o;
  unsigned flags;
  asm volatile(
"v_mbcnt_lo_u32_b32 v242, -1, 0\n"
"v_mbcnt_hi_u32_b32 v242, -1, v242\n"
"s_getpc_b64 s[52:53]\n"
"Lpc%=:\n"
"s_and_b32 s53, s53, 0xffff\n"
"s_mov_b32 s54, Lend%=-Lpc%=\n"
"s_mov_b32 s55, 0x20000\n"
"s_lshr_b32 s73, %[bid], 3\n"
"s_and_b32 s73, s73, 15\n"
"s_lshl_b32 s73, s73, 2\n"
"s_add_u32 s73, s73, %[wid]\n"
"s_lshl_b32 s73, s73, 10\n"
"v_lshlrev_b32 v243, 4, v242\n"
"buffer_load_dwordx4 a[252:255], v243, s[52:55], s73 offen\n"
"s_lshr_b32 s72, %[bid], 3\n"
"s_and_b32 s72, s72, 3\n"
"s_cmp_eq_u32 s72, 0\n"
"s_cbranch_scc1 Lsg%=\n"
"Lsh%=:\n"
"s_sleep 46\n"
"s_sub_u32 s72, s72, 1\n"
"s_cmp_lg_u32 s72, 0\n"
"s_cbranch_scc1 Lsh%=\n"
"Lsg%=:\n"
"s_mul_i32 s70, %[pid], 196608\n"
"s_mul_hi_u32 s71, %[pid], 196608\n"
"s_add_u32 s36, %[ql], s70\n"
"s_addc_u32 s37, %[qh], s71\n"
"s_and_b32 s37, s37, 0xffff\n"
"s_mov_b32 s38, 196608\n"
"s_mov_b32 s39, 0x20000\n"
"v_bfe_u32 v243, v242, 2, 3\n"
"v_lshlrev_b32 v243, 8, v243\n"
"v_lshrrev_b32 v244, 5, v242\n"
"v_lshl_add_u32 v243, v244, 6, v243\n"
"v_and_b32 v244, 3, v242\n"
"v_lshl_add_u32 v243, v244, 4, v243\n"
"v_and_b32 v244, 16, v242\n"
"v_lshlrev_b32 v244, 1, v244\n"
"v_xor_b32 v226, v244, v243\n"
"s_mul_i32 s59, %[wid], 24576\n"
"s_add_u32 s58, s59, %[ldsb]\n"
"s_add_u32 s58, s58, 65536\n"
"s_add_u32 s60, s59, 0\n"
"s_add_u32 m0, s58, 0\n"
"s_nop 0\n"
"buffer_load_dwordx4 v226, s[36:39], s60 offen nt lds\n"
"buffer_load_dwordx4 v226, s[36:39], s60 offen offset:2048 nt lds\n"
"s_add_u32 s60, s59, 128\n"
"s_add_u32 m0, s58, 1024\n"
"s_nop 0\n"
"buffer_load_dwordx4 v226, s[36:39], s60 offen nt lds\n"
"buffer_load_dwordx4 v226, s[36:39], s60 offen offset:2048 nt lds\n"
"s_add_u32 s40, %[kl], s70\n"
"s_addc_u32 s41, %[kh], s71\n"
"s_and_b32 s41, s41, 0xffff\n"
"s_mov_b32 s42, 196608\n"
"s_mov_b32 s43, 0x20000\n"
"s_lshr_b32 s72, %[wid], 1\n"
"s_and_b32 s73, %[wid], 1\n"
"s_lshl_b32 s57, s72, 12\n"
"s_lshl_b32 s56, s73, 10\n"
"s_add_u32 s56, s56, s57\n"
"s_add_u32 s56, s56, %[ldsb]\n"
"s_lshl_b32 s73, s73, 7\n"
"s_add_u32 s57, s57, s73\n"
"s_add_u32 m0, s56, 0\n"
"s_add_u32 s72, s57, 0\n"
"buffer_load_dwordx4 v226, s[40:43], s72 offen nt lds\n"
"buffer_load_dwordx4 v226, s[40:43], s72 offen offset:2048 nt lds\n"
"s_add_u32 s60, s59, 4096\n"
"s_add_u32 m0, s58, 4096\n"
"s_nop 0\n"
"buffer_load_dwordx4 v226, s[36:39], s60 offen nt lds\n"
"buffer_load_dwordx4 v226, s[36:39], s60 offen offset:2048 nt lds\n"
"s_add_u32 s60, s59, 4224\n"
"s_add_u32 m0, s58, 5120\n"
"s_nop 0\n"
"buffer_load_dwordx4 v226, s[36:39], s60 offen nt lds\n"
"buffer_load_dwordx4 v226, s[36:39], s60 offen offset:2048 nt lds\n"
"s_add_u32 s44, %[vl], s70\n"
"s_addc_u32 s45, %[vh], s71\n"
"s_and_b32 s45, s45, 0xffff\n"
"s_mov_b32 s46, 196608\n"
"s_mov_b32 s47, 0x20000\n"
"s_add_u32 m0, s56, 32768\n"
"s_add_u32 s72, s57, 0\n"
"buffer_load_dwordx4 v226, s[44:47], s72 offen nt lds\n"
"buffer_load_dwordx4 v226, s[44:47], s72 offen offset:2048 nt lds\n"
"s_add_u32 s48, %[ol], s70\n"
"s_addc_u32 s49, %[oh], s71\n"
"s_and_b32 s49, s49, 0xffff\n"
"s_mov_b32 s50, 196608\n"
"s_mov_b32 s51, 0x20000\n"
"s_mov_b32 s62, 0x3e0293ee\n"
"s_mov_b32 s63, 0x26800000\n"
"s_mov_b32 s64, 0x58800000\n"
"s_mov_b64 s[66:67], 0\n"
"s_mov_b64 s[68:69], 0\n"
"v_mov_b32 v243, 0x3f803f80\n"
"v_and_b32 v244, 3, v242\n"
"v_cmp_eq_u32 vcc, 0, v244\n"
"s_nop 1\n"
"v_cndmask_b32 v243, 0, v243, vcc\n"
"v_accvgpr_write_b32 a184, v243\n"
"v_accvgpr_write_b32 a185, v243\n"
"v_accvgpr_write_b32 a186, v243\n"
"v_accvgpr_write_b32 a187, v243\n"
"v_bfe_u32 v243, v242, 3, 1\n"
"v_lshlrev_b32 v243, 11, v243\n"
"v_and_b32 v244, 7, v242\n"
"v_lshl_add_u32 v243, v244, 6, v243\n"
"v_bfe_u32 v244, v242, 2, 1\n"
"v_lshlrev_b32 v244, 1, v244\n"
"v_lshrrev_b32 v245, 4, v242\n"
"v_xor_b32 v244, v244, v245\n"
"v_lshl_add_u32 v243, v244, 4, v243\n"
"v_add_u32 v224, %[ldsb], v243\n"
"v_add_u32 v225, s58, v243\n"
"v_lshrrev_b32 v243, 5, v242\n"
"v_lshlrev_b32 v243, 11, v243\n"
"v_bfe_u32 v244, v242, 4, 1\n"
"v_lshl_add_u32 v243, v244, 8, v243\n"
"v_lshl_add_u32 v243, v244, 5, v243\n"
"v_bfe_u32 v244, v242, 2, 2\n"
"v_lshl_add_u32 v243, v244, 6, v243\n"
"v_and_b32 v244, 3, v242\n"
"v_lshl_add_u32 v243, v244, 3, v243\n"
"v_add_u32 v243, 32768, v243\n"
"v_add_u32 v228, %[ldsb], v243\n"
"v_xor_b32 v243, 32, v243\n"
"v_add_u32 v229, %[ldsb], v243\n"
"v_and_b32 v243, 15, v242\n"
"v_lshlrev_b32 v243, 8, v243\n"
"v_and_b32 v244, 7, v242\n"
"v_lshrrev_b32 v245, 5, v242\n"
"v_xor_b32 v244, v244, v245\n"
"v_lshl_add_u32 v243, v244, 4, v243\n"
"v_bfe_u32 v244, v242, 4, 1\n"
"v_lshl_add_u32 v243, v244, 3, v243\n"
"v_add_u32 v232, s58, v243\n"
"v_lshrrev_b32 v243, 4, v242\n"
"v_and_b32 v244, 15, v242\n"
"v_xor_b32 v244, v244, v243\n"
"v_lshlrev_b32 v243, 8, v243\n"
"v_lshl_add_u32 v243, v244, 4, v243\n"
"v_add_u32 v230, s58, v243\n"
"v_xor_b32 v243, 64, v243\n"
"v_add_u32 v231, s58, v243\n"
"v_lshlrev_b32 v227, 4, v242\n"
"s_waitcnt vmcnt(8)\n"
"ds_read_b128 a[0:3], v225 offset:0\n"
"ds_read_b128 a[4:7], v225 offset:512\n"
"ds_read_b128 a[8:11], v225 offset:1024\n"
"ds_read_b128 a[12:15], v225 offset:1536\n"
"s_waitcnt vmcnt(6)\n"
"s_barrier\n"
"ds_read_b128 a[96:99], v224 offset:0\n"
"ds_read_b128 a[100:103], v224 offset:512\n"
"ds_read_b128 a[104:107], v224 offset:1024\n"
"ds_read_b128 a[108:111], v224 offset:1536\n"
"ds_read_b128 a[112:115], v224 offset:4096\n"
"ds_read_b128 a[116:119], v224 offset:4608\n"
"ds_read_b128 a[120:123], v224 offset:5120\n"
"ds_read_b128 a[124:127], v224 offset:5632\n"
"s_waitcnt lgkmcnt(0)\n"
"v_mfma_f32_16x16x32_bf16 v[192:195], a[96:99], a[0:3], 0\n"
"s_add_u32 s60, s59, 8192\n"
"s_add_u32 m0, s58, 8192\n"
"v_mfma_f32_16x16x32_bf16 v[192:195], a[100:103], a[4:7], v[192:195]\n"
"buffer_load_dwordx4 v226, s[36:39], s60 offen nt lds\n"
"v_mfma_f32_16x16x32_bf16 v[192:195], a[104:107], a[8:11], v[192:195]\n"
"buffer_load_dwordx4 v226, s[36:39], s60 offen offset:2048 nt lds\n"
"v_mfma_f32_16x16x32_bf16 v[192:195], a[108:111], a[12:15], v[192:195]\n"
"s_add_u32 s60, s59, 8320\n"
"s_add_u32 m0, s58, 9216\n"
"v_mfma_f32_16x16x32_bf16 v[196:199], a[112:115], a[0:3], 0\n"
"buffer_load_dwordx4 v226, s[36:39], s60 offen nt lds\n"
"v_mfma_f32_16x16x32_bf16 v[196:199], a[116:119], a[4:7], v[196:199]\n"
"buffer_load_dwordx4 v226, s[36:39], s60 offen offset:2048 nt lds\n"
"v_mfma_f32_16x16x32_bf16 v[196:199], a[120:123], a[8:11], v[196:199]\n"
"s_add_u32 s60, s59, 12288\n"
"s_add_u32 m0, s58, 12288\n"
"v_mfma_f32_16x16x32_bf16 v[196:199], a[124:127], a[12:15], v[196:199]\n"
"buffer_load_dwordx4 v226, s[36:39], s60 offen nt lds\n"
"v_mul_f32 v192, s62, v192\n"
"buffer_load_dwordx4 v226, s[36:39], s60 offen offset:2048 nt lds\n"
"v_exp_f32 v192, v192\n"
"v_mul_f32 v193, s62, v193\n"
"v_exp_f32 v193, v193\n"
"v_mul_f32 v194, s62, v194\n"
"v_cvt_pk_bf16_f32 v192, v192, v193\n"
"v_exp_f32 v194, v194\n"
"s_add_u32 s60, s59, 12416\n"
"s_add_u32 m0, s58, 13312\n"
"s_nop 0\n"
"buffer_load_dwordx4 v226, s[36:39], s60 offen nt lds\n"
"v_mul_f32 v195, s62, v195\n"
"v_exp_f32 v195, v195\n"
"v_mul_f32 v196, s62, v196\n"
"buffer_load_dwordx4 v226, s[36:39], s60 offen offset:2048 nt lds\n"
"v_cvt_pk_bf16_f32 v193, v194, v195\n"
"v_exp_f32 v196, v196\n"
"v_mul_f32 v197, s62, v197\n"
"v_mul_f32 v198, s62, v198\n"
"s_waitcnt vmcnt(10)\n"
"ds_read_b128 a[16:19], v225 offset:4096\n"
"v_exp_f32 v197, v197\n"
"s_add_u32 s60, s59, 16384\n"
"s_add_u32 m0, s58, 16384\n"
"s_nop 0\n"
"buffer_load_dwordx4 v226, s[36:39], s60 offen nt lds\n"
"ds_read_b128 a[20:23], v225 offset:4608\n"
"v_cvt_pk_bf16_f32 v194, v196, v197\n"
"v_exp_f32 v198, v198\n"
"buffer_load_dwordx4 v226, s[36:39], s60 offen offset:2048 nt lds\n"
"ds_read_b128 a[24:27], v225 offset:5120\n"
"v_mul_f32 v199, s62, v199\n"
"v_exp_f32 v199, v199\n"
"ds_read_b128 a[28:31], v225 offset:5632\n"
"v_cvt_pk_bf16_f32 v195, v198, v199\n"
"s_add_u32 s60, s59, 16512\n"
"s_add_u32 m0, s58, 17408\n"
"s_waitcnt lgkmcnt(3)\n"
"v_mfma_f32_16x16x32_bf16 v[200:203], a[96:99], a[16:19], 0\n"
"buffer_load_dwordx4 v226, s[36:39], s60 offen nt lds\n"
"s_waitcnt lgkmcnt(2)\n"
"v_mfma_f32_16x16x32_bf16 v[200:203], a[100:103], a[20:23], v[200:203]\n"
"buffer_load_dwordx4 v226, s[36:39], s60 offen offset:2048 nt lds\n"
"s_waitcnt lgkmcnt(1)\n"
"v_mfma_f32_16x16x32_bf16 v[200:203], a[104:107], a[24:27], v[200:203]\n"
"s_add_u32 m0, s56, 8192\n"
"s_add_u32 s65, s57, 8192\n"
"s_waitcnt lgkmcnt(0)\n"
"v_mfma_f32_16x16x32_bf16 v[200:203], a[108:111], a[28:31], v[200:203]\n"
"buffer_load_dwordx4 v226, s[40:43], s65 offen nt lds\n"
"v_mfma_f32_16x16x32_bf16 v[204:207], a[112:115], a[16:19], 0\n"
"buffer_load_dwordx4 v226, s[40:43], s65 offen offset:2048 nt lds\n"
"v_mfma_f32_16x16x32_bf16 v[204:207], a[116:119], a[20:23], v[204:207]\n"
"s_add_u32 s60, s59, 20480\n"
"s_add_u32 m0, s58, 20480\n"
"v_mfma_f32_16x16x32_bf16 v[204:207], a[120:123], a[24:27], v[204:207]\n"
"buffer_load_dwordx4 v226, s[36:39], s60 offen nt lds\n"
"v_mul_f32 v200, s62, v200\n"
"v_mfma_f32_16x16x32_bf16 v[204:207], a[124:127], a[28:31], v[204:207]\n"
"buffer_load_dwordx4 v226, s[36:39], s60 offen offset:2048 nt lds\n"
"s_waitcnt vmcnt(16)\n"
"s_barrier\n"
"ds_read_b64_tr_b16 a[128:129], v228 offset:0\n"
"v_exp_f32 v200, v200\n"
"v_mul_f32 v201, s62, v201\n"
"s_add_u32 s60, s59, 20608\n"
"s_add_u32 m0, s58, 21504\n"
"ds_read_b64_tr_b16 a[130:131], v228 offset:4096\n"
"buffer_load_dwordx4 v226, s[36:39], s60 offen nt lds\n"
"v_exp_f32 v201, v201\n"
"v_mul_f32 v202, s62, v202\n"
"v_cvt_pk_bf16_f32 v200, v200, v201\n"
"ds_read_b64_tr_b16 a[132:133], v229 offset:0\n"
"buffer_load_dwordx4 v226, s[36:39], s60 offen offset:2048 nt lds\n"
"v_exp_f32 v202, v202\n"
"v_mul_f32 v203, s62, v203\n"
"v_mul_f32 v204, s62, v204\n"
"ds_read_b64_tr_b16 a[134:135], v229 offset:4096\n"
"v_exp_f32 v203, v203\n"
"v_mul_f32 v205, s62, v205\n"
"s_add_u32 m0, s56, 40960\n"
"s_add_u32 s65, s57, 8192\n"
"ds_read_b64_tr_b16 a[136:137], v228 offset:512\n"
"buffer_load_dwordx4 v226, s[44:47], s65 offen nt lds\n"
"s_waitcnt vmcnt(15)\n"
"ds_read_b128 a[32:35], v225 offset:8192\n"
"v_cvt_pk_bf16_f32 v201, v202, v203\n"
"v_mul_f32 v206, s62, v206\n"
"ds_read_b64_tr_b16 a[138:139], v228 offset:4608\n"
"buffer_load_dwordx4 v226, s[44:47], s65 offen offset:2048 nt lds\n"
"ds_read_b128 a[36:39], v225 offset:8704\n"
"v_exp_f32 v204, v204\n"
"v_mul_f32 v207, s62, v207\n"
"ds_read_b64_tr_b16 a[140:141], v229 offset:512\n"
"ds_read_b128 a[40:43], v225 offset:9216\n"
"v_exp_f32 v205, v205\n"
"v_exp_f32 v206, v206\n"
"ds_read_b64_tr_b16 a[142:143], v229 offset:4608\n"
"ds_read_b128 a[44:47], v225 offset:9728\n"
"v_cvt_pk_bf16_f32 v202, v204, v205\n"
"v_exp_f32 v207, v207\n"
"s_waitcnt lgkmcnt(6)\n"
"v_mfma_f32_16x16x32_bf16 v[208:211], a[96:99], a[32:35], 0\n"
"ds_read_b64_tr_b16 a[144:145], v228 offset:1024\n"
"s_waitcnt lgkmcnt(5)\n"
"v_mfma_f32_16x16x32_bf16 v[208:211], a[100:103], a[36:39], v[208:211]\n"
"ds_read_b64_tr_b16 a[146:147], v228 offset:5120\n"
"s_waitcnt lgkmcnt(4)\n"
"v_mfma_f32_16x16x32_bf16 v[208:211], a[104:107], a[40:43], v[208:211]\n"
"ds_read_b64_tr_b16 a[148:149], v229 offset:1024\n"
"s_waitcnt lgkmcnt(3)\n"
"v_mfma_f32_16x16x32_bf16 v[208:211], a[108:111], a[44:47], v[208:211]\n"
"ds_read_b64_tr_b16 a[150:151], v229 offset:5120\n"
"v_cvt_pk_bf16_f32 v203, v206, v207\n"
"v_mfma_f32_16x16x32_bf16 v[212:215], a[112:115], a[32:35], 0\n"
"ds_read_b64_tr_b16 a[152:153], v228 offset:1536\n"
"v_mfma_f32_16x16x32_bf16 v[212:215], a[116:119], a[36:39], v[212:215]\n"
"ds_read_b64_tr_b16 a[154:155], v228 offset:5632\n"
"v_mfma_f32_16x16x32_bf16 v[212:215], a[120:123], a[40:43], v[212:215]\n"
"ds_read_b64_tr_b16 a[156:157], v229 offset:1536\n"
"v_mul_f32 v208, s62, v208\n"
"v_mfma_f32_16x16x32_bf16 v[212:215], a[124:127], a[44:47], v[212:215]\n"
"ds_read_b64_tr_b16 a[158:159], v229 offset:5632\n"
"v_mul_f32 v209, s62, v209\n"
"v_mfma_f32_16x16x32_bf16 v[0:3], a[128:131], v[192:195], 0\n"
"v_exp_f32 v208, v208\n"
"v_mfma_f32_16x16x32_bf16 v[4:7], a[132:135], v[192:195], 0\n"
"v_exp_f32 v209, v209\n"
"v_mfma_f32_16x16x32_bf16 v[8:11], a[136:139], v[192:195], 0\n"
"v_mul_f32 v210, s62, v210\n"
"v_cvt_pk_bf16_f32 v208, v208, v209\n"
"v_mfma_f32_16x16x32_bf16 v[12:15], a[140:143], v[192:195], 0\n"
"v_mul_f32 v211, s62, v211\n"
"s_waitcnt lgkmcnt(2)\n"
"v_mfma_f32_16x16x32_bf16 v[16:19], a[144:147], v[192:195], 0\n"
"v_exp_f32 v210, v210\n"
"v_mfma_f32_16x16x32_bf16 v[20:23], a[148:151], v[192:195], 0\n"
"s_waitcnt vmcnt(12)\n"
"ds_read_b128 a[48:51], v225 offset:12288\n"
"v_mfma_f32_16x16x32_bf16 v[24:27], a[152:155], v[192:195], 0\n"
"ds_read_b128 a[52:55], v225 offset:12800\n"
"s_waitcnt lgkmcnt(2)\n"
"v_mfma_f32_16x16x32_bf16 v[28:31], a[156:159], v[192:195], 0\n"
"ds_read_b128 a[56:59], v225 offset:13312\n"
"v_mul_f32 v212, s62, v212\n"
"v_mfma_f32_16x16x32_bf16 a[160:163], a[184:187], v[192:195], 0\n"
"ds_read_b128 a[60:63], v225 offset:13824\n"
"s_waitcnt lgkmcnt(3)\n"
"v_mfma_f32_16x16x32_bf16 v[192:195], a[96:99], a[48:51], 0\n"
"v_mul_f32 v213, s62, v213\n"
"s_waitcnt lgkmcnt(2)\n"
"v_mfma_f32_16x16x32_bf16 v[192:195], a[100:103], a[52:55], v[192:195]\n"
"v_mul_f32 v214, s62, v214\n"
"s_waitcnt lgkmcnt(1)\n"
"v_mfma_f32_16x16x32_bf16 v[192:195], a[104:107], a[56:59], v[192:195]\n"
"v_mul_f32 v215, s62, v215\n"
"s_waitcnt lgkmcnt(0)\n"
"v_mfma_f32_16x16x32_bf16 v[192:195], a[108:111], a[60:63], v[192:195]\n"
"v_exp_f32 v211, v211\n"
"v_mfma_f32_16x16x32_bf16 v[196:199], a[112:115], a[48:51], 0\n"
"v_cvt_pk_bf16_f32 v209, v210, v211\n"
"v_mfma_f32_16x16x32_bf16 v[196:199], a[116:119], a[52:55], v[196:199]\n"
"v_exp_f32 v212, v212\n"
"v_mfma_f32_16x16x32_bf16 v[196:199], a[120:123], a[56:59], v[196:199]\n"
"v_exp_f32 v213, v213\n"
"v_mfma_f32_16x16x32_bf16 v[196:199], a[124:127], a[60:63], v[196:199]\n"
"v_mul_f32 v192, s62, v192\n"
"v_mul_f32 v193, s62, v193\n"
"v_mfma_f32_16x16x32_bf16 v[32:35], a[128:131], v[200:203], 0\n"
"v_exp_f32 v192, v192\n"
"v_mfma_f32_16x16x32_bf16 v[36:39], a[132:135], v[200:203], 0\n"
"v_cvt_pk_bf16_f32 v210, v212, v213\n"
"v_mul_f32 v194, s62, v194\n"
"v_mfma_f32_16x16x32_bf16 v[40:43], a[136:139], v[200:203], 0\n"
"v_exp_f32 v193, v193\n"
"v_mfma_f32_16x16x32_bf16 v[44:47], a[140:143], v[200:203], 0\n"
"v_exp_f32 v214, v214\n"
"v_mfma_f32_16x16x32_bf16 v[48:51], a[144:147], v[200:203], 0\n"
"s_waitcnt vmcnt(0)\n"
"s_barrier\n"
"v_mfma_f32_16x16x32_bf16 v[52:55], a[148:151], v[200:203], 0\n"
"ds_read_b128 a[64:67], v225 offset:16384\n"
"v_cvt_pk_bf16_f32 v192, v192, v193\n"
"v_mfma_f32_16x16x32_bf16 v[56:59], a[152:155], v[200:203], 0\n"
"ds_read_b128 a[68:71], v225 offset:16896\n"
"s_add_u32 m0, s56, 0\n"
"s_add_u32 s72, s57, 32768\n"
"v_mfma_f32_16x16x32_bf16 v[60:63], a[156:159], v[200:203], 0\n"
"buffer_load_dwordx4 v226, s[40:43], s72 offen nt lds\n"
"ds_read_b128 a[72:75], v225 offset:17408\n"
"v_mfma_f32_16x16x32_bf16 a[164:167], a[184:187], v[200:203], 0\n"
"buffer_load_dwordx4 v226, s[40:43], s72 offen offset:2048 nt lds\n"
"ds_read_b128 a[76:79], v225 offset:17920\n"
"s_waitcnt lgkmcnt(3)\n"
"v_mfma_f32_16x16x32_bf16 v[200:203], a[96:99], a[64:67], 0\n"
"s_add_u32 m0, s56, 32768\n"
"s_waitcnt lgkmcnt(2)\n"
"v_mfma_f32_16x16x32_bf16 v[200:203], a[100:103], a[68:71], v[200:203]\n"
"buffer_load_dwordx4 v226, s[44:47], s72 offen nt lds\n"
"s_waitcnt lgkmcnt(1)\n"
"v_mfma_f32_16x16x32_bf16 v[200:203], a[104:107], a[72:75], v[200:203]\n"
"buffer_load_dwordx4 v226, s[44:47], s72 offen offset:2048 nt lds\n"
"v_exp_f32 v194, v194\n"
"s_waitcnt lgkmcnt(0)\n"
"v_mfma_f32_16x16x32_bf16 v[200:203], a[108:111], a[76:79], v[200:203]\n"
"v_exp_f32 v215, v215\n"
"v_mul_f32 v195, s62, v195\n"
"v_mfma_f32_16x16x32_bf16 v[204:207], a[112:115], a[64:67], 0\n"
"v_cvt_pk_bf16_f32 v211, v214, v215\n"
"v_mul_f32 v196, s62, v196\n"
"v_mfma_f32_16x16x32_bf16 v[204:207], a[116:119], a[68:71], v[204:207]\n"
"v_exp_f32 v195, v195\n"
"v_mfma_f32_16x16x32_bf16 v[204:207], a[120:123], a[72:75], v[204:207]\n"
"v_cvt_pk_bf16_f32 v193, v194, v195\n"
"v_mul_f32 v200, s62, v200\n"
"v_mfma_f32_16x16x32_bf16 v[204:207], a[124:127], a[76:79], v[204:207]\n"
"v_exp_f32 v196, v196\n"
"v_mfma_f32_16x16x32_bf16 v[64:67], a[128:131], v[208:211], 0\n"
"s_add_u32 m0, s56, 16384\n"
"s_add_u32 s65, s57, 16384\n"
"v_mfma_f32_16x16x32_bf16 v[68:71], a[132:135], v[208:211], 0\n"
"buffer_load_dwordx4 v226, s[40:43], s65 offen nt lds\n"
"v_mul_f32 v197, s62, v197\n"
"v_mfma_f32_16x16x32_bf16 v[72:75], a[136:139], v[208:211], 0\n"
"buffer_load_dwordx4 v226, s[40:43], s65 offen offset:2048 nt lds\n"
"v_mul_f32 v201, s62, v201\n"
"v_mfma_f32_16x16x32_bf16 v[76:79], a[140:143], v[208:211], 0\n"
"s_add_u32 m0, s56, 49152\n"
"s_add_u32 s65, s57, 16384\n"
"v_mfma_f32_16x16x32_bf16 v[80:83], a[144:147], v[208:211], 0\n"
"buffer_load_dwordx4 v226, s[44:47], s65 offen nt lds\n"
"v_exp_f32 v200, v200\n"
"v_mfma_f32_16x16x32_bf16 v[84:87], a[148:151], v[208:211], 0\n"
"buffer_load_dwordx4 v226, s[44:47], s65 offen offset:2048 nt lds\n"
"ds_read_b128 a[80:83], v225 offset:20480\n"
"v_exp_f32 v197, v197\n"
"v_mfma_f32_16x16x32_bf16 v[88:91], a[152:155], v[208:211], 0\n"
"ds_read_b128 a[84:87], v225 offset:20992\n"
"v_mul_f32 v198, s62, v198\n"
"v_mfma_f32_16x16x32_bf16 v[92:95], a[156:159], v[208:211], 0\n"
"ds_read_b128 a[88:91], v225 offset:21504\n"
"v_cvt_pk_bf16_f32 v194, v196, v197\n"
"v_exp_f32 v201, v201\n"
"v_mfma_f32_16x16x32_bf16 a[168:171], a[184:187], v[208:211], 0\n"
"ds_read_b128 a[92:95], v225 offset:22016\n"
"v_exp_f32 v198, v198\n"
"s_waitcnt lgkmcnt(3)\n"
"v_mfma_f32_16x16x32_bf16 v[208:211], a[96:99], a[80:83], 0\n"
"ds_read_b128 a[96:99], v224 offset:8192\n"
"v_mul_f32 v202, s62, v202\n"
"v_cvt_pk_bf16_f32 v200, v200, v201\n"
"s_waitcnt lgkmcnt(3)\n"
"v_mfma_f32_16x16x32_bf16 v[208:211], a[100:103], a[84:87], v[208:211]\n"
"v_mul_f32 v199, s62, v199\n"
"s_waitcnt lgkmcnt(2)\n"
"v_mfma_f32_16x16x32_bf16 v[208:211], a[104:107], a[88:91], v[208:211]\n"
"ds_read_b128 a[100:103], v224 offset:8704\n"
"v_exp_f32 v202, v202\n"
"s_waitcnt lgkmcnt(2)\n"
"v_mfma_f32_16x16x32_bf16 v[208:211], a[108:111], a[92:95], v[208:211]\n"
"v_exp_f32 v199, v199\n"
"v_mul_f32 v203, s62, v203\n"
"v_mfma_f32_16x16x32_bf16 v[212:215], a[112:115], a[80:83], 0\n"
"ds_read_b128 a[104:107], v224 offset:9216\n"
"v_cvt_pk_bf16_f32 v195, v198, v199\n"
"v_mfma_f32_16x16x32_bf16 v[212:215], a[116:119], a[84:87], v[212:215]\n"
"v_exp_f32 v203, v203\n"
"v_mfma_f32_16x16x32_bf16 v[212:215], a[120:123], a[88:91], v[212:215]\n"
"ds_read_b128 a[108:111], v224 offset:9728\n"
"v_mul_f32 v204, s62, v204\n"
"v_mfma_f32_16x16x32_bf16 v[212:215], a[124:127], a[92:95], v[212:215]\n"
"v_cvt_pk_bf16_f32 v201, v202, v203\n"
"v_mul_f32 v208, s62, v208\n"
"v_mfma_f32_16x16x32_bf16 v[96:99], a[128:131], v[192:195], 0\n"
"ds_read_b128 a[112:115], v224 offset:12288\n"
"v_mul_f32 v205, s62, v205\n"
"v_mfma_f32_16x16x32_bf16 v[100:103], a[132:135], v[192:195], 0\n"
"v_exp_f32 v204, v204\n"
"v_mfma_f32_16x16x32_bf16 v[104:107], a[136:139], v[192:195], 0\n"
"ds_read_b128 a[116:119], v224 offset:12800\n"
"v_mul_f32 v209, s62, v209\n"
"v_mfma_f32_16x16x32_bf16 v[108:111], a[140:143], v[192:195], 0\n"
"v_exp_f32 v208, v208\n"
"v_mfma_f32_16x16x32_bf16 v[112:115], a[144:147], v[192:195], 0\n"
"ds_read_b128 a[120:123], v224 offset:13312\n"
"v_mul_f32 v206, s62, v206\n"
"v_mfma_f32_16x16x32_bf16 v[116:119], a[148:151], v[192:195], 0\n"
"v_exp_f32 v205, v205\n"
"v_mfma_f32_16x16x32_bf16 v[120:123], a[152:155], v[192:195], 0\n"
"ds_read_b128 a[124:127], v224 offset:13824\n"
"v_cvt_pk_bf16_f32 v202, v204, v205\n"
"v_mfma_f32_16x16x32_bf16 v[124:127], a[156:159], v[192:195], 0\n"
"v_exp_f32 v209, v209\n"
"v_mfma_f32_16x16x32_bf16 a[172:175], a[184:187], v[192:195], 0\n"
"v_exp_f32 v206, v206\n"
"s_waitcnt lgkmcnt(2)\n"
"v_mfma_f32_16x16x32_bf16 v[192:195], a[96:99], a[0:3], 0\n"
"v_mul_f32 v210, s62, v210\n"
"v_cvt_pk_bf16_f32 v208, v208, v209\n"
"v_mfma_f32_16x16x32_bf16 v[192:195], a[100:103], a[4:7], v[192:195]\n"
"v_mul_f32 v207, s62, v207\n"
"v_mul_f32 v211, s62, v211\n"
"v_mfma_f32_16x16x32_bf16 v[192:195], a[104:107], a[8:11], v[192:195]\n"
"v_exp_f32 v210, v210\n"
"v_mfma_f32_16x16x32_bf16 v[192:195], a[108:111], a[12:15], v[192:195]\n"
"v_exp_f32 v207, v207\n"
"v_mfma_f32_16x16x32_bf16 v[196:199], a[112:115], a[0:3], 0\n"
"v_cvt_pk_bf16_f32 v203, v206, v207\n"
"v_mul_f32 v212, s62, v212\n"
"v_mfma_f32_16x16x32_bf16 v[196:199], a[116:119], a[4:7], v[196:199]\n"
"v_exp_f32 v211, v211\n"
"s_waitcnt lgkmcnt(0)\n"
"v_mfma_f32_16x16x32_bf16 v[196:199], a[120:123], a[8:11], v[196:199]\n"
"v_cvt_pk_bf16_f32 v209, v210, v211\n"
"v_mul_f32 v192, s62, v192\n"
"v_mfma_f32_16x16x32_bf16 v[196:199], a[124:127], a[12:15], v[196:199]\n"
"v_exp_f32 v212, v212\n"
"v_mfma_f32_16x16x32_bf16 v[128:131], a[128:131], v[200:203], 0\n"
"v_mul_f32 v213, s62, v213\n"
"v_mul_f32 v193, s62, v193\n"
"v_mfma_f32_16x16x32_bf16 v[132:135], a[132:135], v[200:203], 0\n"
"s_add_u32 m0, s56, 24576\n"
"s_add_u32 s65, s57, 24576\n"
"v_mfma_f32_16x16x32_bf16 v[136:139], a[136:139], v[200:203], 0\n"
"buffer_load_dwordx4 v226, s[40:43], s65 offen nt lds\n"
"v_mul_f32 v214, s62, v214\n"
"v_mfma_f32_16x16x32_bf16 v[140:143], a[140:143], v[200:203], 0\n"
"buffer_load_dwordx4 v226, s[40:43], s65 offen offset:2048 nt lds\n"
"v_mul_f32 v194, s62, v194\n"
"v_mfma_f32_16x16x32_bf16 v[144:147], a[144:147], v[200:203], 0\n"
"v_exp_f32 v192, v192\n"
"s_add_u32 m0, s56, 57344\n"
"s_add_u32 s65, s57, 24576\n"
"v_mfma_f32_16x16x32_bf16 v[148:151], a[148:151], v[200:203], 0\n"
"buffer_load_dwordx4 v226, s[44:47], s65 offen nt lds\n"
"v_exp_f32 v213, v213\n"
"v_mfma_f32_16x16x32_bf16 v[152:155], a[152:155], v[200:203], 0\n"
"buffer_load_dwordx4 v226, s[44:47], s65 offen offset:2048 nt lds\n"
"v_cvt_pk_bf16_f32 v210, v212, v213\n"
"v_mfma_f32_16x16x32_bf16 v[156:159], a[156:159], v[200:203], 0\n"
"v_exp_f32 v193, v193\n"
"v_mfma_f32_16x16x32_bf16 a[176:179], a[184:187], v[200:203], 0\n"
"v_exp_f32 v214, v214\n"
"v_mfma_f32_16x16x32_bf16 v[200:203], a[96:99], a[16:19], 0\n"
"v_cvt_pk_bf16_f32 v192, v192, v193\n"
"v_mul_f32 v215, s62, v215\n"
"v_mfma_f32_16x16x32_bf16 v[200:203], a[100:103], a[20:23], v[200:203]\n"
"v_exp_f32 v194, v194\n"
"v_mfma_f32_16x16x32_bf16 v[200:203], a[104:107], a[24:27], v[200:203]\n"
"v_exp_f32 v215, v215\n"
"v_mfma_f32_16x16x32_bf16 v[200:203], a[108:111], a[28:31], v[200:203]\n"
"v_mul_f32 v195, s62, v195\n"
"v_cvt_pk_bf16_f32 v211, v214, v215\n"
"v_mfma_f32_16x16x32_bf16 v[204:207], a[112:115], a[16:19], 0\n"
"v_exp_f32 v195, v195\n"
"v_mfma_f32_16x16x32_bf16 v[204:207], a[116:119], a[20:23], v[204:207]\n"
"v_mul_f32 v196, s62, v196\n"
"v_cvt_pk_bf16_f32 v193, v194, v195\n"
"v_mfma_f32_16x16x32_bf16 v[204:207], a[120:123], a[24:27], v[204:207]\n"
"v_exp_f32 v196, v196\n"
"v_mfma_f32_16x16x32_bf16 v[204:207], a[124:127], a[28:31], v[204:207]\n"
"v_mul_f32 v200, s62, v200\n"
"v_mul_f32 v197, s62, v197\n"
"v_mfma_f32_16x16x32_bf16 v[160:163], a[128:131], v[208:211], 0\n"
"ds_read_b64_tr_b16 a[128:129], v228 offset:8192\n"
"v_mul_f32 v201, s62, v201\n"
"v_mfma_f32_16x16x32_bf16 v[164:167], a[132:135], v[208:211], 0\n"
"ds_read_b64_tr_b16 a[130:131], v228 offset:12288\n"
"v_mul_f32 v198, s62, v198\n"
"v_mfma_f32_16x16x32_bf16 v[168:171], a[136:139], v[208:211], 0\n"
"ds_read_b64_tr_b16 a[132:133], v229 offset:8192\n"
"v_mul_f32 v202, s62, v202\n"
"v_mfma_f32_16x16x32_bf16 v[172:175], a[140:143], v[208:211], 0\n"
"ds_read_b64_tr_b16 a[134:135], v229 offset:12288\n"
"v_mul_f32 v199, s62, v199\n"
"v_mfma_f32_16x16x32_bf16 v[176:179], a[144:147], v[208:211], 0\n"
"ds_read_b64_tr_b16 a[136:137], v228 offset:8704\n"
"v_exp_f32 v200, v200\n"
"v_mfma_f32_16x16x32_bf16 v[180:183], a[148:151], v[208:211], 0\n"
"ds_read_b64_tr_b16 a[138:139], v228 offset:12800\n"
"v_exp_f32 v197, v197\n"
"v_mfma_f32_16x16x32_bf16 v[184:187], a[152:155], v[208:211], 0\n"
"ds_read_b64_tr_b16 a[140:141], v229 offset:8704\n"
"v_cvt_pk_bf16_f32 v194, v196, v197\n"
"v_mfma_f32_16x16x32_bf16 v[188:191], a[156:159], v[208:211], 0\n"
"ds_read_b64_tr_b16 a[142:143], v229 offset:12800\n"
"v_exp_f32 v201, v201\n"
"v_mfma_f32_16x16x32_bf16 a[180:183], a[184:187], v[208:211], 0\n"
"ds_read_b64_tr_b16 a[144:145], v228 offset:9216\n"
"v_exp_f32 v198, v198\n"
"v_mfma_f32_16x16x32_bf16 v[208:211], a[96:99], a[32:35], 0\n"
"ds_read_b64_tr_b16 a[146:147], v228 offset:13312\n"
"v_cvt_pk_bf16_f32 v200, v200, v201\n"
"v_mfma_f32_16x16x32_bf16 v[208:211], a[100:103], a[36:39], v[208:211]\n"
"ds_read_b64_tr_b16 a[148:149], v229 offset:9216\n"
"v_mul_f32 v203, s62, v203\n"
"v_mfma_f32_16x16x32_bf16 v[208:211], a[104:107], a[40:43], v[208:211]\n"
"ds_read_b64_tr_b16 a[150:151], v229 offset:13312\n"
"v_exp_f32 v202, v202\n"
"v_mfma_f32_16x16x32_bf16 v[208:211], a[108:111], a[44:47], v[208:211]\n"
"ds_read_b64_tr_b16 a[152:153], v228 offset:9728\n"
"v_exp_f32 v199, v199\n"
"v_mfma_f32_16x16x32_bf16 v[212:215], a[112:115], a[32:35], 0\n"
"ds_read_b64_tr_b16 a[154:155], v228 offset:13824\n"
"v_cvt_pk_bf16_f32 v195, v198, v199\n"
"v_mfma_f32_16x16x32_bf16 v[212:215], a[116:119], a[36:39], v[212:215]\n"
"ds_read_b64_tr_b16 a[156:157], v229 offset:9728\n"
"v_exp_f32 v203, v203\n"
"v_mfma_f32_16x16x32_bf16 v[212:215], a[120:123], a[40:43], v[212:215]\n"
"ds_read_b64_tr_b16 a[158:159], v229 offset:13824\n"
"v_mul_f32 v204, s62, v204\n"
"v_mfma_f32_16x16x32_bf16 v[212:215], a[124:127], a[44:47], v[212:215]\n"
"v_cvt_pk_bf16_f32 v201, v202, v203\n"
"s_waitcnt lgkmcnt(5)\n"
"v_mfma_f32_16x16x32_bf16 v[0:3], a[128:131], v[192:195], v[0:3]\n"
"v_exp_f32 v204, v204\n"
"v_mfma_f32_16x16x32_bf16 v[4:7], a[132:135], v[192:195], v[4:7]\n"
"v_mul_f32 v208, s62, v208\n"
"v_mul_f32 v205, s62, v205\n"
"v_mfma_f32_16x16x32_bf16 v[8:11], a[136:139], v[192:195], v[8:11]\n"
"v_exp_f32 v208, v208\n"
"v_mfma_f32_16x16x32_bf16 v[12:15], a[140:143], v[192:195], v[12:15]\n"
"v_exp_f32 v205, v205\n"
"v_mfma_f32_16x16x32_bf16 v[16:19], a[144:147], v[192:195], v[16:19]\n"
"v_mul_f32 v209, s62, v209\n"
"s_waitcnt lgkmcnt(0)\n"
"v_mfma_f32_16x16x32_bf16 v[20:23], a[148:151], v[192:195], v[20:23]\n"
"v_mul_f32 v206, s62, v206\n"
"v_cvt_pk_bf16_f32 v202, v204, v205\n"
"v_mfma_f32_16x16x32_bf16 v[24:27], a[152:155], v[192:195], v[24:27]\n"
"v_exp_f32 v209, v209\n"
"v_mfma_f32_16x16x32_bf16 v[28:31], a[156:159], v[192:195], v[28:31]\n"
"v_exp_f32 v206, v206\n"
"v_mfma_f32_16x16x32_bf16 a[160:163], a[184:187], v[192:195], a[160:163]\n"
"v_mul_f32 v210, s62, v210\n"
"v_cvt_pk_bf16_f32 v208, v208, v209\n"
"v_mfma_f32_16x16x32_bf16 v[192:195], a[96:99], a[48:51], 0\n"
"v_mul_f32 v207, s62, v207\n"
"v_mul_f32 v211, s62, v211\n"
"v_mfma_f32_16x16x32_bf16 v[192:195], a[100:103], a[52:55], v[192:195]\n"
"v_exp_f32 v210, v210\n"
"v_mfma_f32_16x16x32_bf16 v[192:195], a[104:107], a[56:59], v[192:195]\n"
"v_exp_f32 v207, v207\n"
"v_mfma_f32_16x16x32_bf16 v[192:195], a[108:111], a[60:63], v[192:195]\n"
"v_cvt_pk_bf16_f32 v203, v206, v207\n"
"v_mul_f32 v212, s62, v212\n"
"v_mfma_f32_16x16x32_bf16 v[196:199], a[112:115], a[48:51], 0\n"
"v_exp_f32 v211, v211\n"
"v_mfma_f32_16x16x32_bf16 v[196:199], a[116:119], a[52:55], v[196:199]\n"
"v_cvt_pk_bf16_f32 v209, v210, v211\n"
"v_mul_f32 v213, s62, v213\n"
"v_mfma_f32_16x16x32_bf16 v[196:199], a[120:123], a[56:59], v[196:199]\n"
"v_exp_f32 v212, v212\n"
"v_mfma_f32_16x16x32_bf16 v[196:199], a[124:127], a[60:63], v[196:199]\n"
"v_mul_f32 v192, s62, v192\n"
"v_mul_f32 v193, s62, v193\n"
"v_mfma_f32_16x16x32_bf16 v[32:35], a[128:131], v[200:203], v[32:35]\n"
"v_exp_f32 v192, v192\n"
"v_mfma_f32_16x16x32_bf16 v[36:39], a[132:135], v[200:203], v[36:39]\n"
"v_exp_f32 v213, v213\n"
"v_mfma_f32_16x16x32_bf16 v[40:43], a[136:139], v[200:203], v[40:43]\n"
"v_mul_f32 v214, s62, v214\n"
"v_cvt_pk_bf16_f32 v210, v212, v213\n"
"v_mfma_f32_16x16x32_bf16 v[44:47], a[140:143], v[200:203], v[44:47]\n"
"v_exp_f32 v193, v193\n"
"v_mfma_f32_16x16x32_bf16 v[48:51], a[144:147], v[200:203], v[48:51]\n"
"s_waitcnt vmcnt(4)\n"
"s_barrier\n"
"v_mfma_f32_16x16x32_bf16 v[52:55], a[148:151], v[200:203], v[52:55]\n"
"v_exp_f32 v214, v214\n"
"v_mfma_f32_16x16x32_bf16 v[56:59], a[152:155], v[200:203], v[56:59]\n"
"s_add_u32 m0, s56, 8192\n"
"s_add_u32 s72, s57, 40960\n"
"v_mfma_f32_16x16x32_bf16 v[60:63], a[156:159], v[200:203], v[60:63]\n"
"buffer_load_dwordx4 v226, s[40:43], s72 offen nt lds\n"
"v_mul_f32 v194, s62, v194\n"
"v_mfma_f32_16x16x32_bf16 a[164:167], a[184:187], v[200:203], a[164:167]\n"
"buffer_load_dwordx4 v226, s[40:43], s72 offen offset:2048 nt lds\n"
"v_cvt_pk_bf16_f32 v192, v192, v193\n"
"v_mfma_f32_16x16x32_bf16 v[200:203], a[96:99], a[64:67], 0\n"
"v_mul_f32 v215, s62, v215\n"
"s_add_u32 m0, s56, 40960\n"
"v_mfma_f32_16x16x32_bf16 v[200:203], a[100:103], a[68:71], v[200:203]\n"
"buffer_load_dwordx4 v226, s[44:47], s72 offen nt lds\n"
"v_mul_f32 v195, s62, v195\n"
"v_mfma_f32_16x16x32_bf16 v[200:203], a[104:107], a[72:75], v[200:203]\n"
"buffer_load_dwordx4 v226, s[44:47], s72 offen offset:2048 nt lds\n"
"v_exp_f32 v194, v194\n"
"v_mfma_f32_16x16x32_bf16 v[200:203], a[108:111], a[76:79], v[200:203]\n"
"v_exp_f32 v215, v215\n"
"v_mfma_f32_16x16x32_bf16 v[204:207], a[112:115], a[64:67], 0\n"
"v_cvt_pk_bf16_f32 v211, v214, v215\n"
"v_mul_f32 v196, s62, v196\n"
"v_mfma_f32_16x16x32_bf16 v[204:207], a[116:119], a[68:71], v[204:207]\n"
"v_exp_f32 v195, v195\n"
"v_mfma_f32_16x16x32_bf16 v[204:207], a[120:123], a[72:75], v[204:207]\n"
"v_cvt_pk_bf16_f32 v193, v194, v195\n"
"v_mul_f32 v200, s62, v200\n"
"v_mfma_f32_16x16x32_bf16 v[204:207], a[124:127], a[76:79], v[204:207]\n"
"v_exp_f32 v196, v196\n"
"v_mfma_f32_16x16x32_bf16 v[64:67], a[128:131], v[208:211], v[64:67]\n"
"v_mul_f32 v197, s62, v197\n"
"v_mul_f32 v201, s62, v201\n"
"v_mfma_f32_16x16x32_bf16 v[68:71], a[132:135], v[208:211], v[68:71]\n"
"v_exp_f32 v200, v200\n"
"v_mfma_f32_16x16x32_bf16 v[72:75], a[136:139], v[208:211], v[72:75]\n"
"v_exp_f32 v197, v197\n"
"v_mfma_f32_16x16x32_bf16 v[76:79], a[140:143], v[208:211], v[76:79]\n"
"v_mul_f32 v198, s62, v198\n"
"v_cvt_pk_bf16_f32 v194, v196, v197\n"
"v_mfma_f32_16x16x32_bf16 v[80:83], a[144:147], v[208:211], v[80:83]\n"
"v_exp_f32 v201, v201\n"
"v_mfma_f32_16x16x32_bf16 v[84:87], a[148:151], v[208:211], v[84:87]\n"
"v_exp_f32 v198, v198\n"
"v_mfma_f32_16x16x32_bf16 v[88:91], a[152:155], v[208:211], v[88:91]\n"
"v_mul_f32 v202, s62, v202\n"
"v_cvt_pk_bf16_f32 v200, v200, v201\n"
"v_mfma_f32_16x16x32_bf16 v[92:95], a[156:159], v[208:211], v[92:95]\n"
"v_mul_f32 v199, s62, v199\n"
"v_mul_f32 v203, s62, v203\n"
"v_mfma_f32_16x16x32_bf16 a[168:171], a[184:187], v[208:211], a[168:171]\n"
"v_exp_f32 v202, v202\n"
"v_mfma_f32_16x16x32_bf16 v[208:211], a[96:99], a[80:83], 0\n"
"ds_read_b128 a[96:99], v224 offset:16384\n"
"v_mul_f32 v204, s62, v204\n"
"v_mfma_f32_16x16x32_bf16 v[208:211], a[100:103], a[84:87], v[208:211]\n"
"v_exp_f32 v199, v199\n"
"v_mfma_f32_16x16x32_bf16 v[208:211], a[104:107], a[88:91], v[208:211]\n"
"ds_read_b128 a[100:103], v224 offset:16896\n"
"v_cvt_pk_bf16_f32 v195, v198, v199\n"
"v_mfma_f32_16x16x32_bf16 v[208:211], a[108:111], a[92:95], v[208:211]\n"
"v_exp_f32 v203, v203\n"
"v_mfma_f32_16x16x32_bf16 v[212:215], a[112:115], a[80:83], 0\n"
"ds_read_b128 a[104:107], v224 offset:17408\n"
"v_cvt_pk_bf16_f32 v201, v202, v203\n"
"v_mfma_f32_16x16x32_bf16 v[212:215], a[116:119], a[84:87], v[212:215]\n"
"v_exp_f32 v204, v204\n"
"v_mfma_f32_16x16x32_bf16 v[212:215], a[120:123], a[88:91], v[212:215]\n"
"ds_read_b128 a[108:111], v224 offset:17920\n"
"v_mul_f32 v208, s62, v208\n"
"v_mfma_f32_16x16x32_bf16 v[212:215], a[124:127], a[92:95], v[212:215]\n"
"v_mul_f32 v205, s62, v205\n"
"v_mul_f32 v209, s62, v209\n"
"v_mfma_f32_16x16x32_bf16 v[96:99], a[128:131], v[192:195], v[96:99]\n"
"ds_read_b128 a[112:115], v224 offset:20480\n"
"v_mul_f32 v206, s62, v206\n"
"v_mfma_f32_16x16x32_bf16 v[100:103], a[132:135], v[192:195], v[100:103]\n"
"v_exp_f32 v208, v208\n"
"v_mfma_f32_16x16x32_bf16 v[104:107], a[136:139], v[192:195], v[104:107]\n"
"ds_read_b128 a[116:119], v224 offset:20992\n"
"v_mul_f32 v210, s62, v210\n"
"v_mfma_f32_16x16x32_bf16 v[108:111], a[140:143], v[192:195], v[108:111]\n"
"v_exp_f32 v205, v205\n"
"v_mfma_f32_16x16x32_bf16 v[112:115], a[144:147], v[192:195], v[112:115]\n"
"ds_read_b128 a[120:123], v224 offset:21504\n"
"v_cvt_pk_bf16_f32 v202, v204, v205\n"
"v_mfma_f32_16x16x32_bf16 v[116:119], a[148:151], v[192:195], v[116:119]\n"
"v_exp_f32 v209, v209\n"
"v_mfma_f32_16x16x32_bf16 v[120:123], a[152:155], v[192:195], v[120:123]\n"
"ds_read_b128 a[124:127], v224 offset:22016\n"
"v_cvt_pk_bf16_f32 v208, v208, v209\n"
"v_mfma_f32_16x16x32_bf16 v[124:127], a[156:159], v[192:195], v[124:127]\n"
"v_exp_f32 v206, v206\n"
"v_mfma_f32_16x16x32_bf16 a[172:175], a[184:187], v[192:195], a[172:175]\n"
"v_mul_f32 v207, s62, v207\n"
"s_waitcnt lgkmcnt(2)\n"
"v_mfma_f32_16x16x32_bf16 v[192:195], a[96:99], a[0:3], 0\n"
"v_exp_f32 v210, v210\n"
"v_mfma_f32_16x16x32_bf16 v[192:195], a[100:103], a[4:7], v[192:195]\n"
"v_exp_f32 v207, v207\n"
"v_mfma_f32_16x16x32_bf16 v[192:195], a[104:107], a[8:11], v[192:195]\n"
"v_mul_f32 v211, s62, v211\n"
"v_cvt_pk_bf16_f32 v203, v206, v207\n"
"v_mfma_f32_16x16x32_bf16 v[192:195], a[108:111], a[12:15], v[192:195]\n"
"v_exp_f32 v211, v211\n"
"v_mfma_f32_16x16x32_bf16 v[196:199], a[112:115], a[0:3], 0\n"
"v_mul_f32 v212, s62, v212\n"
"v_cvt_pk_bf16_f32 v209, v210, v211\n"
"v_mfma_f32_16x16x32_bf16 v[196:199], a[116:119], a[4:7], v[196:199]\n"
"v_mul_f32 v213, s62, v213\n"
"s_waitcnt lgkmcnt(0)\n"
"v_mfma_f32_16x16x32_bf16 v[196:199], a[120:123], a[8:11], v[196:199]\n"
"v_exp_f32 v212, v212\n"
"v_mfma_f32_16x16x32_bf16 v[196:199], a[124:127], a[12:15], v[196:199]\n"
"v_mul_f32 v192, s62, v192\n"
"v_mul_f32 v193, s62, v193\n"
"v_mfma_f32_16x16x32_bf16 v[128:131], a[128:131], v[200:203], v[128:131]\n"
"v_exp_f32 v192, v192\n"
"v_mfma_f32_16x16x32_bf16 v[132:135], a[132:135], v[200:203], v[132:135]\n"
"v_exp_f32 v213, v213\n"
"v_mfma_f32_16x16x32_bf16 v[136:139], a[136:139], v[200:203], v[136:139]\n"
"v_mul_f32 v214, s62, v214\n"
"v_cvt_pk_bf16_f32 v210, v212, v213\n"
"v_mfma_f32_16x16x32_bf16 v[140:143], a[140:143], v[200:203], v[140:143]\n"
"v_exp_f32 v193, v193\n"
"v_mfma_f32_16x16x32_bf16 v[144:147], a[144:147], v[200:203], v[144:147]\n"
"v_exp_f32 v214, v214\n"
"v_mfma_f32_16x16x32_bf16 v[148:151], a[148:151], v[200:203], v[148:151]\n"
"v_mul_f32 v194, s62, v194\n"
"v_cvt_pk_bf16_f32 v192, v192, v193\n"
"v_mfma_f32_16x16x32_bf16 v[152:155], a[152:155], v[200:203], v[152:155]\n"
"v_mul_f32 v215, s62, v215\n"
"v_mul_f32 v195, s62, v195\n"
"v_mfma_f32_16x16x32_bf16 v[156:159], a[156:159], v[200:203], v[156:159]\n"
"v_exp_f32 v194, v194\n"
"v_mfma_f32_16x16x32_bf16 a[176:179], a[184:187], v[200:203], a[176:179]\n"
"v_exp_f32 v215, v215\n"
"v_mfma_f32_16x16x32_bf16 v[200:203], a[96:99], a[16:19], 0\n"
"v_cvt_pk_bf16_f32 v211, v214, v215\n"
"v_mul_f32 v196, s62, v196\n"
"v_mfma_f32_16x16x32_bf16 v[200:203], a[100:103], a[20:23], v[200:203]\n"
"v_exp_f32 v195, v195\n"
"v_mfma_f32_16x16x32_bf16 v[200:203], a[104:107], a[24:27], v[200:203]\n"
"v_cvt_pk_bf16_f32 v193, v194, v195\n"
"v_mul_f32 v197, s62, v197\n"
"v_mfma_f32_16x16x32_bf16 v[200:203], a[108:111], a[28:31], v[200:203]\n"
"v_exp_f32 v196, v196\n"
"v_mfma_f32_16x16x32_bf16 v[204:207], a[112:115], a[16:19], 0\n"
"v_exp_f32 v197, v197\n"
"v_mfma_f32_16x16x32_bf16 v[204:207], a[116:119], a[20:23], v[204:207]\n"
"v_mul_f32 v198, s62, v198\n"
"v_cvt_pk_bf16_f32 v194, v196, v197\n"
"v_mfma_f32_16x16x32_bf16 v[204:207], a[120:123], a[24:27], v[204:207]\n"
"v_exp_f32 v198, v198\n"
"v_mfma_f32_16x16x32_bf16 v[204:207], a[124:127], a[28:31], v[204:207]\n"
"v_mul_f32 v200, s62, v200\n"
"v_mul_f32 v201, s62, v201\n"
"v_mfma_f32_16x16x32_bf16 v[160:163], a[128:131], v[208:211], v[160:163]\n"
"ds_read_b64_tr_b16 a[128:129], v228 offset:16384\n"
"v_mul_f32 v202, s62, v202\n"
"v_mfma_f32_16x16x32_bf16 v[164:167], a[132:135], v[208:211], v[164:167]\n"
"ds_read_b64_tr_b16 a[130:131], v228 offset:20480\n"
"v_mul_f32 v199, s62, v199\n"
"v_mfma_f32_16x16x32_bf16 v[168:171], a[136:139], v[208:211], v[168:171]\n"
"ds_read_b64_tr_b16 a[132:133], v229 offset:16384\n"
"v_mul_f32 v203, s62, v203\n"
"v_mfma_f32_16x16x32_bf16 v[172:175], a[140:143], v[208:211], v[172:175]\n"
"ds_read_b64_tr_b16 a[134:135], v229 offset:20480\n"
"v_mul_f32 v204, s62, v204\n"
"v_mfma_f32_16x16x32_bf16 v[176:179], a[144:147], v[208:211], v[176:179]\n"
"ds_read_b64_tr_b16 a[136:137], v228 offset:16896\n"
"v_exp_f32 v200, v200\n"
"v_mfma_f32_16x16x32_bf16 v[180:183], a[148:151], v[208:211], v[180:183]\n"
"ds_read_b64_tr_b16 a[138:139], v228 offset:20992\n"
"v_mul_f32 v205, s62, v205\n"
"v_mfma_f32_16x16x32_bf16 v[184:187], a[152:155], v[208:211], v[184:187]\n"
"ds_read_b64_tr_b16 a[140:141], v229 offset:16896\n"
"v_mul_f32 v206, s62, v206\n"
"v_mfma_f32_16x16x32_bf16 v[188:191], a[156:159], v[208:211], v[188:191]\n"
"ds_read_b64_tr_b16 a[142:143], v229 offset:20992\n"
"v_exp_f32 v201, v201\n"
"v_mfma_f32_16x16x32_bf16 a[180:183], a[184:187], v[208:211], a[180:183]\n"
"ds_read_b64_tr_b16 a[144:145], v228 offset:17408\n"
"v_cvt_pk_bf16_f32 v200, v200, v201\n"
"v_mfma_f32_16x16x32_bf16 v[208:211], a[96:99], a[32:35], 0\n"
"ds_read_b64_tr_b16 a[146:147], v228 offset:21504\n"
"v_mul_f32 v207, s62, v207\n"
"v_mfma_f32_16x16x32_bf16 v[208:211], a[100:103], a[36:39], v[208:211]\n"
"ds_read_b64_tr_b16 a[148:149], v229 offset:17408\n"
"v_mfma_f32_16x16x32_bf16 v[208:211], a[104:107], a[40:43], v[208:211]\n"
"ds_read_b64_tr_b16 a[150:151], v229 offset:21504\n"
"v_exp_f32 v202, v202\n"
"v_mfma_f32_16x16x32_bf16 v[208:211], a[108:111], a[44:47], v[208:211]\n"
"ds_read_b64_tr_b16 a[152:153], v228 offset:17920\n"
"v_exp_f32 v199, v199\n"
"v_mfma_f32_16x16x32_bf16 v[212:215], a[112:115], a[32:35], 0\n"
"ds_read_b64_tr_b16 a[154:155], v228 offset:22016\n"
"v_cvt_pk_bf16_f32 v195, v198, v199\n"
"v_mfma_f32_16x16x32_bf16 v[212:215], a[116:119], a[36:39], v[212:215]\n"
"ds_read_b64_tr_b16 a[156:157], v229 offset:17920\n"
"v_exp_f32 v203, v203\n"
"v_mfma_f32_16x16x32_bf16 v[212:215], a[120:123], a[40:43], v[212:215]\n"
"ds_read_b64_tr_b16 a[158:159], v229 offset:22016\n"
"v_cvt_pk_bf16_f32 v201, v202, v203\n"
"v_mfma_f32_16x16x32_bf16 v[212:215], a[124:127], a[44:47], v[212:215]\n"
"v_mul_f32 v208, s62, v208\n"
"s_waitcnt lgkmcnt(5)\n"
"v_mfma_f32_16x16x32_bf16 v[0:3], a[128:131], v[192:195], v[0:3]\n"
"v_exp_f32 v204, v204\n"
"v_mfma_f32_16x16x32_bf16 v[4:7], a[132:135], v[192:195], v[4:7]\n"
"v_exp_f32 v208, v208\n"
"v_mfma_f32_16x16x32_bf16 v[8:11], a[136:139], v[192:195], v[8:11]\n"
"v_exp_f32 v205, v205\n"
"v_mfma_f32_16x16x32_bf16 v[12:15], a[140:143], v[192:195], v[12:15]\n"
"v_mul_f32 v209, s62, v209\n"
"v_cvt_pk_bf16_f32 v202, v204, v205\n"
"v_mfma_f32_16x16x32_bf16 v[16:19], a[144:147], v[192:195], v[16:19]\n"
"v_mul_f32 v210, s62, v210\n"
"s_waitcnt lgkmcnt(0)\n"
"v_mfma_f32_16x16x32_bf16 v[20:23], a[148:151], v[192:195], v[20:23]\n"
"v_exp_f32 v209, v209\n"
"v_mfma_f32_16x16x32_bf16 v[24:27], a[152:155], v[192:195], v[24:27]\n"
"v_exp_f32 v206, v206\n"
"v_mfma_f32_16x16x32_bf16 v[28:31], a[156:159], v[192:195], v[28:31]\n"
"v_cvt_pk_bf16_f32 v208, v208, v209\n"
"v_mul_f32 v211, s62, v211\n"
"v_mfma_f32_16x16x32_bf16 a[160:163], a[184:187], v[192:195], a[160:163]\n"
"v_exp_f32 v210, v210\n"
"v_mfma_f32_16x16x32_bf16 v[192:195], a[96:99], a[48:51], 0\n"
"v_exp_f32 v207, v207\n"
"v_mfma_f32_16x16x32_bf16 v[192:195], a[100:103], a[52:55], v[192:195]\n"
"v_cvt_pk_bf16_f32 v203, v206, v207\n"
"v_mul_f32 v212, s62, v212\n"
"v_mfma_f32_16x16x32_bf16 v[192:195], a[104:107], a[56:59], v[192:195]\n"
"v_exp_f32 v211, v211\n"
"v_mfma_f32_16x16x32_bf16 v[192:195], a[108:111], a[60:63], v[192:195]\n"
"v_cvt_pk_bf16_f32 v209, v210, v211\n"
"v_mul_f32 v213, s62, v213\n"
"v_mfma_f32_16x16x32_bf16 v[196:199], a[112:115], a[48:51], 0\n"
"v_exp_f32 v212, v212\n"
"v_mfma_f32_16x16x32_bf16 v[196:199], a[116:119], a[52:55], v[196:199]\n"
"v_exp_f32 v213, v213\n"
"v_mfma_f32_16x16x32_bf16 v[196:199], a[120:123], a[56:59], v[196:199]\n"
"v_mul_f32 v214, s62, v214\n"
"v_mul_f32 v192, s62, v192\n"
"v_mfma_f32_16x16x32_bf16 v[196:199], a[124:127], a[60:63], v[196:199]\n"
"v_exp_f32 v192, v192\n"
"v_mfma_f32_16x16x32_bf16 v[32:35], a[128:131], v[200:203], v[32:35]\n"
"v_mul_f32 v193, s62, v193\n"
"v_cvt_pk_bf16_f32 v210, v212, v213\n"
"v_mfma_f32_16x16x32_bf16 v[36:39], a[132:135], v[200:203], v[36:39]\n"
"v_exp_f32 v193, v193\n"
"v_mfma_f32_16x16x32_bf16 v[40:43], a[136:139], v[200:203], v[40:43]\n"
"v_exp_f32 v214, v214\n"
"v_mfma_f32_16x16x32_bf16 v[44:47], a[140:143], v[200:203], v[44:47]\n"
"v_mul_f32 v194, s62, v194\n"
"v_cvt_pk_bf16_f32 v192, v192, v193\n"
"v_mfma_f32_16x16x32_bf16 v[48:51], a[144:147], v[200:203], v[48:51]\n"
"s_waitcnt vmcnt(4)\n"
"s_barrier\n"
"v_mfma_f32_16x16x32_bf16 v[52:55], a[148:151], v[200:203], v[52:55]\n"
"v_mul_f32 v215, s62, v215\n"
"v_mul_f32 v195, s62, v195\n"
"v_mfma_f32_16x16x32_bf16 v[56:59], a[152:155], v[200:203], v[56:59]\n"
"s_add_u32 m0, s56, 16384\n"
"s_add_u32 s72, s57, 49152\n"
"v_mfma_f32_16x16x32_bf16 v[60:63], a[156:159], v[200:203], v[60:63]\n"
"buffer_load_dwordx4 v226, s[40:43], s72 offen nt lds\n"
"v_mul_f32 v196, s62, v196\n"
"v_mfma_f32_16x16x32_bf16 a[164:167], a[184:187], v[200:203], a[164:167]\n"
"buffer_load_dwordx4 v226, s[40:43], s72 offen offset:2048 nt lds\n"
"v_mul_f32 v197, s62, v197\n"
"v_mfma_f32_16x16x32_bf16 v[200:203], a[96:99], a[64:67], 0\n"
"v_mul_f32 v198, s62, v198\n"
"s_add_u32 m0, s56, 49152\n"
"v_mfma_f32_16x16x32_bf16 v[200:203], a[100:103], a[68:71], v[200:203]\n"
"buffer_load_dwordx4 v226, s[44:47], s72 offen nt lds\n"
"v_mul_f32 v199, s62, v199\n"
"v_mfma_f32_16x16x32_bf16 v[200:203], a[104:107], a[72:75], v[200:203]\n"
"buffer_load_dwordx4 v226, s[44:47], s72 offen offset:2048 nt lds\n"
"v_exp_f32 v194, v194\n"
"v_mfma_f32_16x16x32_bf16 v[200:203], a[108:111], a[76:79], v[200:203]\n"
"v_exp_f32 v215, v215\n"
"v_mfma_f32_16x16x32_bf16 v[204:207], a[112:115], a[64:67], 0\n"
"v_cvt_pk_bf16_f32 v211, v214, v215\n"
"v_mfma_f32_16x16x32_bf16 v[204:207], a[116:119], a[68:71], v[204:207]\n"
"v_exp_f32 v195, v195\n"
"v_mfma_f32_16x16x32_bf16 v[204:207], a[120:123], a[72:75], v[204:207]\n"
"v_cvt_pk_bf16_f32 v193, v194, v195\n"
"v_mfma_f32_16x16x32_bf16 v[204:207], a[124:127], a[76:79], v[204:207]\n"
"v_exp_f32 v196, v196\n"
"v_mfma_f32_16x16x32_bf16 v[64:67], a[128:131], v[208:211], v[64:67]\n"
"v_mul_f32 v200, s62, v200\n"
"v_mul_f32 v201, s62, v201\n"
"v_mfma_f32_16x16x32_bf16 v[68:71], a[132:135], v[208:211], v[68:71]\n"
"v_exp_f32 v200, v200\n"
"v_mfma_f32_16x16x32_bf16 v[72:75], a[136:139], v[208:211], v[72:75]\n"
"v_exp_f32 v197, v197\n"
"v_mfma_f32_16x16x32_bf16 v[76:79], a[140:143], v[208:211], v[76:79]\n"
"v_cvt_pk_bf16_f32 v194, v196, v197\n"
"v_mul_f32 v202, s62, v202\n"
"v_mfma_f32_16x16x32_bf16 v[80:83], a[144:147], v[208:211], v[80:83]\n"
"v_exp_f32 v201, v201\n"
"v_mfma_f32_16x16x32_bf16 v[84:87], a[148:151], v[208:211], v[84:87]\n"
"v_exp_f32 v198, v198\n"
"v_mfma_f32_16x16x32_bf16 v[88:91], a[152:155], v[208:211], v[88:91]\n"
"v_cvt_pk_bf16_f32 v200, v200, v201\n"
"v_mul_f32 v203, s62, v203\n"
"v_mfma_f32_16x16x32_bf16 v[92:95], a[156:159], v[208:211], v[92:95]\n"
"v_exp_f32 v202, v202\n"
"v_mfma_f32_16x16x32_bf16 a[168:171], a[184:187], v[208:211], a[168:171]\n"
"v_exp_f32 v199, v199\n"
"v_mfma_f32_16x16x32_bf16 v[208:211], a[96:99], a[80:83], 0\n"
"ds_read_b128 a[96:99], v224 offset:24576\n"
"v_cvt_pk_bf16_f32 v195, v198, v199\n"
"v_mfma_f32_16x16x32_bf16 v[208:211], a[100:103], a[84:87], v[208:211]\n"
"v_exp_f32 v203, v203\n"
"v_mfma_f32_16x16x32_bf16 v[208:211], a[104:107], a[88:91], v[208:211]\n"
"ds_read_b128 a[100:103], v224 offset:25088\n"
"v_mul_f32 v204, s62, v204\n"
"v_mfma_f32_16x16x32_bf16 v[208:211], a[108:111], a[92:95], v[208:211]\n"
"v_cvt_pk_bf16_f32 v201, v202, v203\n"
"v_mul_f32 v205, s62, v205\n"
"v_mfma_f32_16x16x32_bf16 v[212:215], a[112:115], a[80:83], 0\n"
"ds_read_b128 a[104:107], v224 offset:25600\n"
"v_mul_f32 v206, s62, v206\n"
"v_mfma_f32_16x16x32_bf16 v[212:215], a[116:119], a[84:87], v[212:215]\n"
"v_exp_f32 v204, v204\n"
"v_mfma_f32_16x16x32_bf16 v[212:215], a[120:123], a[88:91], v[212:215]\n"
"ds_read_b128 a[108:111], v224 offset:26112\n"
"v_mul_f32 v208, s62, v208\n"
"v_mfma_f32_16x16x32_bf16 v[212:215], a[124:127], a[92:95], v[212:215]\n"
"v_exp_f32 v208, v208\n"
"v_mfma_f32_16x16x32_bf16 v[96:99], a[128:131], v[192:195], v[96:99]\n"
"ds_read_b128 a[112:115], v224 offset:28672\n"
"v_mul_f32 v209, s62, v209\n"
"v_mfma_f32_16x16x32_bf16 v[100:103], a[132:135], v[192:195], v[100:103]\n"
"v_exp_f32 v205, v205\n"
"v_mfma_f32_16x16x32_bf16 v[104:107], a[136:139], v[192:195], v[104:107]\n"
"ds_read_b128 a[116:119], v224 offset:29184\n"
"v_cvt_pk_bf16_f32 v202, v204, v205\n"
"v_mfma_f32_16x16x32_bf16 v[108:111], a[140:143], v[192:195], v[108:111]\n"
"v_exp_f32 v209, v209\n"
"v_mfma_f32_16x16x32_bf16 v[112:115], a[144:147], v[192:195], v[112:115]\n"
"ds_read_b128 a[120:123], v224 offset:29696\n"
"v_mul_f32 v210, s62, v210\n"
"v_mfma_f32_16x16x32_bf16 v[116:119], a[148:151], v[192:195], v[116:119]\n"
"v_exp_f32 v206, v206\n"
"v_mfma_f32_16x16x32_bf16 v[120:123], a[152:155], v[192:195], v[120:123]\n"
"ds_read_b128 a[124:127], v224 offset:30208\n"
"v_cvt_pk_bf16_f32 v208, v208, v209\n"
"v_mfma_f32_16x16x32_bf16 v[124:127], a[156:159], v[192:195], v[124:127]\n"
"v_mul_f32 v207, s62, v207\n"
"v_mul_f32 v211, s62, v211\n"
"v_mfma_f32_16x16x32_bf16 a[172:175], a[184:187], v[192:195], a[172:175]\n"
"v_mul_f32 v212, s62, v212\n"
"s_waitcnt lgkmcnt(2)\n"
"v_mfma_f32_16x16x32_bf16 v[192:195], a[96:99], a[0:3], 0\n"
"v_exp_f32 v210, v210\n"
"v_mfma_f32_16x16x32_bf16 v[192:195], a[100:103], a[4:7], v[192:195]\n"
"v_exp_f32 v207, v207\n"
"v_mfma_f32_16x16x32_bf16 v[192:195], a[104:107], a[8:11], v[192:195]\n"
"v_cvt_pk_bf16_f32 v203, v206, v207\n"
"v_mul_f32 v213, s62, v213\n"
"v_mfma_f32_16x16x32_bf16 v[192:195], a[108:111], a[12:15], v[192:195]\n"
"v_exp_f32 v211, v211\n"
"v_mfma_f32_16x16x32_bf16 v[196:199], a[112:115], a[0:3], 0\n"
"v_cvt_pk_bf16_f32 v209, v210, v211\n"
"v_mul_f32 v214, s62, v214\n"
"v_mfma_f32_16x16x32_bf16 v[196:199], a[116:119], a[4:7], v[196:199]\n"
"v_mul_f32 v215, s62, v215\n"
"s_waitcnt lgkmcnt(0)\n"
"v_mfma_f32_16x16x32_bf16 v[196:199], a[120:123], a[8:11], v[196:199]\n"
"v_exp_f32 v212, v212\n"
"v_mfma_f32_16x16x32_bf16 v[196:199], a[124:127], a[12:15], v[196:199]\n"
"v_mul_f32 v192, s62, v192\n"
"v_mul_f32 v193, s62, v193\n"
"v_mfma_f32_16x16x32_bf16 v[128:131], a[128:131], v[200:203], v[128:131]\n"
"v_exp_f32 v192, v192\n"
"v_mfma_f32_16x16x32_bf16 v[132:135], a[132:135], v[200:203], v[132:135]\n"
"v_exp_f32 v213, v213\n"
"v_mfma_f32_16x16x32_bf16 v[136:139], a[136:139], v[200:203], v[136:139]\n"
"v_cvt_pk_bf16_f32 v210, v212, v213\n"
"v_mul_f32 v194, s62, v194\n"
"v_mfma_f32_16x16x32_bf16 v[140:143], a[140:143], v[200:203], v[140:143]\n"
"v_exp_f32 v193, v193\n"
"v_mfma_f32_16x16x32_bf16 v[144:147], a[144:147], v[200:203], v[144:147]\n"
"v_exp_f32 v214, v214\n"
"v_mfma_f32_16x16x32_bf16 v[148:151], a[148:151], v[200:203], v[148:151]\n"
"v_cvt_pk_bf16_f32 v192, v192, v193\n"
"v_mul_f32 v195, s62, v195\n"
"v_mfma_f32_16x16x32_bf16 v[152:155], a[152:155], v[200:203], v[152:155]\n"
"v_exp_f32 v194, v194\n"
"v_mfma_f32_16x16x32_bf16 v[156:159], a[156:159], v[200:203], v[156:159]\n"
"v_exp_f32 v215, v215\n"
"v_mfma_f32_16x16x32_bf16 a[176:179], a[184:187], v[200:203], a[176:179]\n"
"v_cvt_pk_bf16_f32 v211, v214, v215\n"
"v_mul_f32 v196, s62, v196\n"
"v_mfma_f32_16x16x32_bf16 v[200:203], a[96:99], a[16:19], 0\n"
"v_exp_f32 v195, v195\n"
"v_mfma_f32_16x16x32_bf16 v[200:203], a[100:103], a[20:23], v[200:203]\n"
"v_cvt_pk_bf16_f32 v193, v194, v195\n"
"v_mul_f32 v197, s62, v197\n"
"v_mfma_f32_16x16x32_bf16 v[200:203], a[104:107], a[24:27], v[200:203]\n"
"v_exp_f32 v196, v196\n"
"v_mfma_f32_16x16x32_bf16 v[200:203], a[108:111], a[28:31], v[200:203]\n"
"v_exp_f32 v197, v197\n"
"v_mfma_f32_16x16x32_bf16 v[204:207], a[112:115], a[16:19], 0\n"
"v_mul_f32 v198, s62, v198\n"
"v_cvt_pk_bf16_f32 v194, v196, v197\n"
"v_mfma_f32_16x16x32_bf16 v[204:207], a[116:119], a[20:23], v[204:207]\n"
"v_exp_f32 v198, v198\n"
"v_mfma_f32_16x16x32_bf16 v[204:207], a[120:123], a[24:27], v[204:207]\n"
"v_mul_f32 v199, s62, v199\n"
"v_mul_f32 v200, s62, v200\n"
"v_mfma_f32_16x16x32_bf16 v[204:207], a[124:127], a[28:31], v[204:207]\n"
"v_exp_f32 v200, v200\n"
"v_mfma_f32_16x16x32_bf16 v[160:163], a[128:131], v[208:211], v[160:163]\n"
"ds_read_b64_tr_b16 a[128:129], v228 offset:24576\n"
"v_mul_f32 v201, s62, v201\n"
"v_mfma_f32_16x16x32_bf16 v[164:167], a[132:135], v[208:211], v[164:167]\n"
"ds_read_b64_tr_b16 a[130:131], v228 offset:28672\n"
"v_mul_f32 v202, s62, v202\n"
"v_mfma_f32_16x16x32_bf16 v[168:171], a[136:139], v[208:211], v[168:171]\n"
"ds_read_b64_tr_b16 a[132:133], v229 offset:24576\n"
"v_mul_f32 v203, s62, v203\n"
"v_mfma_f32_16x16x32_bf16 v[172:175], a[140:143], v[208:211], v[172:175]\n"
"ds_read_b64_tr_b16 a[134:135], v229 offset:28672\n"
"v_mul_f32 v204, s62, v204\n"
"v_mfma_f32_16x16x32_bf16 v[176:179], a[144:147], v[208:211], v[176:179]\n"
"ds_read_b64_tr_b16 a[136:137], v228 offset:25088\n"
"v_mul_f32 v205, s62, v205\n"
"v_mfma_f32_16x16x32_bf16 v[180:183], a[148:151], v[208:211], v[180:183]\n"
"ds_read_b64_tr_b16 a[138:139], v228 offset:29184\n"
"v_mul_f32 v206, s62, v206\n"
"v_mfma_f32_16x16x32_bf16 v[184:187], a[152:155], v[208:211], v[184:187]\n"
"ds_read_b64_tr_b16 a[140:141], v229 offset:25088\n"
"v_mul_f32 v207, s62, v207\n"
"v_mfma_f32_16x16x32_bf16 v[188:191], a[156:159], v[208:211], v[188:191]\n"
"ds_read_b64_tr_b16 a[142:143], v229 offset:29184\n"
"v_exp_f32 v201, v201\n"
"v_mfma_f32_16x16x32_bf16 a[180:183], a[184:187], v[208:211], a[180:183]\n"
"ds_read_b64_tr_b16 a[144:145], v228 offset:25600\n"
"v_cvt_pk_bf16_f32 v200, v200, v201\n"
"v_mfma_f32_16x16x32_bf16 v[208:211], a[96:99], a[32:35], 0\n"
"ds_read_b64_tr_b16 a[146:147], v228 offset:29696\n"
"v_mfma_f32_16x16x32_bf16 v[208:211], a[100:103], a[36:39], v[208:211]\n"
"ds_read_b64_tr_b16 a[148:149], v229 offset:25600\n"
"v_mfma_f32_16x16x32_bf16 v[208:211], a[104:107], a[40:43], v[208:211]\n"
"ds_read_b64_tr_b16 a[150:151], v229 offset:29696\n"
"v_exp_f32 v202, v202\n"
"v_mfma_f32_16x16x32_bf16 v[208:211], a[108:111], a[44:47], v[208:211]\n"
"ds_read_b64_tr_b16 a[152:153], v228 offset:26112\n"
"v_exp_f32 v199, v199\n"
"v_mfma_f32_16x16x32_bf16 v[212:215], a[112:115], a[32:35], 0\n"
"ds_read_b64_tr_b16 a[154:155], v228 offset:30208\n"
"v_cvt_pk_bf16_f32 v195, v198, v199\n"
"v_mfma_f32_16x16x32_bf16 v[212:215], a[116:119], a[36:39], v[212:215]\n"
"ds_read_b64_tr_b16 a[156:157], v229 offset:26112\n"
"v_exp_f32 v203, v203\n"
"v_mfma_f32_16x16x32_bf16 v[212:215], a[120:123], a[40:43], v[212:215]\n"
"ds_read_b64_tr_b16 a[158:159], v229 offset:30208\n"
"v_cvt_pk_bf16_f32 v201, v202, v203\n"
"v_mfma_f32_16x16x32_bf16 v[212:215], a[124:127], a[44:47], v[212:215]\n"
"v_mul_f32 v208, s62, v208\n"
"s_waitcnt lgkmcnt(5)\n"
"v_mfma_f32_16x16x32_bf16 v[0:3], a[128:131], v[192:195], v[0:3]\n"
"v_exp_f32 v204, v204\n"
"v_mfma_f32_16x16x32_bf16 v[4:7], a[132:135], v[192:195], v[4:7]\n"
"v_exp_f32 v208, v208\n"
"v_mfma_f32_16x16x32_bf16 v[8:11], a[136:139], v[192:195], v[8:11]\n"
"v_exp_f32 v205, v205\n"
"v_mfma_f32_16x16x32_bf16 v[12:15], a[140:143], v[192:195], v[12:15]\n"
"v_mul_f32 v209, s62, v209\n"
"v_cvt_pk_bf16_f32 v202, v204, v205\n"
"v_mfma_f32_16x16x32_bf16 v[16:19], a[144:147], v[192:195], v[16:19]\n"
"v_mul_f32 v210, s62, v210\n"
"s_waitcnt lgkmcnt(0)\n"
"v_mfma_f32_16x16x32_bf16 v[20:23], a[148:151], v[192:195], v[20:23]\n"
"v_exp_f32 v209, v209\n"
"v_mfma_f32_16x16x32_bf16 v[24:27], a[152:155], v[192:195], v[24:27]\n"
"v_exp_f32 v206, v206\n"
"v_mfma_f32_16x16x32_bf16 v[28:31], a[156:159], v[192:195], v[28:31]\n"
"v_cvt_pk_bf16_f32 v208, v208, v209\n"
"v_mul_f32 v211, s62, v211\n"
"v_mfma_f32_16x16x32_bf16 a[160:163], a[184:187], v[192:195], a[160:163]\n"
"v_exp_f32 v210, v210\n"
"v_mfma_f32_16x16x32_bf16 v[192:195], a[96:99], a[48:51], 0\n"
"v_exp_f32 v207, v207\n"
"v_mfma_f32_16x16x32_bf16 v[192:195], a[100:103], a[52:55], v[192:195]\n"
"v_cvt_pk_bf16_f32 v203, v206, v207\n"
"v_mul_f32 v212, s62, v212\n"
"v_mfma_f32_16x16x32_bf16 v[192:195], a[104:107], a[56:59], v[192:195]\n"
"v_exp_f32 v211, v211\n"
"v_mfma_f32_16x16x32_bf16 v[192:195], a[108:111], a[60:63], v[192:195]\n"
"v_cvt_pk_bf16_f32 v209, v210, v211\n"
"v_mul_f32 v213, s62, v213\n"
"v_mfma_f32_16x16x32_bf16 v[196:199], a[112:115], a[48:51], 0\n"
"v_exp_f32 v212, v212\n"
"v_mfma_f32_16x16x32_bf16 v[196:199], a[116:119], a[52:55], v[196:199]\n"
"v_exp_f32 v213, v213\n"
"v_mfma_f32_16x16x32_bf16 v[196:199], a[120:123], a[56:59], v[196:199]\n"
"v_mul_f32 v214, s62, v214\n"
"v_mul_f32 v192, s62, v192\n"
"v_mfma_f32_16x16x32_bf16 v[196:199], a[124:127], a[60:63], v[196:199]\n"
"v_exp_f32 v192, v192\n"
"v_mfma_f32_16x16x32_bf16 v[32:35], a[128:131], v[200:203], v[32:35]\n"
"v_mul_f32 v193, s62, v193\n"
"v_cvt_pk_bf16_f32 v210, v212, v213\n"
"v_mfma_f32_16x16x32_bf16 v[36:39], a[132:135], v[200:203], v[36:39]\n"
"v_exp_f32 v193, v193\n"
"v_mfma_f32_16x16x32_bf16 v[40:43], a[136:139], v[200:203], v[40:43]\n"
"v_exp_f32 v214, v214\n"
"v_mfma_f32_16x16x32_bf16 v[44:47], a[140:143], v[200:203], v[44:47]\n"
"v_mul_f32 v194, s62, v194\n"
"v_cvt_pk_bf16_f32 v192, v192, v193\n"
"v_mfma_f32_16x16x32_bf16 v[48:51], a[144:147], v[200:203], v[48:51]\n"
"s_barrier\n"
"v_mfma_f32_16x16x32_bf16 v[52:55], a[148:151], v[200:203], v[52:55]\n"
"v_mul_f32 v215, s62, v215\n"
"v_mul_f32 v195, s62, v195\n"
"v_mfma_f32_16x16x32_bf16 v[56:59], a[152:155], v[200:203], v[56:59]\n"
"s_add_u32 m0, s56, 24576\n"
"s_add_u32 s72, s57, 57344\n"
"v_mfma_f32_16x16x32_bf16 v[60:63], a[156:159], v[200:203], v[60:63]\n"
"buffer_load_dwordx4 v226, s[40:43], s72 offen nt lds\n"
"v_mul_f32 v196, s62, v196\n"
"v_mfma_f32_16x16x32_bf16 a[164:167], a[184:187], v[200:203], a[164:167]\n"
"buffer_load_dwordx4 v226, s[40:43], s72 offen offset:2048 nt lds\n"
"v_mul_f32 v197, s62, v197\n"
"v_mfma_f32_16x16x32_bf16 v[200:203], a[96:99], a[64:67], 0\n"
"v_mul_f32 v198, s62, v198\n"
"s_add_u32 m0, s56, 57344\n"
"v_mfma_f32_16x16x32_bf16 v[200:203], a[100:103], a[68:71], v[200:203]\n"
"buffer_load_dwordx4 v226, s[44:47], s72 offen nt lds\n"
"v_mul_f32 v199, s62, v199\n"
"v_mfma_f32_16x16x32_bf16 v[200:203], a[104:107], a[72:75], v[200:203]\n"
"buffer_load_dwordx4 v226, s[44:47], s72 offen offset:2048 nt lds\n"
"v_exp_f32 v194, v194\n"
"v_mfma_f32_16x16x32_bf16 v[200:203], a[108:111], a[76:79], v[200:203]\n"
"v_exp_f32 v215, v215\n"
"v_mfma_f32_16x16x32_bf16 v[204:207], a[112:115], a[64:67], 0\n"
"v_cvt_pk_bf16_f32 v211, v214, v215\n"
"v_mfma_f32_16x16x32_bf16 v[204:207], a[116:119], a[68:71], v[204:207]\n"
"v_exp_f32 v195, v195\n"
"v_mfma_f32_16x16x32_bf16 v[204:207], a[120:123], a[72:75], v[204:207]\n"
"v_cvt_pk_bf16_f32 v193, v194, v195\n"
"v_mfma_f32_16x16x32_bf16 v[204:207], a[124:127], a[76:79], v[204:207]\n"
"v_exp_f32 v196, v196\n"
"v_mfma_f32_16x16x32_bf16 v[64:67], a[128:131], v[208:211], v[64:67]\n"
"v_mul_f32 v200, s62, v200\n"
"v_mul_f32 v201, s62, v201\n"
"v_mfma_f32_16x16x32_bf16 v[68:71], a[132:135], v[208:211], v[68:71]\n"
"v_exp_f32 v200, v200\n"
"v_mfma_f32_16x16x32_bf16 v[72:75], a[136:139], v[208:211], v[72:75]\n"
"v_exp_f32 v197, v197\n"
"v_mfma_f32_16x16x32_bf16 v[76:79], a[140:143], v[208:211], v[76:79]\n"
"v_cvt_pk_bf16_f32 v194, v196, v197\n"
"v_mul_f32 v202, s62, v202\n"
"v_mfma_f32_16x16x32_bf16 v[80:83], a[144:147], v[208:211], v[80:83]\n"
"v_exp_f32 v201, v201\n"
"v_mfma_f32_16x16x32_bf16 v[84:87], a[148:151], v[208:211], v[84:87]\n"
"v_exp_f32 v198, v198\n"
"v_mfma_f32_16x16x32_bf16 v[88:91], a[152:155], v[208:211], v[88:91]\n"
"v_cvt_pk_bf16_f32 v200, v200, v201\n"
"v_mul_f32 v203, s62, v203\n"
"v_mfma_f32_16x16x32_bf16 v[92:95], a[156:159], v[208:211], v[92:95]\n"
"v_exp_f32 v202, v202\n"
"v_mfma_f32_16x16x32_bf16 a[168:171], a[184:187], v[208:211], a[168:171]\n"
"v_exp_f32 v199, v199\n"
"v_mfma_f32_16x16x32_bf16 v[208:211], a[96:99], a[80:83], 0\n"
"ds_read_b128 a[96:99], v224 offset:0\n"
"v_cvt_pk_bf16_f32 v195, v198, v199\n"
"v_mfma_f32_16x16x32_bf16 v[208:211], a[100:103], a[84:87], v[208:211]\n"
"v_exp_f32 v203, v203\n"
"v_mfma_f32_16x16x32_bf16 v[208:211], a[104:107], a[88:91], v[208:211]\n"
"ds_read_b128 a[100:103], v224 offset:512\n"
"v_mul_f32 v204, s62, v204\n"
"v_mfma_f32_16x16x32_bf16 v[208:211], a[108:111], a[92:95], v[208:211]\n"
"v_cvt_pk_bf16_f32 v201, v202, v203\n"
"v_mul_f32 v205, s62, v205\n"
"v_mfma_f32_16x16x32_bf16 v[212:215], a[112:115], a[80:83], 0\n"
"ds_read_b128 a[104:107], v224 offset:1024\n"
"v_mul_f32 v206, s62, v206\n"
"v_mfma_f32_16x16x32_bf16 v[212:215], a[116:119], a[84:87], v[212:215]\n"
"v_exp_f32 v204, v204\n"
"v_mfma_f32_16x16x32_bf16 v[212:215], a[120:123], a[88:91], v[212:215]\n"
"ds_read_b128 a[108:111], v224 offset:1536\n"
"v_mul_f32 v208, s62, v208\n"
"v_mfma_f32_16x16x32_bf16 v[212:215], a[124:127], a[92:95], v[212:215]\n"
"v_exp_f32 v208, v208\n"
"v_mfma_f32_16x16x32_bf16 v[96:99], a[128:131], v[192:195], v[96:99]\n"
"ds_read_b128 a[112:115], v224 offset:4096\n"
"v_mul_f32 v209, s62, v209\n"
"v_mfma_f32_16x16x32_bf16 v[100:103], a[132:135], v[192:195], v[100:103]\n"
"v_exp_f32 v205, v205\n"
"v_mfma_f32_16x16x32_bf16 v[104:107], a[136:139], v[192:195], v[104:107]\n"
"ds_read_b128 a[116:119], v224 offset:4608\n"
"v_cvt_pk_bf16_f32 v202, v204, v205\n"
"v_mfma_f32_16x16x32_bf16 v[108:111], a[140:143], v[192:195], v[108:111]\n"
"v_exp_f32 v209, v209\n"
"v_mfma_f32_16x16x32_bf16 v[112:115], a[144:147], v[192:195], v[112:115]\n"
"ds_read_b128 a[120:123], v224 offset:5120\n"
"v_mul_f32 v210, s62, v210\n"
"v_mfma_f32_16x16x32_bf16 v[116:119], a[148:151], v[192:195], v[116:119]\n"
"v_exp_f32 v206, v206\n"
"v_mfma_f32_16x16x32_bf16 v[120:123], a[152:155], v[192:195], v[120:123]\n"
"ds_read_b128 a[124:127], v224 offset:5632\n"
"v_cvt_pk_bf16_f32 v208, v208, v209\n"
"v_mfma_f32_16x16x32_bf16 v[124:127], a[156:159], v[192:195], v[124:127]\n"
"v_mul_f32 v207, s62, v207\n"
"v_mul_f32 v211, s62, v211\n"
"v_mfma_f32_16x16x32_bf16 a[172:175], a[184:187], v[192:195], a[172:175]\n"
"v_mul_f32 v212, s62, v212\n"
"s_waitcnt lgkmcnt(2)\n"
"v_mfma_f32_16x16x32_bf16 v[192:195], a[96:99], a[0:3], 0\n"
"v_exp_f32 v210, v210\n"
"v_mfma_f32_16x16x32_bf16 v[192:195], a[100:103], a[4:7], v[192:195]\n"
"v_exp_f32 v207, v207\n"
"v_mfma_f32_16x16x32_bf16 v[192:195], a[104:107], a[8:11], v[192:195]\n"
"v_cvt_pk_bf16_f32 v203, v206, v207\n"
"v_mul_f32 v213, s62, v213\n"
"v_mfma_f32_16x16x32_bf16 v[192:195], a[108:111], a[12:15], v[192:195]\n"
"v_exp_f32 v211, v211\n"
"v_mfma_f32_16x16x32_bf16 v[196:199], a[112:115], a[0:3], 0\n"
"v_cvt_pk_bf16_f32 v209, v210, v211\n"
"v_mul_f32 v214, s62, v214\n"
"v_mfma_f32_16x16x32_bf16 v[196:199], a[116:119], a[4:7], v[196:199]\n"
"v_mul_f32 v215, s62, v215\n"
"s_waitcnt lgkmcnt(0)\n"
"v_mfma_f32_16x16x32_bf16 v[196:199], a[120:123], a[8:11], v[196:199]\n"
"v_exp_f32 v212, v212\n"
"v_mfma_f32_16x16x32_bf16 v[196:199], a[124:127], a[12:15], v[196:199]\n"
"v_mul_f32 v192, s62, v192\n"
"v_mul_f32 v193, s62, v193\n"
"v_mfma_f32_16x16x32_bf16 v[128:131], a[128:131], v[200:203], v[128:131]\n"
"v_exp_f32 v192, v192\n"
"v_mfma_f32_16x16x32_bf16 v[132:135], a[132:135], v[200:203], v[132:135]\n"
"v_exp_f32 v213, v213\n"
"v_mfma_f32_16x16x32_bf16 v[136:139], a[136:139], v[200:203], v[136:139]\n"
"v_cvt_pk_bf16_f32 v210, v212, v213\n"
"v_mul_f32 v194, s62, v194\n"
"v_mfma_f32_16x16x32_bf16 v[140:143], a[140:143], v[200:203], v[140:143]\n"
"v_exp_f32 v193, v193\n"
"v_mfma_f32_16x16x32_bf16 v[144:147], a[144:147], v[200:203], v[144:147]\n"
"v_exp_f32 v214, v214\n"
"v_mfma_f32_16x16x32_bf16 v[148:151], a[148:151], v[200:203], v[148:151]\n"
"v_cvt_pk_bf16_f32 v192, v192, v193\n"
"v_mul_f32 v195, s62, v195\n"
"v_mfma_f32_16x16x32_bf16 v[152:155], a[152:155], v[200:203], v[152:155]\n"
"v_exp_f32 v194, v194\n"
"v_mfma_f32_16x16x32_bf16 v[156:159], a[156:159], v[200:203], v[156:159]\n"
"v_exp_f32 v215, v215\n"
"v_mfma_f32_16x16x32_bf16 a[176:179], a[184:187], v[200:203], a[176:179]\n"
"v_cvt_pk_bf16_f32 v211, v214, v215\n"
"v_mul_f32 v196, s62, v196\n"
"v_mfma_f32_16x16x32_bf16 v[200:203], a[96:99], a[16:19], 0\n"
"v_exp_f32 v195, v195\n"
"v_mfma_f32_16x16x32_bf16 v[200:203], a[100:103], a[20:23], v[200:203]\n"
"v_cvt_pk_bf16_f32 v193, v194, v195\n"
"v_mul_f32 v197, s62, v197\n"
"v_mfma_f32_16x16x32_bf16 v[200:203], a[104:107], a[24:27], v[200:203]\n"
"v_exp_f32 v196, v196\n"
"v_mfma_f32_16x16x32_bf16 v[200:203], a[108:111], a[28:31], v[200:203]\n"
"v_exp_f32 v197, v197\n"
"v_mfma_f32_16x16x32_bf16 v[204:207], a[112:115], a[16:19], 0\n"
"v_mul_f32 v198, s62, v198\n"
"v_cvt_pk_bf16_f32 v194, v196, v197\n"
"v_mfma_f32_16x16x32_bf16 v[204:207], a[116:119], a[20:23], v[204:207]\n"
"v_exp_f32 v198, v198\n"
"v_mfma_f32_16x16x32_bf16 v[204:207], a[120:123], a[24:27], v[204:207]\n"
"v_mul_f32 v199, s62, v199\n"
"v_mul_f32 v200, s62, v200\n"
"v_mfma_f32_16x16x32_bf16 v[204:207], a[124:127], a[28:31], v[204:207]\n"
"v_exp_f32 v200, v200\n"
"v_mfma_f32_16x16x32_bf16 v[160:163], a[128:131], v[208:211], v[160:163]\n"
"ds_read_b64_tr_b16 a[128:129], v228 offset:0\n"
"v_mul_f32 v201, s62, v201\n"
"v_mfma_f32_16x16x32_bf16 v[164:167], a[132:135], v[208:211], v[164:167]\n"
"ds_read_b64_tr_b16 a[130:131], v228 offset:4096\n"
"v_mul_f32 v202, s62, v202\n"
"v_mfma_f32_16x16x32_bf16 v[168:171], a[136:139], v[208:211], v[168:171]\n"
"ds_read_b64_tr_b16 a[132:133], v229 offset:0\n"
"v_mul_f32 v203, s62, v203\n"
"v_mfma_f32_16x16x32_bf16 v[172:175], a[140:143], v[208:211], v[172:175]\n"
"ds_read_b64_tr_b16 a[134:135], v229 offset:4096\n"
"v_mul_f32 v204, s62, v204\n"
"v_mfma_f32_16x16x32_bf16 v[176:179], a[144:147], v[208:211], v[176:179]\n"
"ds_read_b64_tr_b16 a[136:137], v228 offset:512\n"
"v_mul_f32 v205, s62, v205\n"
"v_mfma_f32_16x16x32_bf16 v[180:183], a[148:151], v[208:211], v[180:183]\n"
"ds_read_b64_tr_b16 a[138:139], v228 offset:4608\n"
"v_mul_f32 v206, s62, v206\n"
"v_mfma_f32_16x16x32_bf16 v[184:187], a[152:155], v[208:211], v[184:187]\n"
"ds_read_b64_tr_b16 a[140:141], v229 offset:512\n"
"v_mul_f32 v207, s62, v207\n"
"v_mfma_f32_16x16x32_bf16 v[188:191], a[156:159], v[208:211], v[188:191]\n"
"ds_read_b64_tr_b16 a[142:143], v229 offset:4608\n"
"v_exp_f32 v201, v201\n"
"v_mfma_f32_16x16x32_bf16 a[180:183], a[184:187], v[208:211], a[180:183]\n"
"ds_read_b64_tr_b16 a[144:145], v228 offset:1024\n"
"v_cvt_pk_bf16_f32 v200, v200, v201\n"
"v_mfma_f32_16x16x32_bf16 v[208:211], a[96:99], a[32:35], 0\n"
"ds_read_b64_tr_b16 a[146:147], v228 offset:5120\n"
"v_mfma_f32_16x16x32_bf16 v[208:211], a[100:103], a[36:39], v[208:211]\n"
"ds_read_b64_tr_b16 a[148:149], v229 offset:1024\n"
"v_mfma_f32_16x16x32_bf16 v[208:211], a[104:107], a[40:43], v[208:211]\n"
"ds_read_b64_tr_b16 a[150:151], v229 offset:5120\n"
"v_exp_f32 v202, v202\n"
"v_mfma_f32_16x16x32_bf16 v[208:211], a[108:111], a[44:47], v[208:211]\n"
"ds_read_b64_tr_b16 a[152:153], v228 offset:1536\n"
"v_exp_f32 v199, v199\n"
"v_mfma_f32_16x16x32_bf16 v[212:215], a[112:115], a[32:35], 0\n"
"ds_read_b64_tr_b16 a[154:155], v228 offset:5632\n"
"v_cvt_pk_bf16_f32 v195, v198, v199\n"
"v_mfma_f32_16x16x32_bf16 v[212:215], a[116:119], a[36:39], v[212:215]\n"
"ds_read_b64_tr_b16 a[156:157], v229 offset:1536\n"
"v_exp_f32 v203, v203\n"
"v_mfma_f32_16x16x32_bf16 v[212:215], a[120:123], a[40:43], v[212:215]\n"
"ds_read_b64_tr_b16 a[158:159], v229 offset:5632\n"
"v_cvt_pk_bf16_f32 v201, v202, v203\n"
"v_mfma_f32_16x16x32_bf16 v[212:215], a[124:127], a[44:47], v[212:215]\n"
"v_mul_f32 v208, s62, v208\n"
"s_waitcnt lgkmcnt(5)\n"
"v_mfma_f32_16x16x32_bf16 v[0:3], a[128:131], v[192:195], v[0:3]\n"
"v_exp_f32 v204, v204\n"
"v_mfma_f32_16x16x32_bf16 v[4:7], a[132:135], v[192:195], v[4:7]\n"
"v_exp_f32 v208, v208\n"
"v_mfma_f32_16x16x32_bf16 v[8:11], a[136:139], v[192:195], v[8:11]\n"
"v_exp_f32 v205, v205\n"
"v_mfma_f32_16x16x32_bf16 v[12:15], a[140:143], v[192:195], v[12:15]\n"
"v_mul_f32 v209, s62, v209\n"
"v_cvt_pk_bf16_f32 v202, v204, v205\n"
"v_mfma_f32_16x16x32_bf16 v[16:19], a[144:147], v[192:195], v[16:19]\n"
"v_mul_f32 v210, s62, v210\n"
"s_waitcnt lgkmcnt(0)\n"
"v_mfma_f32_16x16x32_bf16 v[20:23], a[148:151], v[192:195], v[20:23]\n"
"v_exp_f32 v209, v209\n"
"v_mfma_f32_16x16x32_bf16 v[24:27], a[152:155], v[192:195], v[24:27]\n"
"v_exp_f32 v206, v206\n"
"v_mfma_f32_16x16x32_bf16 v[28:31], a[156:159], v[192:195], v[28:31]\n"
"v_cvt_pk_bf16_f32 v208, v208, v209\n"
"v_mul_f32 v211, s62, v211\n"
"v_mfma_f32_16x16x32_bf16 a[160:163], a[184:187], v[192:195], a[160:163]\n"
"v_exp_f32 v210, v210\n"
"v_mfma_f32_16x16x32_bf16 v[192:195], a[96:99], a[48:51], 0\n"
"v_exp_f32 v207, v207\n"
"v_mfma_f32_16x16x32_bf16 v[192:195], a[100:103], a[52:55], v[192:195]\n"
"v_cvt_pk_bf16_f32 v203, v206, v207\n"
"v_mul_f32 v212, s62, v212\n"
"v_mfma_f32_16x16x32_bf16 v[192:195], a[104:107], a[56:59], v[192:195]\n"
"v_exp_f32 v211, v211\n"
"v_mfma_f32_16x16x32_bf16 v[192:195], a[108:111], a[60:63], v[192:195]\n"
"v_cvt_pk_bf16_f32 v209, v210, v211\n"
"v_mul_f32 v213, s62, v213\n"
"v_mfma_f32_16x16x32_bf16 v[196:199], a[112:115], a[48:51], 0\n"
"v_exp_f32 v212, v212\n"
"v_mfma_f32_16x16x32_bf16 v[196:199], a[116:119], a[52:55], v[196:199]\n"
"v_exp_f32 v213, v213\n"
"v_mfma_f32_16x16x32_bf16 v[196:199], a[120:123], a[56:59], v[196:199]\n"
"v_mul_f32 v214, s62, v214\n"
"v_mul_f32 v192, s62, v192\n"
"v_mfma_f32_16x16x32_bf16 v[196:199], a[124:127], a[60:63], v[196:199]\n"
"v_exp_f32 v192, v192\n"
"v_mfma_f32_16x16x32_bf16 v[32:35], a[128:131], v[200:203], v[32:35]\n"
"v_mul_f32 v193, s62, v193\n"
"v_cvt_pk_bf16_f32 v210, v212, v213\n"
"v_mfma_f32_16x16x32_bf16 v[36:39], a[132:135], v[200:203], v[36:39]\n"
"v_exp_f32 v193, v193\n"
"v_mfma_f32_16x16x32_bf16 v[40:43], a[136:139], v[200:203], v[40:43]\n"
"v_exp_f32 v214, v214\n"
"v_mfma_f32_16x16x32_bf16 v[44:47], a[140:143], v[200:203], v[44:47]\n"
"v_mul_f32 v194, s62, v194\n"
"v_cvt_pk_bf16_f32 v192, v192, v193\n"
"v_mfma_f32_16x16x32_bf16 v[48:51], a[144:147], v[200:203], v[48:51]\n"
"s_waitcnt vmcnt(8)\n"
"s_barrier\n"
"v_mfma_f32_16x16x32_bf16 v[52:55], a[148:151], v[200:203], v[52:55]\n"
"v_mul_f32 v215, s62, v215\n"
"v_mul_f32 v195, s62, v195\n"
"v_mfma_f32_16x16x32_bf16 v[56:59], a[152:155], v[200:203], v[56:59]\n"
"s_add_u32 m0, s56, 0\n"
"s_add_u32 s72, s57, 65536\n"
"v_mfma_f32_16x16x32_bf16 v[60:63], a[156:159], v[200:203], v[60:63]\n"
"buffer_load_dwordx4 v226, s[40:43], s72 offen nt lds\n"
"v_mul_f32 v196, s62, v196\n"
"v_mfma_f32_16x16x32_bf16 a[164:167], a[184:187], v[200:203], a[164:167]\n"
"buffer_load_dwordx4 v226, s[40:43], s72 offen offset:2048 nt lds\n"
"v_mul_f32 v197, s62, v197\n"
"v_mfma_f32_16x16x32_bf16 v[200:203], a[96:99], a[64:67], 0\n"
"v_mul_f32 v198, s62, v198\n"
"s_add_u32 m0, s56, 32768\n"
"v_mfma_f32_16x16x32_bf16 v[200:203], a[100:103], a[68:71], v[200:203]\n"
"buffer_load_dwordx4 v226, s[44:47], s72 offen nt lds\n"
"v_mul_f32 v199, s62, v199\n"
"v_mfma_f32_16x16x32_bf16 v[200:203], a[104:107], a[72:75], v[200:203]\n"
"buffer_load_dwordx4 v226, s[44:47], s72 offen offset:2048 nt lds\n"
"v_exp_f32 v194, v194\n"
"v_mfma_f32_16x16x32_bf16 v[200:203], a[108:111], a[76:79], v[200:203]\n"
"v_exp_f32 v215, v215\n"
"v_mfma_f32_16x16x32_bf16 v[204:207], a[112:115], a[64:67], 0\n"
"v_cvt_pk_bf16_f32 v211, v214, v215\n"
"v_mfma_f32_16x16x32_bf16 v[204:207], a[116:119], a[68:71], v[204:207]\n"
"v_exp_f32 v195, v195\n"
"v_mfma_f32_16x16x32_bf16 v[204:207], a[120:123], a[72:75], v[204:207]\n"
"v_cvt_pk_bf16_f32 v193, v194, v195\n"
"v_mfma_f32_16x16x32_bf16 v[204:207], a[124:127], a[76:79], v[204:207]\n"
"v_exp_f32 v196, v196\n"
"v_mfma_f32_16x16x32_bf16 v[64:67], a[128:131], v[208:211], v[64:67]\n"
"v_mul_f32 v200, s62, v200\n"
"v_mul_f32 v201, s62, v201\n"
"v_mfma_f32_16x16x32_bf16 v[68:71], a[132:135], v[208:211], v[68:71]\n"
"v_exp_f32 v200, v200\n"
"v_mfma_f32_16x16x32_bf16 v[72:75], a[136:139], v[208:211], v[72:75]\n"
"v_exp_f32 v197, v197\n"
"v_mfma_f32_16x16x32_bf16 v[76:79], a[140:143], v[208:211], v[76:79]\n"
"v_cvt_pk_bf16_f32 v194, v196, v197\n"
"v_mul_f32 v202, s62, v202\n"
"v_mfma_f32_16x16x32_bf16 v[80:83], a[144:147], v[208:211], v[80:83]\n"
"v_exp_f32 v201, v201\n"
"v_mfma_f32_16x16x32_bf16 v[84:87], a[148:151], v[208:211], v[84:87]\n"
"v_exp_f32 v198, v198\n"
"v_mfma_f32_16x16x32_bf16 v[88:91], a[152:155], v[208:211], v[88:91]\n"
"v_cvt_pk_bf16_f32 v200, v200, v201\n"
"v_mul_f32 v203, s62, v203\n"
"v_mfma_f32_16x16x32_bf16 v[92:95], a[156:159], v[208:211], v[92:95]\n"
"v_exp_f32 v202, v202\n"
"v_mfma_f32_16x16x32_bf16 a[168:171], a[184:187], v[208:211], a[168:171]\n"
"v_exp_f32 v199, v199\n"
"v_mfma_f32_16x16x32_bf16 v[208:211], a[96:99], a[80:83], 0\n"
"ds_read_b128 a[96:99], v224 offset:8192\n"
"v_cvt_pk_bf16_f32 v195, v198, v199\n"
"v_mfma_f32_16x16x32_bf16 v[208:211], a[100:103], a[84:87], v[208:211]\n"
"v_exp_f32 v203, v203\n"
"v_mfma_f32_16x16x32_bf16 v[208:211], a[104:107], a[88:91], v[208:211]\n"
"ds_read_b128 a[100:103], v224 offset:8704\n"
"v_mul_f32 v204, s62, v204\n"
"v_mfma_f32_16x16x32_bf16 v[208:211], a[108:111], a[92:95], v[208:211]\n"
"v_cvt_pk_bf16_f32 v201, v202, v203\n"
"v_mul_f32 v205, s62, v205\n"
"v_mfma_f32_16x16x32_bf16 v[212:215], a[112:115], a[80:83], 0\n"
"ds_read_b128 a[104:107], v224 offset:9216\n"
"v_mul_f32 v206, s62, v206\n"
"v_mfma_f32_16x16x32_bf16 v[212:215], a[116:119], a[84:87], v[212:215]\n"
"v_exp_f32 v204, v204\n"
"v_mfma_f32_16x16x32_bf16 v[212:215], a[120:123], a[88:91], v[212:215]\n"
"ds_read_b128 a[108:111], v224 offset:9728\n"
"v_mul_f32 v208, s62, v208\n"
"v_mfma_f32_16x16x32_bf16 v[212:215], a[124:127], a[92:95], v[212:215]\n"
"v_exp_f32 v208, v208\n"
"v_mfma_f32_16x16x32_bf16 v[96:99], a[128:131], v[192:195], v[96:99]\n"
"ds_read_b128 a[112:115], v224 offset:12288\n"
"v_mul_f32 v209, s62, v209\n"
"v_mfma_f32_16x16x32_bf16 v[100:103], a[132:135], v[192:195], v[100:103]\n"
"v_exp_f32 v205, v205\n"
"v_mfma_f32_16x16x32_bf16 v[104:107], a[136:139], v[192:195], v[104:107]\n"
"ds_read_b128 a[116:119], v224 offset:12800\n"
"v_cvt_pk_bf16_f32 v202, v204, v205\n"
"v_mfma_f32_16x16x32_bf16 v[108:111], a[140:143], v[192:195], v[108:111]\n"
"v_exp_f32 v209, v209\n"
"v_mfma_f32_16x16x32_bf16 v[112:115], a[144:147], v[192:195], v[112:115]\n"
"ds_read_b128 a[120:123], v224 offset:13312\n"
"v_mul_f32 v210, s62, v210\n"
"v_mfma_f32_16x16x32_bf16 v[116:119], a[148:151], v[192:195], v[116:119]\n"
"v_exp_f32 v206, v206\n"
"v_mfma_f32_16x16x32_bf16 v[120:123], a[152:155], v[192:195], v[120:123]\n"
"ds_read_b128 a[124:127], v224 offset:13824\n"
"v_cvt_pk_bf16_f32 v208, v208, v209\n"
"v_mfma_f32_16x16x32_bf16 v[124:127], a[156:159], v[192:195], v[124:127]\n"
"v_mul_f32 v207, s62, v207\n"
"v_mul_f32 v211, s62, v211\n"
"v_mfma_f32_16x16x32_bf16 a[172:175], a[184:187], v[192:195], a[172:175]\n"
"v_mul_f32 v212, s62, v212\n"
"s_waitcnt lgkmcnt(2)\n"
"v_mfma_f32_16x16x32_bf16 v[192:195], a[96:99], a[0:3], 0\n"
"v_exp_f32 v210, v210\n"
"v_mfma_f32_16x16x32_bf16 v[192:195], a[100:103], a[4:7], v[192:195]\n"
"v_exp_f32 v207, v207\n"
"v_mfma_f32_16x16x32_bf16 v[192:195], a[104:107], a[8:11], v[192:195]\n"
"v_cvt_pk_bf16_f32 v203, v206, v207\n"
"v_mul_f32 v213, s62, v213\n"
"v_mfma_f32_16x16x32_bf16 v[192:195], a[108:111], a[12:15], v[192:195]\n"
"v_exp_f32 v211, v211\n"
"v_mfma_f32_16x16x32_bf16 v[196:199], a[112:115], a[0:3], 0\n"
"v_cvt_pk_bf16_f32 v209, v210, v211\n"
"v_mul_f32 v214, s62, v214\n"
"v_mfma_f32_16x16x32_bf16 v[196:199], a[116:119], a[4:7], v[196:199]\n"
"v_mul_f32 v215, s62, v215\n"
"s_waitcnt lgkmcnt(0)\n"
"v_mfma_f32_16x16x32_bf16 v[196:199], a[120:123], a[8:11], v[196:199]\n"
"v_exp_f32 v212, v212\n"
"v_mfma_f32_16x16x32_bf16 v[196:199], a[124:127], a[12:15], v[196:199]\n"
"v_mul_f32 v192, s62, v192\n"
"v_mul_f32 v193, s62, v193\n"
"v_mfma_f32_16x16x32_bf16 v[128:131], a[128:131], v[200:203], v[128:131]\n"
"v_exp_f32 v192, v192\n"
"v_mfma_f32_16x16x32_bf16 v[132:135], a[132:135], v[200:203], v[132:135]\n"
"v_exp_f32 v213, v213\n"
"v_mfma_f32_16x16x32_bf16 v[136:139], a[136:139], v[200:203], v[136:139]\n"
"v_cvt_pk_bf16_f32 v210, v212, v213\n"
"v_mul_f32 v194, s62, v194\n"
"v_mfma_f32_16x16x32_bf16 v[140:143], a[140:143], v[200:203], v[140:143]\n"
"v_exp_f32 v193, v193\n"
"v_mfma_f32_16x16x32_bf16 v[144:147], a[144:147], v[200:203], v[144:147]\n"
"v_exp_f32 v214, v214\n"
"v_mfma_f32_16x16x32_bf16 v[148:151], a[148:151], v[200:203], v[148:151]\n"
"v_cvt_pk_bf16_f32 v192, v192, v193\n"
"v_mul_f32 v195, s62, v195\n"
"v_mfma_f32_16x16x32_bf16 v[152:155], a[152:155], v[200:203], v[152:155]\n"
"v_exp_f32 v194, v194\n"
"v_mfma_f32_16x16x32_bf16 v[156:159], a[156:159], v[200:203], v[156:159]\n"
"v_exp_f32 v215, v215\n"
"v_mfma_f32_16x16x32_bf16 a[176:179], a[184:187], v[200:203], a[176:179]\n"
"v_cvt_pk_bf16_f32 v211, v214, v215\n"
"v_mul_f32 v196, s62, v196\n"
"v_mfma_f32_16x16x32_bf16 v[200:203], a[96:99], a[16:19], 0\n"
"v_exp_f32 v195, v195\n"
"v_mfma_f32_16x16x32_bf16 v[200:203], a[100:103], a[20:23], v[200:203]\n"
"v_cvt_pk_bf16_f32 v193, v194, v195\n"
"v_mul_f32 v197, s62, v197\n"
"v_mfma_f32_16x16x32_bf16 v[200:203], a[104:107], a[24:27], v[200:203]\n"
"v_exp_f32 v196, v196\n"
"v_mfma_f32_16x16x32_bf16 v[200:203], a[108:111], a[28:31], v[200:203]\n"
"v_exp_f32 v197, v197\n"
"v_mfma_f32_16x16x32_bf16 v[204:207], a[112:115], a[16:19], 0\n"
"v_mul_f32 v198, s62, v198\n"
"v_cvt_pk_bf16_f32 v194, v196, v197\n"
"v_mfma_f32_16x16x32_bf16 v[204:207], a[116:119], a[20:23], v[204:207]\n"
"v_exp_f32 v198, v198\n"
"v_mfma_f32_16x16x32_bf16 v[204:207], a[120:123], a[24:27], v[204:207]\n"
"v_mul_f32 v199, s62, v199\n"
"v_mul_f32 v200, s62, v200\n"
"v_mfma_f32_16x16x32_bf16 v[204:207], a[124:127], a[28:31], v[204:207]\n"
"v_exp_f32 v200, v200\n"
"v_mfma_f32_16x16x32_bf16 v[160:163], a[128:131], v[208:211], v[160:163]\n"
"ds_read_b64_tr_b16 a[128:129], v228 offset:8192\n"
"v_mul_f32 v201, s62, v201\n"
"v_mfma_f32_16x16x32_bf16 v[164:167], a[132:135], v[208:211], v[164:167]\n"
"ds_read_b64_tr_b16 a[130:131], v228 offset:12288\n"
"v_mul_f32 v202, s62, v202\n"
"v_mfma_f32_16x16x32_bf16 v[168:171], a[136:139], v[208:211], v[168:171]\n"
"ds_read_b64_tr_b16 a[132:133], v229 offset:8192\n"
"v_mul_f32 v203, s62, v203\n"
"v_mfma_f32_16x16x32_bf16 v[172:175], a[140:143], v[208:211], v[172:175]\n"
"ds_read_b64_tr_b16 a[134:135], v229 offset:12288\n"
"v_mul_f32 v204, s62, v204\n"
"v_mfma_f32_16x16x32_bf16 v[176:179], a[144:147], v[208:211], v[176:179]\n"
"ds_read_b64_tr_b16 a[136:137], v228 offset:8704\n"
"v_mul_f32 v205, s62, v205\n"
"v_mfma_f32_16x16x32_bf16 v[180:183], a[148:151], v[208:211], v[180:183]\n"
"ds_read_b64_tr_b16 a[138:139], v228 offset:12800\n"
"v_mul_f32 v206, s62, v206\n"
"v_mfma_f32_16x16x32_bf16 v[184:187], a[152:155], v[208:211], v[184:187]\n"
"ds_read_b64_tr_b16 a[140:141], v229 offset:8704\n"
"v_mul_f32 v207, s62, v207\n"
"v_mfma_f32_16x16x32_bf16 v[188:191], a[156:159], v[208:211], v[188:191]\n"
"ds_read_b64_tr_b16 a[142:143], v229 offset:12800\n"
"v_exp_f32 v201, v201\n"
"v_mfma_f32_16x16x32_bf16 a[180:183], a[184:187], v[208:211], a[180:183]\n"
"ds_read_b64_tr_b16 a[144:145], v228 offset:9216\n"
"v_cvt_pk_bf16_f32 v200, v200, v201\n"
"v_mfma_f32_16x16x32_bf16 v[208:211], a[96:99], a[32:35], 0\n"
"ds_read_b64_tr_b16 a[146:147], v228 offset:13312\n"
"v_mfma_f32_16x16x32_bf16 v[208:211], a[100:103], a[36:39], v[208:211]\n"
"ds_read_b64_tr_b16 a[148:149], v229 offset:9216\n"
"v_mfma_f32_16x16x32_bf16 v[208:211], a[104:107], a[40:43], v[208:211]\n"
"ds_read_b64_tr_b16 a[150:151], v229 offset:13312\n"
"v_exp_f32 v202, v202\n"
"v_mfma_f32_16x16x32_bf16 v[208:211], a[108:111], a[44:47], v[208:211]\n"
"ds_read_b64_tr_b16 a[152:153], v228 offset:9728\n"
"v_exp_f32 v199, v199\n"
"v_mfma_f32_16x16x32_bf16 v[212:215], a[112:115], a[32:35], 0\n"
"ds_read_b64_tr_b16 a[154:155], v228 offset:13824\n"
"v_cvt_pk_bf16_f32 v195, v198, v199\n"
"v_mfma_f32_16x16x32_bf16 v[212:215], a[116:119], a[36:39], v[212:215]\n"
"ds_read_b64_tr_b16 a[156:157], v229 offset:9728\n"
"v_exp_f32 v203, v203\n"
"v_mfma_f32_16x16x32_bf16 v[212:215], a[120:123], a[40:43], v[212:215]\n"
"ds_read_b64_tr_b16 a[158:159], v229 offset:13824\n"
"v_cvt_pk_bf16_f32 v201, v202, v203\n"
"v_mfma_f32_16x16x32_bf16 v[212:215], a[124:127], a[44:47], v[212:215]\n"
"v_mul_f32 v208, s62, v208\n"
"s_waitcnt lgkmcnt(5)\n"
"v_mfma_f32_16x16x32_bf16 v[0:3], a[128:131], v[192:195], v[0:3]\n"
"v_exp_f32 v204, v204\n"
"v_mfma_f32_16x16x32_bf16 v[4:7], a[132:135], v[192:195], v[4:7]\n"
"v_exp_f32 v208, v208\n"
"v_mfma_f32_16x16x32_bf16 v[8:11], a[136:139], v[192:195], v[8:11]\n"
"v_exp_f32 v205, v205\n"
"v_mfma_f32_16x16x32_bf16 v[12:15], a[140:143], v[192:195], v[12:15]\n"
"v_mul_f32 v209, s62, v209\n"
"v_cvt_pk_bf16_f32 v202, v204, v205\n"
"v_mfma_f32_16x16x32_bf16 v[16:19], a[144:147], v[192:195], v[16:19]\n"
"v_mul_f32 v210, s62, v210\n"
"s_waitcnt lgkmcnt(0)\n"
"v_mfma_f32_16x16x32_bf16 v[20:23], a[148:151], v[192:195], v[20:23]\n"
"v_exp_f32 v209, v209\n"
"v_mfma_f32_16x16x32_bf16 v[24:27], a[152:155], v[192:195], v[24:27]\n"
"v_exp_f32 v206, v206\n"
"v_mfma_f32_16x16x32_bf16 v[28:31], a[156:159], v[192:195], v[28:31]\n"
"v_cvt_pk_bf16_f32 v208, v208, v209\n"
"v_mul_f32 v211, s62, v211\n"
"v_mfma_f32_16x16x32_bf16 a[160:163], a[184:187], v[192:195], a[160:163]\n"
"v_exp_f32 v210, v210\n"
"v_mfma_f32_16x16x32_bf16 v[192:195], a[96:99], a[48:51], 0\n"
"v_exp_f32 v207, v207\n"
"v_mfma_f32_16x16x32_bf16 v[192:195], a[100:103], a[52:55], v[192:195]\n"
"v_cvt_pk_bf16_f32 v203, v206, v207\n"
"v_mul_f32 v212, s62, v212\n"
"v_mfma_f32_16x16x32_bf16 v[192:195], a[104:107], a[56:59], v[192:195]\n"
"v_exp_f32 v211, v211\n"
"v_mfma_f32_16x16x32_bf16 v[192:195], a[108:111], a[60:63], v[192:195]\n"
"v_cvt_pk_bf16_f32 v209, v210, v211\n"
"v_mul_f32 v213, s62, v213\n"
"v_mfma_f32_16x16x32_bf16 v[196:199], a[112:115], a[48:51], 0\n"
"v_exp_f32 v212, v212\n"
"v_mfma_f32_16x16x32_bf16 v[196:199], a[116:119], a[52:55], v[196:199]\n"
"v_exp_f32 v213, v213\n"
"v_mfma_f32_16x16x32_bf16 v[196:199], a[120:123], a[56:59], v[196:199]\n"
"v_mul_f32 v214, s62, v214\n"
"v_mul_f32 v192, s62, v192\n"
"v_mfma_f32_16x16x32_bf16 v[196:199], a[124:127], a[60:63], v[196:199]\n"
"v_exp_f32 v192, v192\n"
"v_mfma_f32_16x16x32_bf16 v[32:35], a[128:131], v[200:203], v[32:35]\n"
"v_mul_f32 v193, s62, v193\n"
"v_cvt_pk_bf16_f32 v210, v212, v213\n"
"v_mfma_f32_16x16x32_bf16 v[36:39], a[132:135], v[200:203], v[36:39]\n"
"v_exp_f32 v193, v193\n"
"v_mfma_f32_16x16x32_bf16 v[40:43], a[136:139], v[200:203], v[40:43]\n"
"v_exp_f32 v214, v214\n"
"v_mfma_f32_16x16x32_bf16 v[44:47], a[140:143], v[200:203], v[44:47]\n"
"v_mul_f32 v194, s62, v194\n"
"v_cvt_pk_bf16_f32 v192, v192, v193\n"
"v_mfma_f32_16x16x32_bf16 v[48:51], a[144:147], v[200:203], v[48:51]\n"
"s_waitcnt vmcnt(8)\n"
"s_barrier\n"
"v_mfma_f32_16x16x32_bf16 v[52:55], a[148:151], v[200:203], v[52:55]\n"
"v_mul_f32 v215, s62, v215\n"
"v_mul_f32 v195, s62, v195\n"
"v_mfma_f32_16x16x32_bf16 v[56:59], a[152:155], v[200:203], v[56:59]\n"
"s_add_u32 m0, s56, 8192\n"
"s_add_u32 s72, s57, 73728\n"
"v_mfma_f32_16x16x32_bf16 v[60:63], a[156:159], v[200:203], v[60:63]\n"
"buffer_load_dwordx4 v226, s[40:43], s72 offen nt lds\n"
"v_mul_f32 v196, s62, v196\n"
"v_mfma_f32_16x16x32_bf16 a[164:167], a[184:187], v[200:203], a[164:167]\n"
"buffer_load_dwordx4 v226, s[40:43], s72 offen offset:2048 nt lds\n"
"v_mul_f32 v197, s62, v197\n"
"v_mfma_f32_16x16x32_bf16 v[200:203], a[96:99], a[64:67], 0\n"
"v_mul_f32 v198, s62, v198\n"
"s_add_u32 m0, s56, 40960\n"
"v_mfma_f32_16x16x32_bf16 v[200:203], a[100:103], a[68:71], v[200:203]\n"
"buffer_load_dwordx4 v226, s[44:47], s72 offen nt lds\n"
"v_mul_f32 v199, s62, v199\n"
"v_mfma_f32_16x16x32_bf16 v[200:203], a[104:107], a[72:75], v[200:203]\n"
"buffer_load_dwordx4 v226, s[44:47], s72 offen offset:2048 nt lds\n"
"v_exp_f32 v194, v194\n"
"v_mfma_f32_16x16x32_bf16 v[200:203], a[108:111], a[76:79], v[200:203]\n"
"v_exp_f32 v215, v215\n"
"s_add_u32 s60, s59, 98304\n"
"s_add_u32 m0, s58, 0\n"
"v_mfma_f32_16x16x32_bf16 v[204:207], a[112:115], a[64:67], 0\n"
"buffer_load_dwordx4 v226, s[36:39], s60 offen nt lds\n"
"v_cvt_pk_bf16_f32 v211, v214, v215\n"
"v_mfma_f32_16x16x32_bf16 v[204:207], a[116:119], a[68:71], v[204:207]\n"
"buffer_load_dwordx4 v226, s[36:39], s60 offen offset:2048 nt lds\n"
"v_exp_f32 v195, v195\n"
"v_mfma_f32_16x16x32_bf16 v[204:207], a[120:123], a[72:75], v[204:207]\n"
"s_add_u32 s60, s59, 98432\n"
"s_add_u32 m0, s58, 1024\n"
"v_mfma_f32_16x16x32_bf16 v[204:207], a[124:127], a[76:79], v[204:207]\n"
"buffer_load_dwordx4 v226, s[36:39], s60 offen nt lds\n"
"v_cvt_pk_bf16_f32 v193, v194, v195\n"
"v_mfma_f32_16x16x32_bf16 v[64:67], a[128:131], v[208:211], v[64:67]\n"
"buffer_load_dwordx4 v226, s[36:39], s60 offen offset:2048 nt lds\n"
"v_mul_f32 v200, s62, v200\n"
"v_mfma_f32_16x16x32_bf16 v[68:71], a[132:135], v[208:211], v[68:71]\n"
"v_exp_f32 v196, v196\n"
"v_mfma_f32_16x16x32_bf16 v[72:75], a[136:139], v[208:211], v[72:75]\n"
"v_exp_f32 v200, v200\n"
"v_mfma_f32_16x16x32_bf16 v[76:79], a[140:143], v[208:211], v[76:79]\n"
"v_exp_f32 v197, v197\n"
"v_mfma_f32_16x16x32_bf16 v[80:83], a[144:147], v[208:211], v[80:83]\n"
"v_mul_f32 v201, s62, v201\n"
"v_cvt_pk_bf16_f32 v194, v196, v197\n"
"v_mfma_f32_16x16x32_bf16 v[84:87], a[148:151], v[208:211], v[84:87]\n"
"v_exp_f32 v201, v201\n"
"v_mfma_f32_16x16x32_bf16 v[88:91], a[152:155], v[208:211], v[88:91]\n"
"v_exp_f32 v198, v198\n"
"v_mfma_f32_16x16x32_bf16 v[92:95], a[156:159], v[208:211], v[92:95]\n"
"v_mul_f32 v202, s62, v202\n"
"v_cvt_pk_bf16_f32 v200, v200, v201\n"
"v_mfma_f32_16x16x32_bf16 a[168:171], a[184:187], v[208:211], a[168:171]\n"
"v_exp_f32 v202, v202\n"
"v_mfma_f32_16x16x32_bf16 v[208:211], a[96:99], a[80:83], 0\n"
"ds_read_b128 a[96:99], v224 offset:16384\n"
"v_mul_f32 v203, s62, v203\n"
"v_mfma_f32_16x16x32_bf16 v[208:211], a[100:103], a[84:87], v[208:211]\n"
"v_exp_f32 v199, v199\n"
"v_mfma_f32_16x16x32_bf16 v[208:211], a[104:107], a[88:91], v[208:211]\n"
"ds_read_b128 a[100:103], v224 offset:16896\n"
"v_cvt_pk_bf16_f32 v195, v198, v199\n"
"v_mfma_f32_16x16x32_bf16 v[208:211], a[108:111], a[92:95], v[208:211]\n"
"v_exp_f32 v203, v203\n"
"v_mfma_f32_16x16x32_bf16 v[212:215], a[112:115], a[80:83], 0\n"
"ds_read_b128 a[104:107], v224 offset:17408\n"
"v_mul_f32 v204, s62, v204\n"
"v_mfma_f32_16x16x32_bf16 v[212:215], a[116:119], a[84:87], v[212:215]\n"
"v_cvt_pk_bf16_f32 v201, v202, v203\n"
"v_mul_f32 v205, s62, v205\n"
"v_mfma_f32_16x16x32_bf16 v[212:215], a[120:123], a[88:91], v[212:215]\n"
"ds_read_b128 a[108:111], v224 offset:17920\n"
"v_mul_f32 v208, s62, v208\n"
"v_mfma_f32_16x16x32_bf16 v[212:215], a[124:127], a[92:95], v[212:215]\n"
"v_exp_f32 v204, v204\n"
"v_mfma_f32_16x16x32_bf16 v[96:99], a[128:131], v[192:195], v[96:99]\n"
"ds_read_b128 a[112:115], v224 offset:20480\n"
"v_mul_f32 v209, s62, v209\n"
"v_mfma_f32_16x16x32_bf16 v[100:103], a[132:135], v[192:195], v[100:103]\n"
"v_exp_f32 v208, v208\n"
"v_mfma_f32_16x16x32_bf16 v[104:107], a[136:139], v[192:195], v[104:107]\n"
"ds_read_b128 a[116:119], v224 offset:20992\n"
"v_mul_f32 v206, s62, v206\n"
"v_mfma_f32_16x16x32_bf16 v[108:111], a[140:143], v[192:195], v[108:111]\n"
"v_exp_f32 v205, v205\n"
"v_mfma_f32_16x16x32_bf16 v[112:115], a[144:147], v[192:195], v[112:115]\n"
"ds_read_b128 a[120:123], v224 offset:21504\n"
"v_cvt_pk_bf16_f32 v202, v204, v205\n"
"v_mfma_f32_16x16x32_bf16 v[116:119], a[148:151], v[192:195], v[116:119]\n"
"v_exp_f32 v209, v209\n"
"v_mfma_f32_16x16x32_bf16 v[120:123], a[152:155], v[192:195], v[120:123]\n"
"ds_read_b128 a[124:127], v224 offset:22016\n"
"v_mul_f32 v210, s62, v210\n"
"v_mfma_f32_16x16x32_bf16 v[124:127], a[156:159], v[192:195], v[124:127]\n"
"v_exp_f32 v206, v206\n"
"v_mfma_f32_16x16x32_bf16 a[172:175], a[184:187], v[192:195], a[172:175]\n"
"v_cvt_pk_bf16_f32 v208, v208, v209\n"
"s_waitcnt lgkmcnt(2)\n"
"v_mfma_f32_16x16x32_bf16 v[192:195], a[96:99], a[0:3], 0\n"
"v_mul_f32 v207, s62, v207\n"
"v_mul_f32 v211, s62, v211\n"
"v_mfma_f32_16x16x32_bf16 v[192:195], a[100:103], a[4:7], v[192:195]\n"
"v_exp_f32 v210, v210\n"
"v_mfma_f32_16x16x32_bf16 v[192:195], a[104:107], a[8:11], v[192:195]\n"
"v_exp_f32 v207, v207\n"
"v_mfma_f32_16x16x32_bf16 v[192:195], a[108:111], a[12:15], v[192:195]\n"
"v_cvt_pk_bf16_f32 v203, v206, v207\n"
"v_mul_f32 v212, s62, v212\n"
"v_mfma_f32_16x16x32_bf16 v[196:199], a[112:115], a[0:3], 0\n"
"v_exp_f32 v211, v211\n"
"v_mfma_f32_16x16x32_bf16 v[196:199], a[116:119], a[4:7], v[196:199]\n"
"v_cvt_pk_bf16_f32 v209, v210, v211\n"
"s_waitcnt lgkmcnt(0)\n"
"v_mfma_f32_16x16x32_bf16 v[196:199], a[120:123], a[8:11], v[196:199]\n"
"v_exp_f32 v212, v212\n"
"v_mfma_f32_16x16x32_bf16 v[196:199], a[124:127], a[12:15], v[196:199]\n"
"v_mul_f32 v192, s62, v192\n"
"v_mul_f32 v213, s62, v213\n"
"v_mfma_f32_16x16x32_bf16 v[128:131], a[128:131], v[200:203], v[128:131]\n"
"v_exp_f32 v192, v192\n"
"v_mfma_f32_16x16x32_bf16 v[132:135], a[132:135], v[200:203], v[132:135]\n"
"v_exp_f32 v213, v213\n"
"v_mfma_f32_16x16x32_bf16 v[136:139], a[136:139], v[200:203], v[136:139]\n"
"v_mul_f32 v193, s62, v193\n"
"v_mul_f32 v214, s62, v214\n"
"v_mfma_f32_16x16x32_bf16 v[140:143], a[140:143], v[200:203], v[140:143]\n"
"v_cvt_pk_bf16_f32 v210, v212, v213\n"
"v_mul_f32 v194, s62, v194\n"
"v_mfma_f32_16x16x32_bf16 v[144:147], a[144:147], v[200:203], v[144:147]\n"
"v_exp_f32 v193, v193\n"
"v_mfma_f32_16x16x32_bf16 v[148:151], a[148:151], v[200:203], v[148:151]\n"
"v_exp_f32 v214, v214\n"
"v_mfma_f32_16x16x32_bf16 v[152:155], a[152:155], v[200:203], v[152:155]\n"
"v_cvt_pk_bf16_f32 v192, v192, v193\n"
"v_mul_f32 v215, s62, v215\n"
"v_mfma_f32_16x16x32_bf16 v[156:159], a[156:159], v[200:203], v[156:159]\n"
"v_exp_f32 v194, v194\n"
"v_mfma_f32_16x16x32_bf16 a[176:179], a[184:187], v[200:203], a[176:179]\n"
"v_exp_f32 v215, v215\n"
"v_mfma_f32_16x16x32_bf16 v[200:203], a[96:99], a[16:19], 0\n"
"v_mul_f32 v195, s62, v195\n"
"v_cvt_pk_bf16_f32 v211, v214, v215\n"
"v_mfma_f32_16x16x32_bf16 v[200:203], a[100:103], a[20:23], v[200:203]\n"
"v_exp_f32 v195, v195\n"
"v_mfma_f32_16x16x32_bf16 v[200:203], a[104:107], a[24:27], v[200:203]\n"
"v_mul_f32 v196, s62, v196\n"
"v_cvt_pk_bf16_f32 v193, v194, v195\n"
"v_mfma_f32_16x16x32_bf16 v[200:203], a[108:111], a[28:31], v[200:203]\n"
"v_exp_f32 v196, v196\n"
"v_mfma_f32_16x16x32_bf16 v[204:207], a[112:115], a[16:19], 0\n"
"v_mul_f32 v197, s62, v197\n"
"v_mul_f32 v198, s62, v198\n"
"v_mfma_f32_16x16x32_bf16 v[204:207], a[116:119], a[20:23], v[204:207]\n"
"v_exp_f32 v197, v197\n"
"v_mfma_f32_16x16x32_bf16 v[204:207], a[120:123], a[24:27], v[204:207]\n"
"v_cvt_pk_bf16_f32 v194, v196, v197\n"
"v_mul_f32 v200, s62, v200\n"
"v_mfma_f32_16x16x32_bf16 v[204:207], a[124:127], a[28:31], v[204:207]\n"
"v_exp_f32 v200, v200\n"
"v_mfma_f32_16x16x32_bf16 v[160:163], a[128:131], v[208:211], v[160:163]\n"
"ds_read_b64_tr_b16 a[128:129], v228 offset:16384\n"
"v_mul_f32 v201, s62, v201\n"
"v_mfma_f32_16x16x32_bf16 v[164:167], a[132:135], v[208:211], v[164:167]\n"
"ds_read_b64_tr_b16 a[130:131], v228 offset:20480\n"
"v_mul_f32 v202, s62, v202\n"
"v_mfma_f32_16x16x32_bf16 v[168:171], a[136:139], v[208:211], v[168:171]\n"
"ds_read_b64_tr_b16 a[132:133], v229 offset:16384\n"
"v_mul_f32 v199, s62, v199\n"
"v_mfma_f32_16x16x32_bf16 v[172:175], a[140:143], v[208:211], v[172:175]\n"
"ds_read_b64_tr_b16 a[134:135], v229 offset:20480\n"
"v_mul_f32 v203, s62, v203\n"
"v_mfma_f32_16x16x32_bf16 v[176:179], a[144:147], v[208:211], v[176:179]\n"
"ds_read_b64_tr_b16 a[136:137], v228 offset:16896\n"
"v_mul_f32 v204, s62, v204\n"
"v_mfma_f32_16x16x32_bf16 v[180:183], a[148:151], v[208:211], v[180:183]\n"
"ds_read_b64_tr_b16 a[138:139], v228 offset:20992\n"
"v_mul_f32 v205, s62, v205\n"
"v_mfma_f32_16x16x32_bf16 v[184:187], a[152:155], v[208:211], v[184:187]\n"
"ds_read_b64_tr_b16 a[140:141], v229 offset:16896\n"
"v_mul_f32 v206, s62, v206\n"
"v_mfma_f32_16x16x32_bf16 v[188:191], a[156:159], v[208:211], v[188:191]\n"
"ds_read_b64_tr_b16 a[142:143], v229 offset:20992\n"
"v_exp_f32 v201, v201\n"
"v_mfma_f32_16x16x32_bf16 a[180:183], a[184:187], v[208:211], a[180:183]\n"
"ds_read_b64_tr_b16 a[144:145], v228 offset:17408\n"
"v_exp_f32 v198, v198\n"
"v_mfma_f32_16x16x32_bf16 v[208:211], a[96:99], a[32:35], 0\n"
"ds_read_b64_tr_b16 a[146:147], v228 offset:21504\n"
"v_cvt_pk_bf16_f32 v200, v200, v201\n"
"v_mfma_f32_16x16x32_bf16 v[208:211], a[100:103], a[36:39], v[208:211]\n"
"ds_read_b64_tr_b16 a[148:149], v229 offset:17408\n"
"v_mul_f32 v207, s62, v207\n"
"v_mfma_f32_16x16x32_bf16 v[208:211], a[104:107], a[40:43], v[208:211]\n"
"ds_read_b64_tr_b16 a[150:151], v229 offset:21504\n"
"v_exp_f32 v202, v202\n"
"v_mfma_f32_16x16x32_bf16 v[208:211], a[108:111], a[44:47], v[208:211]\n"
"ds_read_b64_tr_b16 a[152:153], v228 offset:17920\n"
"v_exp_f32 v199, v199\n"
"v_mfma_f32_16x16x32_bf16 v[212:215], a[112:115], a[32:35], 0\n"
"ds_read_b64_tr_b16 a[154:155], v228 offset:22016\n"
"v_cvt_pk_bf16_f32 v195, v198, v199\n"
"v_mfma_f32_16x16x32_bf16 v[212:215], a[116:119], a[36:39], v[212:215]\n"
"ds_read_b64_tr_b16 a[156:157], v229 offset:17920\n"
"v_exp_f32 v203, v203\n"
"v_mfma_f32_16x16x32_bf16 v[212:215], a[120:123], a[40:43], v[212:215]\n"
"ds_read_b64_tr_b16 a[158:159], v229 offset:22016\n"
"v_cvt_pk_bf16_f32 v201, v202, v203\n"
"v_mfma_f32_16x16x32_bf16 v[212:215], a[124:127], a[44:47], v[212:215]\n"
"v_mul_f32 v208, s62, v208\n"
"s_waitcnt lgkmcnt(5)\n"
"v_mfma_f32_16x16x32_bf16 v[0:3], a[128:131], v[192:195], v[0:3]\n"
"v_exp_f32 v204, v204\n"
"v_mfma_f32_16x16x32_bf16 v[4:7], a[132:135], v[192:195], v[4:7]\n"
"v_exp_f32 v208, v208\n"
"v_mfma_f32_16x16x32_bf16 v[8:11], a[136:139], v[192:195], v[8:11]\n"
"v_exp_f32 v205, v205\n"
"v_mfma_f32_16x16x32_bf16 v[12:15], a[140:143], v[192:195], v[12:15]\n"
"v_mul_f32 v209, s62, v209\n"
"v_cvt_pk_bf16_f32 v202, v204, v205\n"
"v_mfma_f32_16x16x32_bf16 v[16:19], a[144:147], v[192:195], v[16:19]\n"
"v_mul_f32 v210, s62, v210\n"
"s_waitcnt lgkmcnt(0)\n"
"v_mfma_f32_16x16x32_bf16 v[20:23], a[148:151], v[192:195], v[20:23]\n"
"v_exp_f32 v209, v209\n"
"v_mfma_f32_16x16x32_bf16 v[24:27], a[152:155], v[192:195], v[24:27]\n"
"v_exp_f32 v206, v206\n"
"v_mfma_f32_16x16x32_bf16 v[28:31], a[156:159], v[192:195], v[28:31]\n"
"v_cvt_pk_bf16_f32 v208, v208, v209\n"
"v_mul_f32 v211, s62, v211\n"
"v_mfma_f32_16x16x32_bf16 a[160:163], a[184:187], v[192:195], a[160:163]\n"
"v_exp_f32 v210, v210\n"
"v_mfma_f32_16x16x32_bf16 v[192:195], a[96:99], a[48:51], 0\n"
"v_exp_f32 v207, v207\n"
"v_mfma_f32_16x16x32_bf16 v[192:195], a[100:103], a[52:55], v[192:195]\n"
"v_cvt_pk_bf16_f32 v203, v206, v207\n"
"v_mul_f32 v212, s62, v212\n"
"v_mfma_f32_16x16x32_bf16 v[192:195], a[104:107], a[56:59], v[192:195]\n"
"v_exp_f32 v211, v211\n"
"v_mfma_f32_16x16x32_bf16 v[192:195], a[108:111], a[60:63], v[192:195]\n"
"v_cvt_pk_bf16_f32 v209, v210, v211\n"
"v_mul_f32 v213, s62, v213\n"
"v_mfma_f32_16x16x32_bf16 v[196:199], a[112:115], a[48:51], 0\n"
"v_exp_f32 v212, v212\n"
"v_mfma_f32_16x16x32_bf16 v[196:199], a[116:119], a[52:55], v[196:199]\n"
"v_exp_f32 v213, v213\n"
"v_mfma_f32_16x16x32_bf16 v[196:199], a[120:123], a[56:59], v[196:199]\n"
"v_mul_f32 v214, s62, v214\n"
"v_mul_f32 v192, s62, v192\n"
"v_mfma_f32_16x16x32_bf16 v[196:199], a[124:127], a[60:63], v[196:199]\n"
"v_exp_f32 v192, v192\n"
"v_mfma_f32_16x16x32_bf16 v[32:35], a[128:131], v[200:203], v[32:35]\n"
"v_mul_f32 v193, s62, v193\n"
"v_cvt_pk_bf16_f32 v210, v212, v213\n"
"v_mfma_f32_16x16x32_bf16 v[36:39], a[132:135], v[200:203], v[36:39]\n"
"v_exp_f32 v193, v193\n"
"v_mfma_f32_16x16x32_bf16 v[40:43], a[136:139], v[200:203], v[40:43]\n"
"v_exp_f32 v214, v214\n"
"v_mfma_f32_16x16x32_bf16 v[44:47], a[140:143], v[200:203], v[44:47]\n"
"v_mul_f32 v194, s62, v194\n"
"v_cvt_pk_bf16_f32 v192, v192, v193\n"
"v_mfma_f32_16x16x32_bf16 v[48:51], a[144:147], v[200:203], v[48:51]\n"
"s_waitcnt vmcnt(12)\n"
"s_barrier\n"
"v_mfma_f32_16x16x32_bf16 v[52:55], a[148:151], v[200:203], v[52:55]\n"
"v_mul_f32 v215, s62, v215\n"
"v_mul_f32 v195, s62, v195\n"
"v_mfma_f32_16x16x32_bf16 v[56:59], a[152:155], v[200:203], v[56:59]\n"
"s_add_u32 m0, s56, 16384\n"
"s_add_u32 s72, s57, 81920\n"
"v_mfma_f32_16x16x32_bf16 v[60:63], a[156:159], v[200:203], v[60:63]\n"
"buffer_load_dwordx4 v226, s[40:43], s72 offen nt lds\n"
"v_mul_f32 v196, s62, v196\n"
"v_mfma_f32_16x16x32_bf16 a[164:167], a[184:187], v[200:203], a[164:167]\n"
"buffer_load_dwordx4 v226, s[40:43], s72 offen offset:2048 nt lds\n"
"v_mul_f32 v197, s62, v197\n"
"v_mfma_f32_16x16x32_bf16 v[200:203], a[96:99], a[64:67], 0\n"
"v_mul_f32 v198, s62, v198\n"
"s_add_u32 m0, s56, 49152\n"
"v_mfma_f32_16x16x32_bf16 v[200:203], a[100:103], a[68:71], v[200:203]\n"
"buffer_load_dwordx4 v226, s[44:47], s72 offen nt lds\n"
"v_mul_f32 v199, s62, v199\n"
"v_mfma_f32_16x16x32_bf16 v[200:203], a[104:107], a[72:75], v[200:203]\n"
"buffer_load_dwordx4 v226, s[44:47], s72 offen offset:2048 nt lds\n"
"v_exp_f32 v194, v194\n"
"v_mfma_f32_16x16x32_bf16 v[200:203], a[108:111], a[76:79], v[200:203]\n"
"v_exp_f32 v215, v215\n"
"s_add_u32 s60, s59, 102400\n"
"s_add_u32 m0, s58, 4096\n"
"v_mfma_f32_16x16x32_bf16 v[204:207], a[112:115], a[64:67], 0\n"
"buffer_load_dwordx4 v226, s[36:39], s60 offen nt lds\n"
"v_cvt_pk_bf16_f32 v211, v214, v215\n"
"v_mfma_f32_16x16x32_bf16 v[204:207], a[116:119], a[68:71], v[204:207]\n"
"buffer_load_dwordx4 v226, s[36:39], s60 offen offset:2048 nt lds\n"
"v_exp_f32 v195, v195\n"
"v_mfma_f32_16x16x32_bf16 v[204:207], a[120:123], a[72:75], v[204:207]\n"
"s_add_u32 s60, s59, 102528\n"
"s_add_u32 m0, s58, 5120\n"
"v_mfma_f32_16x16x32_bf16 v[204:207], a[124:127], a[76:79], v[204:207]\n"
"buffer_load_dwordx4 v226, s[36:39], s60 offen nt lds\n"
"v_cvt_pk_bf16_f32 v193, v194, v195\n"
"v_mfma_f32_16x16x32_bf16 v[64:67], a[128:131], v[208:211], v[64:67]\n"
"buffer_load_dwordx4 v226, s[36:39], s60 offen offset:2048 nt lds\n"
"v_mul_f32 v200, s62, v200\n"
"v_mfma_f32_16x16x32_bf16 v[68:71], a[132:135], v[208:211], v[68:71]\n"
"v_exp_f32 v196, v196\n"
"v_mfma_f32_16x16x32_bf16 v[72:75], a[136:139], v[208:211], v[72:75]\n"
"v_exp_f32 v200, v200\n"
"v_mfma_f32_16x16x32_bf16 v[76:79], a[140:143], v[208:211], v[76:79]\n"
"v_exp_f32 v197, v197\n"
"v_mfma_f32_16x16x32_bf16 v[80:83], a[144:147], v[208:211], v[80:83]\n"
"v_mul_f32 v201, s62, v201\n"
"v_cvt_pk_bf16_f32 v194, v196, v197\n"
"v_mfma_f32_16x16x32_bf16 v[84:87], a[148:151], v[208:211], v[84:87]\n"
"v_exp_f32 v201, v201\n"
"v_mfma_f32_16x16x32_bf16 v[88:91], a[152:155], v[208:211], v[88:91]\n"
"v_exp_f32 v198, v198\n"
"v_mfma_f32_16x16x32_bf16 v[92:95], a[156:159], v[208:211], v[92:95]\n"
"v_mul_f32 v202, s62, v202\n"
"v_cvt_pk_bf16_f32 v200, v200, v201\n"
"v_mfma_f32_16x16x32_bf16 a[168:171], a[184:187], v[208:211], a[168:171]\n"
"v_exp_f32 v202, v202\n"
"v_mfma_f32_16x16x32_bf16 v[208:211], a[96:99], a[80:83], 0\n"
"ds_read_b128 a[96:99], v224 offset:24576\n"
"v_mul_f32 v203, s62, v203\n"
"v_mfma_f32_16x16x32_bf16 v[208:211], a[100:103], a[84:87], v[208:211]\n"
"v_exp_f32 v199, v199\n"
"v_mfma_f32_16x16x32_bf16 v[208:211], a[104:107], a[88:91], v[208:211]\n"
"ds_read_b128 a[100:103], v224 offset:25088\n"
"v_cvt_pk_bf16_f32 v195, v198, v199\n"
"v_mfma_f32_16x16x32_bf16 v[208:211], a[108:111], a[92:95], v[208:211]\n"
"v_exp_f32 v203, v203\n"
"v_mfma_f32_16x16x32_bf16 v[212:215], a[112:115], a[80:83], 0\n"
"ds_read_b128 a[104:107], v224 offset:25600\n"
"v_mul_f32 v204, s62, v204\n"
"v_mfma_f32_16x16x32_bf16 v[212:215], a[116:119], a[84:87], v[212:215]\n"
"v_cvt_pk_bf16_f32 v201, v202, v203\n"
"v_mul_f32 v205, s62, v205\n"
"v_mfma_f32_16x16x32_bf16 v[212:215], a[120:123], a[88:91], v[212:215]\n"
"ds_read_b128 a[108:111], v224 offset:26112\n"
"v_mul_f32 v208, s62, v208\n"
"v_mfma_f32_16x16x32_bf16 v[212:215], a[124:127], a[92:95], v[212:215]\n"
"v_exp_f32 v204, v204\n"
"v_mfma_f32_16x16x32_bf16 v[96:99], a[128:131], v[192:195], v[96:99]\n"
"ds_read_b128 a[112:115], v224 offset:28672\n"
"v_mul_f32 v209, s62, v209\n"
"v_mfma_f32_16x16x32_bf16 v[100:103], a[132:135], v[192:195], v[100:103]\n"
"v_exp_f32 v208, v208\n"
"v_mfma_f32_16x16x32_bf16 v[104:107], a[136:139], v[192:195], v[104:107]\n"
"ds_read_b128 a[116:119], v224 offset:29184\n"
"v_mul_f32 v206, s62, v206\n"
"v_mfma_f32_16x16x32_bf16 v[108:111], a[140:143], v[192:195], v[108:111]\n"
"v_exp_f32 v205, v205\n"
"v_mfma_f32_16x16x32_bf16 v[112:115], a[144:147], v[192:195], v[112:115]\n"
"ds_read_b128 a[120:123], v224 offset:29696\n"
"v_cvt_pk_bf16_f32 v202, v204, v205\n"
"v_mfma_f32_16x16x32_bf16 v[116:119], a[148:151], v[192:195], v[116:119]\n"
"v_exp_f32 v209, v209\n"
"v_mfma_f32_16x16x32_bf16 v[120:123], a[152:155], v[192:195], v[120:123]\n"
"ds_read_b128 a[124:127], v224 offset:30208\n"
"v_mul_f32 v210, s62, v210\n"
"v_mfma_f32_16x16x32_bf16 v[124:127], a[156:159], v[192:195], v[124:127]\n"
"v_exp_f32 v206, v206\n"
"v_mfma_f32_16x16x32_bf16 a[172:175], a[184:187], v[192:195], a[172:175]\n"
"v_cvt_pk_bf16_f32 v208, v208, v209\n"
"s_waitcnt lgkmcnt(2)\n"
"v_mfma_f32_16x16x32_bf16 v[192:195], a[96:99], a[0:3], 0\n"
"v_mul_f32 v207, s62, v207\n"
"v_mul_f32 v211, s62, v211\n"
"v_mfma_f32_16x16x32_bf16 v[192:195], a[100:103], a[4:7], v[192:195]\n"
"v_exp_f32 v210, v210\n"
"v_mfma_f32_16x16x32_bf16 v[192:195], a[104:107], a[8:11], v[192:195]\n"
"v_exp_f32 v207, v207\n"
"v_mfma_f32_16x16x32_bf16 v[192:195], a[108:111], a[12:15], v[192:195]\n"
"v_cvt_pk_bf16_f32 v203, v206, v207\n"
"v_mul_f32 v212, s62, v212\n"
"v_mfma_f32_16x16x32_bf16 v[196:199], a[112:115], a[0:3], 0\n"
"v_exp_f32 v211, v211\n"
"v_mfma_f32_16x16x32_bf16 v[196:199], a[116:119], a[4:7], v[196:199]\n"
"v_cvt_pk_bf16_f32 v209, v210, v211\n"
"s_waitcnt lgkmcnt(0)\n"
"v_mfma_f32_16x16x32_bf16 v[196:199], a[120:123], a[8:11], v[196:199]\n"
"v_exp_f32 v212, v212\n"
"v_mfma_f32_16x16x32_bf16 v[196:199], a[124:127], a[12:15], v[196:199]\n"
"v_mul_f32 v192, s62, v192\n"
"v_mul_f32 v213, s62, v213\n"
"v_mfma_f32_16x16x32_bf16 v[128:131], a[128:131], v[200:203], v[128:131]\n"
"v_exp_f32 v192, v192\n"
"v_mfma_f32_16x16x32_bf16 v[132:135], a[132:135], v[200:203], v[132:135]\n"
"v_exp_f32 v213, v213\n"
"v_mfma_f32_16x16x32_bf16 v[136:139], a[136:139], v[200:203], v[136:139]\n"
"v_mul_f32 v193, s62, v193\n"
"v_mul_f32 v214, s62, v214\n"
"v_mfma_f32_16x16x32_bf16 v[140:143], a[140:143], v[200:203], v[140:143]\n"
"v_cvt_pk_bf16_f32 v210, v212, v213\n"
"v_mul_f32 v194, s62, v194\n"
"v_mfma_f32_16x16x32_bf16 v[144:147], a[144:147], v[200:203], v[144:147]\n"
"v_exp_f32 v193, v193\n"
"v_mfma_f32_16x16x32_bf16 v[148:151], a[148:151], v[200:203], v[148:151]\n"
"v_exp_f32 v214, v214\n"
"v_mfma_f32_16x16x32_bf16 v[152:155], a[152:155], v[200:203], v[152:155]\n"
"v_cvt_pk_bf16_f32 v192, v192, v193\n"
"v_mul_f32 v215, s62, v215\n"
"v_mfma_f32_16x16x32_bf16 v[156:159], a[156:159], v[200:203], v[156:159]\n"
"v_exp_f32 v194, v194\n"
"v_mfma_f32_16x16x32_bf16 a[176:179], a[184:187], v[200:203], a[176:179]\n"
"v_exp_f32 v215, v215\n"
"v_mfma_f32_16x16x32_bf16 v[200:203], a[96:99], a[16:19], 0\n"
"v_mul_f32 v195, s62, v195\n"
"v_cvt_pk_bf16_f32 v211, v214, v215\n"
"v_mfma_f32_16x16x32_bf16 v[200:203], a[100:103], a[20:23], v[200:203]\n"
"v_exp_f32 v195, v195\n"
"v_mfma_f32_16x16x32_bf16 v[200:203], a[104:107], a[24:27], v[200:203]\n"
"v_mul_f32 v196, s62, v196\n"
"v_cvt_pk_bf16_f32 v193, v194, v195\n"
"v_mfma_f32_16x16x32_bf16 v[200:203], a[108:111], a[28:31], v[200:203]\n"
"v_exp_f32 v196, v196\n"
"v_mfma_f32_16x16x32_bf16 v[204:207], a[112:115], a[16:19], 0\n"
"v_mul_f32 v197, s62, v197\n"
"v_mul_f32 v198, s62, v198\n"
"v_mfma_f32_16x16x32_bf16 v[204:207], a[116:119], a[20:23], v[204:207]\n"
"v_exp_f32 v197, v197\n"
"v_mfma_f32_16x16x32_bf16 v[204:207], a[120:123], a[24:27], v[204:207]\n"
"v_cvt_pk_bf16_f32 v194, v196, v197\n"
"v_mul_f32 v200, s62, v200\n"
"v_mfma_f32_16x16x32_bf16 v[204:207], a[124:127], a[28:31], v[204:207]\n"
"v_exp_f32 v200, v200\n"
"v_mfma_f32_16x16x32_bf16 v[160:163], a[128:131], v[208:211], v[160:163]\n"
"ds_read_b64_tr_b16 a[128:129], v228 offset:24576\n"
"v_mul_f32 v201, s62, v201\n"
"v_mfma_f32_16x16x32_bf16 v[164:167], a[132:135], v[208:211], v[164:167]\n"
"ds_read_b64_tr_b16 a[130:131], v228 offset:28672\n"
"v_mul_f32 v202, s62, v202\n"
"v_mfma_f32_16x16x32_bf16 v[168:171], a[136:139], v[208:211], v[168:171]\n"
"ds_read_b64_tr_b16 a[132:133], v229 offset:24576\n"
"v_mul_f32 v199, s62, v199\n"
"v_mfma_f32_16x16x32_bf16 v[172:175], a[140:143], v[208:211], v[172:175]\n"
"ds_read_b64_tr_b16 a[134:135], v229 offset:28672\n"
"v_mul_f32 v203, s62, v203\n"
"v_mfma_f32_16x16x32_bf16 v[176:179], a[144:147], v[208:211], v[176:179]\n"
"ds_read_b64_tr_b16 a[136:137], v228 offset:25088\n"
"v_mul_f32 v204, s62, v204\n"
"v_mfma_f32_16x16x32_bf16 v[180:183], a[148:151], v[208:211], v[180:183]\n"
"ds_read_b64_tr_b16 a[138:139], v228 offset:29184\n"
"v_mul_f32 v205, s62, v205\n"
"v_mfma_f32_16x16x32_bf16 v[184:187], a[152:155], v[208:211], v[184:187]\n"
"ds_read_b64_tr_b16 a[140:141], v229 offset:25088\n"
"v_mul_f32 v206, s62, v206\n"
"v_mfma_f32_16x16x32_bf16 v[188:191], a[156:159], v[208:211], v[188:191]\n"
"ds_read_b64_tr_b16 a[142:143], v229 offset:29184\n"
"v_exp_f32 v201, v201\n"
"v_mfma_f32_16x16x32_bf16 a[180:183], a[184:187], v[208:211], a[180:183]\n"
"ds_read_b64_tr_b16 a[144:145], v228 offset:25600\n"
"v_exp_f32 v198, v198\n"
"v_mfma_f32_16x16x32_bf16 v[208:211], a[96:99], a[32:35], 0\n"
"ds_read_b64_tr_b16 a[146:147], v228 offset:29696\n"
"v_cvt_pk_bf16_f32 v200, v200, v201\n"
"v_mfma_f32_16x16x32_bf16 v[208:211], a[100:103], a[36:39], v[208:211]\n"
"ds_read_b64_tr_b16 a[148:149], v229 offset:25600\n"
"v_mul_f32 v207, s62, v207\n"
"v_mfma_f32_16x16x32_bf16 v[208:211], a[104:107], a[40:43], v[208:211]\n"
"ds_read_b64_tr_b16 a[150:151], v229 offset:29696\n"
"v_exp_f32 v202, v202\n"
"v_mfma_f32_16x16x32_bf16 v[208:211], a[108:111], a[44:47], v[208:211]\n"
"ds_read_b64_tr_b16 a[152:153], v228 offset:26112\n"
"v_exp_f32 v199, v199\n"
"v_mfma_f32_16x16x32_bf16 v[212:215], a[112:115], a[32:35], 0\n"
"ds_read_b64_tr_b16 a[154:155], v228 offset:30208\n"
"v_cvt_pk_bf16_f32 v195, v198, v199\n"
"v_mfma_f32_16x16x32_bf16 v[212:215], a[116:119], a[36:39], v[212:215]\n"
"ds_read_b64_tr_b16 a[156:157], v229 offset:26112\n"
"v_exp_f32 v203, v203\n"
"v_mfma_f32_16x16x32_bf16 v[212:215], a[120:123], a[40:43], v[212:215]\n"
"ds_read_b64_tr_b16 a[158:159], v229 offset:30208\n"
"v_cvt_pk_bf16_f32 v201, v202, v203\n"
"v_mfma_f32_16x16x32_bf16 v[212:215], a[124:127], a[44:47], v[212:215]\n"
"v_mul_f32 v208, s62, v208\n"
"s_waitcnt lgkmcnt(5)\n"
"v_mfma_f32_16x16x32_bf16 v[0:3], a[128:131], v[192:195], v[0:3]\n"
"v_exp_f32 v204, v204\n"
"v_mfma_f32_16x16x32_bf16 v[4:7], a[132:135], v[192:195], v[4:7]\n"
"v_exp_f32 v208, v208\n"
"v_mfma_f32_16x16x32_bf16 v[8:11], a[136:139], v[192:195], v[8:11]\n"
"v_exp_f32 v205, v205\n"
"v_mfma_f32_16x16x32_bf16 v[12:15], a[140:143], v[192:195], v[12:15]\n"
"v_mul_f32 v209, s62, v209\n"
"v_cvt_pk_bf16_f32 v202, v204, v205\n"
"v_mfma_f32_16x16x32_bf16 v[16:19], a[144:147], v[192:195], v[16:19]\n"
"v_mul_f32 v210, s62, v210\n"
"s_waitcnt lgkmcnt(0)\n"
"v_mfma_f32_16x16x32_bf16 v[20:23], a[148:151], v[192:195], v[20:23]\n"
"v_exp_f32 v209, v209\n"
"v_mfma_f32_16x16x32_bf16 v[24:27], a[152:155], v[192:195], v[24:27]\n"
"v_exp_f32 v206, v206\n"
"v_mfma_f32_16x16x32_bf16 v[28:31], a[156:159], v[192:195], v[28:31]\n"
"v_cvt_pk_bf16_f32 v208, v208, v209\n"
"v_mul_f32 v211, s62, v211\n"
"v_mfma_f32_16x16x32_bf16 a[160:163], a[184:187], v[192:195], a[160:163]\n"
"v_exp_f32 v210, v210\n"
"v_mfma_f32_16x16x32_bf16 v[192:195], a[96:99], a[48:51], 0\n"
"v_exp_f32 v207, v207\n"
"v_mfma_f32_16x16x32_bf16 v[192:195], a[100:103], a[52:55], v[192:195]\n"
"v_cvt_pk_bf16_f32 v203, v206, v207\n"
"v_mul_f32 v212, s62, v212\n"
"v_mfma_f32_16x16x32_bf16 v[192:195], a[104:107], a[56:59], v[192:195]\n"
"v_exp_f32 v211, v211\n"
"v_mfma_f32_16x16x32_bf16 v[192:195], a[108:111], a[60:63], v[192:195]\n"
"v_cvt_pk_bf16_f32 v209, v210, v211\n"
"v_mul_f32 v213, s62, v213\n"
"v_mfma_f32_16x16x32_bf16 v[196:199], a[112:115], a[48:51], 0\n"
"v_exp_f32 v212, v212\n"
"v_mfma_f32_16x16x32_bf16 v[196:199], a[116:119], a[52:55], v[196:199]\n"
"v_exp_f32 v213, v213\n"
"v_mfma_f32_16x16x32_bf16 v[196:199], a[120:123], a[56:59], v[196:199]\n"
"v_mul_f32 v214, s62, v214\n"
"v_mul_f32 v192, s62, v192\n"
"v_mfma_f32_16x16x32_bf16 v[196:199], a[124:127], a[60:63], v[196:199]\n"
"v_exp_f32 v192, v192\n"
"v_mfma_f32_16x16x32_bf16 v[32:35], a[128:131], v[200:203], v[32:35]\n"
"v_mul_f32 v193, s62, v193\n"
"v_cvt_pk_bf16_f32 v210, v212, v213\n"
"v_mfma_f32_16x16x32_bf16 v[36:39], a[132:135], v[200:203], v[36:39]\n"
"v_exp_f32 v193, v193\n"
"v_mfma_f32_16x16x32_bf16 v[40:43], a[136:139], v[200:203], v[40:43]\n"
"v_exp_f32 v214, v214\n"
"v_mfma_f32_16x16x32_bf16 v[44:47], a[140:143], v[200:203], v[44:47]\n"
"v_mul_f32 v194, s62, v194\n"
"v_cvt_pk_bf16_f32 v192, v192, v193\n"
"v_mfma_f32_16x16x32_bf16 v[48:51], a[144:147], v[200:203], v[48:51]\n"
"s_waitcnt vmcnt(16)\n"
"s_barrier\n"
"v_mfma_f32_16x16x32_bf16 v[52:55], a[148:151], v[200:203], v[52:55]\n"
"v_mul_f32 v215, s62, v215\n"
"v_mul_f32 v195, s62, v195\n"
"v_mfma_f32_16x16x32_bf16 v[56:59], a[152:155], v[200:203], v[56:59]\n"
"s_add_u32 m0, s56, 24576\n"
"s_add_u32 s72, s57, 90112\n"
"v_mfma_f32_16x16x32_bf16 v[60:63], a[156:159], v[200:203], v[60:63]\n"
"buffer_load_dwordx4 v226, s[40:43], s72 offen nt lds\n"
"v_mul_f32 v196, s62, v196\n"
"v_mfma_f32_16x16x32_bf16 a[164:167], a[184:187], v[200:203], a[164:167]\n"
"buffer_load_dwordx4 v226, s[40:43], s72 offen offset:2048 nt lds\n"
"v_mul_f32 v197, s62, v197\n"
"v_mfma_f32_16x16x32_bf16 v[200:203], a[96:99], a[64:67], 0\n"
"v_mul_f32 v198, s62, v198\n"
"s_add_u32 m0, s56, 57344\n"
"v_mfma_f32_16x16x32_bf16 v[200:203], a[100:103], a[68:71], v[200:203]\n"
"buffer_load_dwordx4 v226, s[44:47], s72 offen nt lds\n"
"v_mul_f32 v199, s62, v199\n"
"v_mfma_f32_16x16x32_bf16 v[200:203], a[104:107], a[72:75], v[200:203]\n"
"buffer_load_dwordx4 v226, s[44:47], s72 offen offset:2048 nt lds\n"
"v_exp_f32 v194, v194\n"
"v_mfma_f32_16x16x32_bf16 v[200:203], a[108:111], a[76:79], v[200:203]\n"
"v_exp_f32 v215, v215\n"
"s_add_u32 s60, s59, 106496\n"
"s_add_u32 m0, s58, 8192\n"
"v_mfma_f32_16x16x32_bf16 v[204:207], a[112:115], a[64:67], 0\n"
"buffer_load_dwordx4 v226, s[36:39], s60 offen nt lds\n"
"v_cvt_pk_bf16_f32 v211, v214, v215\n"
"v_mfma_f32_16x16x32_bf16 v[204:207], a[116:119], a[68:71], v[204:207]\n"
"buffer_load_dwordx4 v226, s[36:39], s60 offen offset:2048 nt lds\n"
"v_exp_f32 v195, v195\n"
"v_mfma_f32_16x16x32_bf16 v[204:207], a[120:123], a[72:75], v[204:207]\n"
"s_add_u32 s60, s59, 106624\n"
"s_add_u32 m0, s58, 9216\n"
"v_mfma_f32_16x16x32_bf16 v[204:207], a[124:127], a[76:79], v[204:207]\n"
"buffer_load_dwordx4 v226, s[36:39], s60 offen nt lds\n"
"v_cvt_pk_bf16_f32 v193, v194, v195\n"
"v_mfma_f32_16x16x32_bf16 v[64:67], a[128:131], v[208:211], v[64:67]\n"
"buffer_load_dwordx4 v226, s[36:39], s60 offen offset:2048 nt lds\n"
"v_mul_f32 v200, s62, v200\n"
"v_mfma_f32_16x16x32_bf16 v[68:71], a[132:135], v[208:211], v[68:71]\n"
"v_exp_f32 v196, v196\n"
"v_mfma_f32_16x16x32_bf16 v[72:75], a[136:139], v[208:211], v[72:75]\n"
"v_exp_f32 v200, v200\n"
"v_mfma_f32_16x16x32_bf16 v[76:79], a[140:143], v[208:211], v[76:79]\n"
"v_exp_f32 v197, v197\n"
"v_mfma_f32_16x16x32_bf16 v[80:83], a[144:147], v[208:211], v[80:83]\n"
"v_mul_f32 v201, s62, v201\n"
"v_cvt_pk_bf16_f32 v194, v196, v197\n"
"v_mfma_f32_16x16x32_bf16 v[84:87], a[148:151], v[208:211], v[84:87]\n"
"v_exp_f32 v201, v201\n"
"v_mfma_f32_16x16x32_bf16 v[88:91], a[152:155], v[208:211], v[88:91]\n"
"v_exp_f32 v198, v198\n"
"v_mfma_f32_16x16x32_bf16 v[92:95], a[156:159], v[208:211], v[92:95]\n"
"v_mul_f32 v202, s62, v202\n"
"v_cvt_pk_bf16_f32 v200, v200, v201\n"
"v_mfma_f32_16x16x32_bf16 a[168:171], a[184:187], v[208:211], a[168:171]\n"
"v_exp_f32 v202, v202\n"
"v_mfma_f32_16x16x32_bf16 v[208:211], a[96:99], a[80:83], 0\n"
"ds_read_b128 a[96:99], v224 offset:0\n"
"v_mul_f32 v203, s62, v203\n"
"v_mfma_f32_16x16x32_bf16 v[208:211], a[100:103], a[84:87], v[208:211]\n"
"v_exp_f32 v199, v199\n"
"v_mfma_f32_16x16x32_bf16 v[208:211], a[104:107], a[88:91], v[208:211]\n"
"ds_read_b128 a[100:103], v224 offset:512\n"
"v_cvt_pk_bf16_f32 v195, v198, v199\n"
"v_mfma_f32_16x16x32_bf16 v[208:211], a[108:111], a[92:95], v[208:211]\n"
"v_exp_f32 v203, v203\n"
"v_mfma_f32_16x16x32_bf16 v[212:215], a[112:115], a[80:83], 0\n"
"ds_read_b128 a[104:107], v224 offset:1024\n"
"v_mul_f32 v204, s62, v204\n"
"v_mfma_f32_16x16x32_bf16 v[212:215], a[116:119], a[84:87], v[212:215]\n"
"v_cvt_pk_bf16_f32 v201, v202, v203\n"
"v_mul_f32 v205, s62, v205\n"
"v_mfma_f32_16x16x32_bf16 v[212:215], a[120:123], a[88:91], v[212:215]\n"
"ds_read_b128 a[108:111], v224 offset:1536\n"
"v_mul_f32 v208, s62, v208\n"
"v_mfma_f32_16x16x32_bf16 v[212:215], a[124:127], a[92:95], v[212:215]\n"
"v_exp_f32 v204, v204\n"
"v_mfma_f32_16x16x32_bf16 v[96:99], a[128:131], v[192:195], v[96:99]\n"
"ds_read_b128 a[112:115], v224 offset:4096\n"
"v_mul_f32 v209, s62, v209\n"
"v_mfma_f32_16x16x32_bf16 v[100:103], a[132:135], v[192:195], v[100:103]\n"
"v_exp_f32 v208, v208\n"
"v_mfma_f32_16x16x32_bf16 v[104:107], a[136:139], v[192:195], v[104:107]\n"
"ds_read_b128 a[116:119], v224 offset:4608\n"
"v_mul_f32 v206, s62, v206\n"
"v_mfma_f32_16x16x32_bf16 v[108:111], a[140:143], v[192:195], v[108:111]\n"
"v_exp_f32 v205, v205\n"
"v_mfma_f32_16x16x32_bf16 v[112:115], a[144:147], v[192:195], v[112:115]\n"
"ds_read_b128 a[120:123], v224 offset:5120\n"
"v_cvt_pk_bf16_f32 v202, v204, v205\n"
"v_mfma_f32_16x16x32_bf16 v[116:119], a[148:151], v[192:195], v[116:119]\n"
"v_exp_f32 v209, v209\n"
"v_mfma_f32_16x16x32_bf16 v[120:123], a[152:155], v[192:195], v[120:123]\n"
"ds_read_b128 a[124:127], v224 offset:5632\n"
"v_mul_f32 v210, s62, v210\n"
"v_mfma_f32_16x16x32_bf16 v[124:127], a[156:159], v[192:195], v[124:127]\n"
"v_exp_f32 v206, v206\n"
"v_mfma_f32_16x16x32_bf16 a[172:175], a[184:187], v[192:195], a[172:175]\n"
"v_cvt_pk_bf16_f32 v208, v208, v209\n"
"s_waitcnt lgkmcnt(2)\n"
"v_mfma_f32_16x16x32_bf16 v[192:195], a[96:99], a[0:3], 0\n"
"v_mul_f32 v207, s62, v207\n"
"v_mul_f32 v211, s62, v211\n"
"v_mfma_f32_16x16x32_bf16 v[192:195], a[100:103], a[4:7], v[192:195]\n"
"v_exp_f32 v210, v210\n"
"v_mfma_f32_16x16x32_bf16 v[192:195], a[104:107], a[8:11], v[192:195]\n"
"v_exp_f32 v207, v207\n"
"v_mfma_f32_16x16x32_bf16 v[192:195], a[108:111], a[12:15], v[192:195]\n"
"v_cvt_pk_bf16_f32 v203, v206, v207\n"
"v_mul_f32 v212, s62, v212\n"
"v_mfma_f32_16x16x32_bf16 v[196:199], a[112:115], a[0:3], 0\n"
"v_exp_f32 v211, v211\n"
"v_mfma_f32_16x16x32_bf16 v[196:199], a[116:119], a[4:7], v[196:199]\n"
"v_cvt_pk_bf16_f32 v209, v210, v211\n"
"s_waitcnt lgkmcnt(0)\n"
"v_mfma_f32_16x16x32_bf16 v[196:199], a[120:123], a[8:11], v[196:199]\n"
"v_exp_f32 v212, v212\n"
"v_mfma_f32_16x16x32_bf16 v[196:199], a[124:127], a[12:15], v[196:199]\n"
"v_mul_f32 v192, s62, v192\n"
"v_mul_f32 v213, s62, v213\n"
"v_mfma_f32_16x16x32_bf16 v[128:131], a[128:131], v[200:203], v[128:131]\n"
"v_exp_f32 v192, v192\n"
"v_mfma_f32_16x16x32_bf16 v[132:135], a[132:135], v[200:203], v[132:135]\n"
"v_exp_f32 v213, v213\n"
"v_mfma_f32_16x16x32_bf16 v[136:139], a[136:139], v[200:203], v[136:139]\n"
"v_mul_f32 v193, s62, v193\n"
"v_mul_f32 v214, s62, v214\n"
"v_mfma_f32_16x16x32_bf16 v[140:143], a[140:143], v[200:203], v[140:143]\n"
"v_cvt_pk_bf16_f32 v210, v212, v213\n"
"v_mul_f32 v194, s62, v194\n"
"v_mfma_f32_16x16x32_bf16 v[144:147], a[144:147], v[200:203], v[144:147]\n"
"v_exp_f32 v193, v193\n"
"v_mfma_f32_16x16x32_bf16 v[148:151], a[148:151], v[200:203], v[148:151]\n"
"v_exp_f32 v214, v214\n"
"v_mfma_f32_16x16x32_bf16 v[152:155], a[152:155], v[200:203], v[152:155]\n"
"v_cvt_pk_bf16_f32 v192, v192, v193\n"
"v_mul_f32 v215, s62, v215\n"
"v_mfma_f32_16x16x32_bf16 v[156:159], a[156:159], v[200:203], v[156:159]\n"
"v_exp_f32 v194, v194\n"
"v_mfma_f32_16x16x32_bf16 a[176:179], a[184:187], v[200:203], a[176:179]\n"
"v_exp_f32 v215, v215\n"
"v_mfma_f32_16x16x32_bf16 v[200:203], a[96:99], a[16:19], 0\n"
"v_mul_f32 v195, s62, v195\n"
"v_cvt_pk_bf16_f32 v211, v214, v215\n"
"v_mfma_f32_16x16x32_bf16 v[200:203], a[100:103], a[20:23], v[200:203]\n"
"v_exp_f32 v195, v195\n"
"v_mfma_f32_16x16x32_bf16 v[200:203], a[104:107], a[24:27], v[200:203]\n"
"v_mul_f32 v196, s62, v196\n"
"v_cvt_pk_bf16_f32 v193, v194, v195\n"
"v_mfma_f32_16x16x32_bf16 v[200:203], a[108:111], a[28:31], v[200:203]\n"
"v_exp_f32 v196, v196\n"
"v_mfma_f32_16x16x32_bf16 v[204:207], a[112:115], a[16:19], 0\n"
"v_mul_f32 v197, s62, v197\n"
"v_mul_f32 v198, s62, v198\n"
"v_mfma_f32_16x16x32_bf16 v[204:207], a[116:119], a[20:23], v[204:207]\n"
"v_exp_f32 v197, v197\n"
"v_mfma_f32_16x16x32_bf16 v[204:207], a[120:123], a[24:27], v[204:207]\n"
"v_cvt_pk_bf16_f32 v194, v196, v197\n"
"v_mul_f32 v200, s62, v200\n"
"v_mfma_f32_16x16x32_bf16 v[204:207], a[124:127], a[28:31], v[204:207]\n"
"v_exp_f32 v200, v200\n"
"v_mfma_f32_16x16x32_bf16 v[160:163], a[128:131], v[208:211], v[160:163]\n"
"ds_read_b64_tr_b16 a[128:129], v228 offset:0\n"
"v_mul_f32 v201, s62, v201\n"
"v_mfma_f32_16x16x32_bf16 v[164:167], a[132:135], v[208:211], v[164:167]\n"
"ds_read_b64_tr_b16 a[130:131], v228 offset:4096\n"
"v_mul_f32 v202, s62, v202\n"
"v_mfma_f32_16x16x32_bf16 v[168:171], a[136:139], v[208:211], v[168:171]\n"
"ds_read_b64_tr_b16 a[132:133], v229 offset:0\n"
"v_mul_f32 v199, s62, v199\n"
"v_mfma_f32_16x16x32_bf16 v[172:175], a[140:143], v[208:211], v[172:175]\n"
"ds_read_b64_tr_b16 a[134:135], v229 offset:4096\n"
"v_mul_f32 v203, s62, v203\n"
"v_mfma_f32_16x16x32_bf16 v[176:179], a[144:147], v[208:211], v[176:179]\n"
"ds_read_b64_tr_b16 a[136:137], v228 offset:512\n"
"v_mul_f32 v204, s62, v204\n"
"v_mfma_f32_16x16x32_bf16 v[180:183], a[148:151], v[208:211], v[180:183]\n"
"ds_read_b64_tr_b16 a[138:139], v228 offset:4608\n"
"v_mul_f32 v205, s62, v205\n"
"v_mfma_f32_16x16x32_bf16 v[184:187], a[152:155], v[208:211], v[184:187]\n"
"ds_read_b64_tr_b16 a[140:141], v229 offset:512\n"
"v_mul_f32 v206, s62, v206\n"
"v_mfma_f32_16x16x32_bf16 v[188:191], a[156:159], v[208:211], v[188:191]\n"
"ds_read_b64_tr_b16 a[142:143], v229 offset:4608\n"
"v_exp_f32 v201, v201\n"
"v_mfma_f32_16x16x32_bf16 a[180:183], a[184:187], v[208:211], a[180:183]\n"
"ds_read_b64_tr_b16 a[144:145], v228 offset:1024\n"
"v_exp_f32 v198, v198\n"
"v_mfma_f32_16x16x32_bf16 v[208:211], a[96:99], a[32:35], 0\n"
"ds_read_b64_tr_b16 a[146:147], v228 offset:5120\n"
"v_cvt_pk_bf16_f32 v200, v200, v201\n"
"v_mfma_f32_16x16x32_bf16 v[208:211], a[100:103], a[36:39], v[208:211]\n"
"ds_read_b64_tr_b16 a[148:149], v229 offset:1024\n"
"v_mul_f32 v207, s62, v207\n"
"v_mfma_f32_16x16x32_bf16 v[208:211], a[104:107], a[40:43], v[208:211]\n"
"ds_read_b64_tr_b16 a[150:151], v229 offset:5120\n"
"v_exp_f32 v202, v202\n"
"v_mfma_f32_16x16x32_bf16 v[208:211], a[108:111], a[44:47], v[208:211]\n"
"ds_read_b64_tr_b16 a[152:153], v228 offset:1536\n"
"v_exp_f32 v199, v199\n"
"v_mfma_f32_16x16x32_bf16 v[212:215], a[112:115], a[32:35], 0\n"
"ds_read_b64_tr_b16 a[154:155], v228 offset:5632\n"
"v_cvt_pk_bf16_f32 v195, v198, v199\n"
"v_mfma_f32_16x16x32_bf16 v[212:215], a[116:119], a[36:39], v[212:215]\n"
"ds_read_b64_tr_b16 a[156:157], v229 offset:1536\n"
"v_exp_f32 v203, v203\n"
"v_mfma_f32_16x16x32_bf16 v[212:215], a[120:123], a[40:43], v[212:215]\n"
"ds_read_b64_tr_b16 a[158:159], v229 offset:5632\n"
"v_cvt_pk_bf16_f32 v201, v202, v203\n"
"v_mfma_f32_16x16x32_bf16 v[212:215], a[124:127], a[44:47], v[212:215]\n"
"v_mul_f32 v208, s62, v208\n"
"s_waitcnt lgkmcnt(5)\n"
"v_mfma_f32_16x16x32_bf16 v[0:3], a[128:131], v[192:195], v[0:3]\n"
"v_exp_f32 v204, v204\n"
"v_mfma_f32_16x16x32_bf16 v[4:7], a[132:135], v[192:195], v[4:7]\n"
"v_exp_f32 v208, v208\n"
"v_mfma_f32_16x16x32_bf16 v[8:11], a[136:139], v[192:195], v[8:11]\n"
"v_exp_f32 v205, v205\n"
"v_mfma_f32_16x16x32_bf16 v[12:15], a[140:143], v[192:195], v[12:15]\n"
"v_mul_f32 v209, s62, v209\n"
"v_cvt_pk_bf16_f32 v202, v204, v205\n"
"v_mfma_f32_16x16x32_bf16 v[16:19], a[144:147], v[192:195], v[16:19]\n"
"v_mul_f32 v210, s62, v210\n"
"s_waitcnt lgkmcnt(0)\n"
"v_mfma_f32_16x16x32_bf16 v[20:23], a[148:151], v[192:195], v[20:23]\n"
"v_exp_f32 v209, v209\n"
"v_mfma_f32_16x16x32_bf16 v[24:27], a[152:155], v[192:195], v[24:27]\n"
"v_exp_f32 v206, v206\n"
"v_mfma_f32_16x16x32_bf16 v[28:31], a[156:159], v[192:195], v[28:31]\n"
"v_cvt_pk_bf16_f32 v208, v208, v209\n"
"v_mul_f32 v211, s62, v211\n"
"v_mfma_f32_16x16x32_bf16 a[160:163], a[184:187], v[192:195], a[160:163]\n"
"v_exp_f32 v210, v210\n"
"v_mfma_f32_16x16x32_bf16 v[192:195], a[96:99], a[48:51], 0\n"
"v_exp_f32 v207, v207\n"
"v_mfma_f32_16x16x32_bf16 v[192:195], a[100:103], a[52:55], v[192:195]\n"
"v_cvt_pk_bf16_f32 v203, v206, v207\n"
"v_mul_f32 v212, s62, v212\n"
"v_mfma_f32_16x16x32_bf16 v[192:195], a[104:107], a[56:59], v[192:195]\n"
"v_exp_f32 v211, v211\n"
"v_mfma_f32_16x16x32_bf16 v[192:195], a[108:111], a[60:63], v[192:195]\n"
"v_cvt_pk_bf16_f32 v209, v210, v211\n"
"v_mul_f32 v213, s62, v213\n"
"v_mfma_f32_16x16x32_bf16 v[196:199], a[112:115], a[48:51], 0\n"
"v_exp_f32 v212, v212\n"
"v_mfma_f32_16x16x32_bf16 v[196:199], a[116:119], a[52:55], v[196:199]\n"
"v_exp_f32 v213, v213\n"
"v_mfma_f32_16x16x32_bf16 v[196:199], a[120:123], a[56:59], v[196:199]\n"
"v_mul_f32 v214, s62, v214\n"
"v_mul_f32 v192, s62, v192\n"
"v_mfma_f32_16x16x32_bf16 v[196:199], a[124:127], a[60:63], v[196:199]\n"
"v_exp_f32 v192, v192\n"
"v_mfma_f32_16x16x32_bf16 v[32:35], a[128:131], v[200:203], v[32:35]\n"
"v_mul_f32 v193, s62, v193\n"
"v_cvt_pk_bf16_f32 v210, v212, v213\n"
"v_mfma_f32_16x16x32_bf16 v[36:39], a[132:135], v[200:203], v[36:39]\n"
"v_exp_f32 v193, v193\n"
"v_mfma_f32_16x16x32_bf16 v[40:43], a[136:139], v[200:203], v[40:43]\n"
"v_exp_f32 v214, v214\n"
"v_mfma_f32_16x16x32_bf16 v[44:47], a[140:143], v[200:203], v[44:47]\n"
"v_mul_f32 v194, s62, v194\n"
"v_cvt_pk_bf16_f32 v192, v192, v193\n"
"v_mfma_f32_16x16x32_bf16 v[48:51], a[144:147], v[200:203], v[48:51]\n"
"s_waitcnt vmcnt(20)\n"
"s_barrier\n"
"v_mfma_f32_16x16x32_bf16 v[52:55], a[148:151], v[200:203], v[52:55]\n"
"v_mul_f32 v215, s62, v215\n"
"v_mul_f32 v195, s62, v195\n"
"v_mfma_f32_16x16x32_bf16 v[56:59], a[152:155], v[200:203], v[56:59]\n"
"s_add_u32 m0, s56, 0\n"
"s_add_u32 s72, s57, 98304\n"
"v_mfma_f32_16x16x32_bf16 v[60:63], a[156:159], v[200:203], v[60:63]\n"
"buffer_load_dwordx4 v226, s[40:43], s72 offen nt lds\n"
"v_mul_f32 v196, s62, v196\n"
"v_mfma_f32_16x16x32_bf16 a[164:167], a[184:187], v[200:203], a[164:167]\n"
"buffer_load_dwordx4 v226, s[40:43], s72 offen offset:2048 nt lds\n"
"v_mul_f32 v197, s62, v197\n"
"v_mfma_f32_16x16x32_bf16 v[200:203], a[96:99], a[64:67], 0\n"
"v_mul_f32 v198, s62, v198\n"
"s_add_u32 m0, s56, 32768\n"
"v_mfma_f32_16x16x32_bf16 v[200:203], a[100:103], a[68:71], v[200:203]\n"
"buffer_load_dwordx4 v226, s[44:47], s72 offen nt lds\n"
"v_mul_f32 v199, s62, v199\n"
"v_mfma_f32_16x16x32_bf16 v[200:203], a[104:107], a[72:75], v[200:203]\n"
"buffer_load_dwordx4 v226, s[44:47], s72 offen offset:2048 nt lds\n"
"v_exp_f32 v194, v194\n"
"v_mfma_f32_16x16x32_bf16 v[200:203], a[108:111], a[76:79], v[200:203]\n"
"v_exp_f32 v215, v215\n"
"s_add_u32 s60, s59, 110592\n"
"s_add_u32 m0, s58, 12288\n"
"v_mfma_f32_16x16x32_bf16 v[204:207], a[112:115], a[64:67], 0\n"
"buffer_load_dwordx4 v226, s[36:39], s60 offen nt lds\n"
"v_cvt_pk_bf16_f32 v211, v214, v215\n"
"v_mfma_f32_16x16x32_bf16 v[204:207], a[116:119], a[68:71], v[204:207]\n"
"buffer_load_dwordx4 v226, s[36:39], s60 offen offset:2048 nt lds\n"
"v_exp_f32 v195, v195\n"
"v_mfma_f32_16x16x32_bf16 v[204:207], a[120:123], a[72:75], v[204:207]\n"
"s_add_u32 s60, s59, 110720\n"
"s_add_u32 m0, s58, 13312\n"
"v_mfma_f32_16x16x32_bf16 v[204:207], a[124:127], a[76:79], v[204:207]\n"
"buffer_load_dwordx4 v226, s[36:39], s60 offen nt lds\n"
"v_cvt_pk_bf16_f32 v193, v194, v195\n"
"v_mfma_f32_16x16x32_bf16 v[64:67], a[128:131], v[208:211], v[64:67]\n"
"buffer_load_dwordx4 v226, s[36:39], s60 offen offset:2048 nt lds\n"
"v_mul_f32 v200, s62, v200\n"
"v_mfma_f32_16x16x32_bf16 v[68:71], a[132:135], v[208:211], v[68:71]\n"
"v_exp_f32 v196, v196\n"
"v_mfma_f32_16x16x32_bf16 v[72:75], a[136:139], v[208:211], v[72:75]\n"
"v_exp_f32 v200, v200\n"
"v_mfma_f32_16x16x32_bf16 v[76:79], a[140:143], v[208:211], v[76:79]\n"
"v_exp_f32 v197, v197\n"
"v_mfma_f32_16x16x32_bf16 v[80:83], a[144:147], v[208:211], v[80:83]\n"
"v_mul_f32 v201, s62, v201\n"
"v_cvt_pk_bf16_f32 v194, v196, v197\n"
"v_mfma_f32_16x16x32_bf16 v[84:87], a[148:151], v[208:211], v[84:87]\n"
"v_exp_f32 v201, v201\n"
"v_mfma_f32_16x16x32_bf16 v[88:91], a[152:155], v[208:211], v[88:91]\n"
"v_exp_f32 v198, v198\n"
"v_mfma_f32_16x16x32_bf16 v[92:95], a[156:159], v[208:211], v[92:95]\n"
"v_mul_f32 v202, s62, v202\n"
"v_cvt_pk_bf16_f32 v200, v200, v201\n"
"v_mfma_f32_16x16x32_bf16 a[168:171], a[184:187], v[208:211], a[168:171]\n"
"v_exp_f32 v202, v202\n"
"v_mfma_f32_16x16x32_bf16 v[208:211], a[96:99], a[80:83], 0\n"
"ds_read_b128 a[96:99], v224 offset:8192\n"
"v_mul_f32 v203, s62, v203\n"
"v_mfma_f32_16x16x32_bf16 v[208:211], a[100:103], a[84:87], v[208:211]\n"
"v_exp_f32 v199, v199\n"
"v_mfma_f32_16x16x32_bf16 v[208:211], a[104:107], a[88:91], v[208:211]\n"
"ds_read_b128 a[100:103], v224 offset:8704\n"
"v_cvt_pk_bf16_f32 v195, v198, v199\n"
"v_mfma_f32_16x16x32_bf16 v[208:211], a[108:111], a[92:95], v[208:211]\n"
"v_exp_f32 v203, v203\n"
"v_mfma_f32_16x16x32_bf16 v[212:215], a[112:115], a[80:83], 0\n"
"ds_read_b128 a[104:107], v224 offset:9216\n"
"v_mul_f32 v204, s62, v204\n"
"v_mfma_f32_16x16x32_bf16 v[212:215], a[116:119], a[84:87], v[212:215]\n"
"v_cvt_pk_bf16_f32 v201, v202, v203\n"
"v_mul_f32 v205, s62, v205\n"
"v_mfma_f32_16x16x32_bf16 v[212:215], a[120:123], a[88:91], v[212:215]\n"
"ds_read_b128 a[108:111], v224 offset:9728\n"
"v_mul_f32 v208, s62, v208\n"
"v_mfma_f32_16x16x32_bf16 v[212:215], a[124:127], a[92:95], v[212:215]\n"
"v_exp_f32 v204, v204\n"
"v_mfma_f32_16x16x32_bf16 v[96:99], a[128:131], v[192:195], v[96:99]\n"
"ds_read_b128 a[112:115], v224 offset:12288\n"
"v_mul_f32 v209, s62, v209\n"
"v_mfma_f32_16x16x32_bf16 v[100:103], a[132:135], v[192:195], v[100:103]\n"
"v_exp_f32 v208, v208\n"
"v_mfma_f32_16x16x32_bf16 v[104:107], a[136:139], v[192:195], v[104:107]\n"
"ds_read_b128 a[116:119], v224 offset:12800\n"
"v_mul_f32 v206, s62, v206\n"
"v_mfma_f32_16x16x32_bf16 v[108:111], a[140:143], v[192:195], v[108:111]\n"
"v_exp_f32 v205, v205\n"
"v_mfma_f32_16x16x32_bf16 v[112:115], a[144:147], v[192:195], v[112:115]\n"
"ds_read_b128 a[120:123], v224 offset:13312\n"
"v_cvt_pk_bf16_f32 v202, v204, v205\n"
"v_mfma_f32_16x16x32_bf16 v[116:119], a[148:151], v[192:195], v[116:119]\n"
"v_exp_f32 v209, v209\n"
"v_mfma_f32_16x16x32_bf16 v[120:123], a[152:155], v[192:195], v[120:123]\n"
"ds_read_b128 a[124:127], v224 offset:13824\n"
"v_mul_f32 v210, s62, v210\n"
"v_mfma_f32_16x16x32_bf16 v[124:127], a[156:159], v[192:195], v[124:127]\n"
"v_exp_f32 v206, v206\n"
"v_mfma_f32_16x16x32_bf16 a[172:175], a[184:187], v[192:195], a[172:175]\n"
"v_cvt_pk_bf16_f32 v208, v208, v209\n"
"s_waitcnt lgkmcnt(2)\n"
"v_mfma_f32_16x16x32_bf16 v[192:195], a[96:99], a[0:3], 0\n"
"v_mul_f32 v207, s62, v207\n"
"v_mul_f32 v211, s62, v211\n"
"v_mfma_f32_16x16x32_bf16 v[192:195], a[100:103], a[4:7], v[192:195]\n"
"v_exp_f32 v210, v210\n"
"v_mfma_f32_16x16x32_bf16 v[192:195], a[104:107], a[8:11], v[192:195]\n"
"v_exp_f32 v207, v207\n"
"v_mfma_f32_16x16x32_bf16 v[192:195], a[108:111], a[12:15], v[192:195]\n"
"v_cvt_pk_bf16_f32 v203, v206, v207\n"
"v_mul_f32 v212, s62, v212\n"
"v_mfma_f32_16x16x32_bf16 v[196:199], a[112:115], a[0:3], 0\n"
"v_exp_f32 v211, v211\n"
"v_mfma_f32_16x16x32_bf16 v[196:199], a[116:119], a[4:7], v[196:199]\n"
"v_cvt_pk_bf16_f32 v209, v210, v211\n"
"s_waitcnt lgkmcnt(0)\n"
"v_mfma_f32_16x16x32_bf16 v[196:199], a[120:123], a[8:11], v[196:199]\n"
"v_exp_f32 v212, v212\n"
"v_mfma_f32_16x16x32_bf16 v[196:199], a[124:127], a[12:15], v[196:199]\n"
"v_mul_f32 v192, s62, v192\n"
"v_mul_f32 v213, s62, v213\n"
"v_mfma_f32_16x16x32_bf16 v[128:131], a[128:131], v[200:203], v[128:131]\n"
"v_exp_f32 v192, v192\n"
"v_mfma_f32_16x16x32_bf16 v[132:135], a[132:135], v[200:203], v[132:135]\n"
"v_exp_f32 v213, v213\n"
"v_mfma_f32_16x16x32_bf16 v[136:139], a[136:139], v[200:203], v[136:139]\n"
"v_mul_f32 v193, s62, v193\n"
"v_mul_f32 v214, s62, v214\n"
"v_mfma_f32_16x16x32_bf16 v[140:143], a[140:143], v[200:203], v[140:143]\n"
"v_cvt_pk_bf16_f32 v210, v212, v213\n"
"v_mul_f32 v194, s62, v194\n"
"v_mfma_f32_16x16x32_bf16 v[144:147], a[144:147], v[200:203], v[144:147]\n"
"v_exp_f32 v193, v193\n"
"v_mfma_f32_16x16x32_bf16 v[148:151], a[148:151], v[200:203], v[148:151]\n"
"v_exp_f32 v214, v214\n"
"v_mfma_f32_16x16x32_bf16 v[152:155], a[152:155], v[200:203], v[152:155]\n"
"v_cvt_pk_bf16_f32 v192, v192, v193\n"
"v_mul_f32 v215, s62, v215\n"
"v_mfma_f32_16x16x32_bf16 v[156:159], a[156:159], v[200:203], v[156:159]\n"
"v_exp_f32 v194, v194\n"
"v_mfma_f32_16x16x32_bf16 a[176:179], a[184:187], v[200:203], a[176:179]\n"
"v_exp_f32 v215, v215\n"
"v_mfma_f32_16x16x32_bf16 v[200:203], a[96:99], a[16:19], 0\n"
"v_mul_f32 v195, s62, v195\n"
"v_cvt_pk_bf16_f32 v211, v214, v215\n"
"v_mfma_f32_16x16x32_bf16 v[200:203], a[100:103], a[20:23], v[200:203]\n"
"v_exp_f32 v195, v195\n"
"v_mfma_f32_16x16x32_bf16 v[200:203], a[104:107], a[24:27], v[200:203]\n"
"v_mul_f32 v196, s62, v196\n"
"v_cvt_pk_bf16_f32 v193, v194, v195\n"
"v_mfma_f32_16x16x32_bf16 v[200:203], a[108:111], a[28:31], v[200:203]\n"
"v_exp_f32 v196, v196\n"
"v_mfma_f32_16x16x32_bf16 v[204:207], a[112:115], a[16:19], 0\n"
"v_mul_f32 v197, s62, v197\n"
"v_mul_f32 v198, s62, v198\n"
"v_mfma_f32_16x16x32_bf16 v[204:207], a[116:119], a[20:23], v[204:207]\n"
"v_exp_f32 v197, v197\n"
"v_mfma_f32_16x16x32_bf16 v[204:207], a[120:123], a[24:27], v[204:207]\n"
"v_cvt_pk_bf16_f32 v194, v196, v197\n"
"v_mul_f32 v200, s62, v200\n"
"v_mfma_f32_16x16x32_bf16 v[204:207], a[124:127], a[28:31], v[204:207]\n"
"v_exp_f32 v200, v200\n"
"v_mfma_f32_16x16x32_bf16 v[160:163], a[128:131], v[208:211], v[160:163]\n"
"ds_read_b64_tr_b16 a[128:129], v228 offset:8192\n"
"v_mul_f32 v201, s62, v201\n"
"v_mfma_f32_16x16x32_bf16 v[164:167], a[132:135], v[208:211], v[164:167]\n"
"ds_read_b64_tr_b16 a[130:131], v228 offset:12288\n"
"v_mul_f32 v202, s62, v202\n"
"v_mfma_f32_16x16x32_bf16 v[168:171], a[136:139], v[208:211], v[168:171]\n"
"ds_read_b64_tr_b16 a[132:133], v229 offset:8192\n"
"v_mul_f32 v199, s62, v199\n"
"v_mfma_f32_16x16x32_bf16 v[172:175], a[140:143], v[208:211], v[172:175]\n"
"ds_read_b64_tr_b16 a[134:135], v229 offset:12288\n"
"v_mul_f32 v203, s62, v203\n"
"v_mfma_f32_16x16x32_bf16 v[176:179], a[144:147], v[208:211], v[176:179]\n"
"ds_read_b64_tr_b16 a[136:137], v228 offset:8704\n"
"v_mul_f32 v204, s62, v204\n"
"v_mfma_f32_16x16x32_bf16 v[180:183], a[148:151], v[208:211], v[180:183]\n"
"ds_read_b64_tr_b16 a[138:139], v228 offset:12800\n"
"v_mul_f32 v205, s62, v205\n"
"v_mfma_f32_16x16x32_bf16 v[184:187], a[152:155], v[208:211], v[184:187]\n"
"ds_read_b64_tr_b16 a[140:141], v229 offset:8704\n"
"v_mul_f32 v206, s62, v206\n"
"v_mfma_f32_16x16x32_bf16 v[188:191], a[156:159], v[208:211], v[188:191]\n"
"ds_read_b64_tr_b16 a[142:143], v229 offset:12800\n"
"v_exp_f32 v201, v201\n"
"v_mfma_f32_16x16x32_bf16 a[180:183], a[184:187], v[208:211], a[180:183]\n"
"ds_read_b64_tr_b16 a[144:145], v228 offset:9216\n"
"v_exp_f32 v198, v198\n"
"v_mfma_f32_16x16x32_bf16 v[208:211], a[96:99], a[32:35], 0\n"
"ds_read_b64_tr_b16 a[146:147], v228 offset:13312\n"
"v_cvt_pk_bf16_f32 v200, v200, v201\n"
"v_mfma_f32_16x16x32_bf16 v[208:211], a[100:103], a[36:39], v[208:211]\n"
"ds_read_b64_tr_b16 a[148:149], v229 offset:9216\n"
"v_mul_f32 v207, s62, v207\n"
"v_mfma_f32_16x16x32_bf16 v[208:211], a[104:107], a[40:43], v[208:211]\n"
"ds_read_b64_tr_b16 a[150:151], v229 offset:13312\n"
"v_exp_f32 v202, v202\n"
"v_mfma_f32_16x16x32_bf16 v[208:211], a[108:111], a[44:47], v[208:211]\n"
"ds_read_b64_tr_b16 a[152:153], v228 offset:9728\n"
"v_exp_f32 v199, v199\n"
"v_mfma_f32_16x16x32_bf16 v[212:215], a[112:115], a[32:35], 0\n"
"ds_read_b64_tr_b16 a[154:155], v228 offset:13824\n"
"v_cvt_pk_bf16_f32 v195, v198, v199\n"
"v_mfma_f32_16x16x32_bf16 v[212:215], a[116:119], a[36:39], v[212:215]\n"
"ds_read_b64_tr_b16 a[156:157], v229 offset:9728\n"
"v_exp_f32 v203, v203\n"
"v_mfma_f32_16x16x32_bf16 v[212:215], a[120:123], a[40:43], v[212:215]\n"
"ds_read_b64_tr_b16 a[158:159], v229 offset:13824\n"
"v_cvt_pk_bf16_f32 v201, v202, v203\n"
"v_mfma_f32_16x16x32_bf16 v[212:215], a[124:127], a[44:47], v[212:215]\n"
"v_mul_f32 v208, s62, v208\n"
"s_waitcnt lgkmcnt(5)\n"
"v_mfma_f32_16x16x32_bf16 v[0:3], a[128:131], v[192:195], v[0:3]\n"
"v_exp_f32 v204, v204\n"
"v_mfma_f32_16x16x32_bf16 v[4:7], a[132:135], v[192:195], v[4:7]\n"
"v_exp_f32 v208, v208\n"
"v_mfma_f32_16x16x32_bf16 v[8:11], a[136:139], v[192:195], v[8:11]\n"
"v_exp_f32 v205, v205\n"
"v_mfma_f32_16x16x32_bf16 v[12:15], a[140:143], v[192:195], v[12:15]\n"
"v_mul_f32 v209, s62, v209\n"
"v_cvt_pk_bf16_f32 v202, v204, v205\n"
"v_mfma_f32_16x16x32_bf16 v[16:19], a[144:147], v[192:195], v[16:19]\n"
"v_mul_f32 v210, s62, v210\n"
"s_waitcnt lgkmcnt(0)\n"
"v_mfma_f32_16x16x32_bf16 v[20:23], a[148:151], v[192:195], v[20:23]\n"
"v_exp_f32 v209, v209\n"
"v_mfma_f32_16x16x32_bf16 v[24:27], a[152:155], v[192:195], v[24:27]\n"
"v_exp_f32 v206, v206\n"
"v_mfma_f32_16x16x32_bf16 v[28:31], a[156:159], v[192:195], v[28:31]\n"
"v_cvt_pk_bf16_f32 v208, v208, v209\n"
"v_mul_f32 v211, s62, v211\n"
"v_mfma_f32_16x16x32_bf16 a[160:163], a[184:187], v[192:195], a[160:163]\n"
"v_exp_f32 v210, v210\n"
"v_mfma_f32_16x16x32_bf16 v[192:195], a[96:99], a[48:51], 0\n"
"v_exp_f32 v207, v207\n"
"v_mfma_f32_16x16x32_bf16 v[192:195], a[100:103], a[52:55], v[192:195]\n"
"v_cvt_pk_bf16_f32 v203, v206, v207\n"
"v_mul_f32 v212, s62, v212\n"
"v_mfma_f32_16x16x32_bf16 v[192:195], a[104:107], a[56:59], v[192:195]\n"
"v_exp_f32 v211, v211\n"
"v_mfma_f32_16x16x32_bf16 v[192:195], a[108:111], a[60:63], v[192:195]\n"
"v_cvt_pk_bf16_f32 v209, v210, v211\n"
"v_mul_f32 v213, s62, v213\n"
"v_mfma_f32_16x16x32_bf16 v[196:199], a[112:115], a[48:51], 0\n"
"v_exp_f32 v212, v212\n"
"v_mfma_f32_16x16x32_bf16 v[196:199], a[116:119], a[52:55], v[196:199]\n"
"v_exp_f32 v213, v213\n"
"v_mfma_f32_16x16x32_bf16 v[196:199], a[120:123], a[56:59], v[196:199]\n"
"v_mul_f32 v214, s62, v214\n"
"v_mul_f32 v192, s62, v192\n"
"v_mfma_f32_16x16x32_bf16 v[196:199], a[124:127], a[60:63], v[196:199]\n"
"v_exp_f32 v192, v192\n"
"v_mfma_f32_16x16x32_bf16 v[32:35], a[128:131], v[200:203], v[32:35]\n"
"v_mul_f32 v193, s62, v193\n"
"v_cvt_pk_bf16_f32 v210, v212, v213\n"
"v_mfma_f32_16x16x32_bf16 v[36:39], a[132:135], v[200:203], v[36:39]\n"
"v_exp_f32 v193, v193\n"
"v_mfma_f32_16x16x32_bf16 v[40:43], a[136:139], v[200:203], v[40:43]\n"
"v_exp_f32 v214, v214\n"
"v_mfma_f32_16x16x32_bf16 v[44:47], a[140:143], v[200:203], v[44:47]\n"
"v_mul_f32 v194, s62, v194\n"
"v_cvt_pk_bf16_f32 v192, v192, v193\n"
"v_mfma_f32_16x16x32_bf16 v[48:51], a[144:147], v[200:203], v[48:51]\n"
"s_waitcnt vmcnt(20)\n"
"s_barrier\n"
"v_mfma_f32_16x16x32_bf16 v[52:55], a[148:151], v[200:203], v[52:55]\n"
"v_mul_f32 v215, s62, v215\n"
"v_mul_f32 v195, s62, v195\n"
"v_mfma_f32_16x16x32_bf16 v[56:59], a[152:155], v[200:203], v[56:59]\n"
"s_add_u32 m0, s56, 8192\n"
"s_add_u32 s72, s57, 106496\n"
"v_mfma_f32_16x16x32_bf16 v[60:63], a[156:159], v[200:203], v[60:63]\n"
"buffer_load_dwordx4 v226, s[40:43], s72 offen nt lds\n"
"v_mul_f32 v196, s62, v196\n"
"v_mfma_f32_16x16x32_bf16 a[164:167], a[184:187], v[200:203], a[164:167]\n"
"buffer_load_dwordx4 v226, s[40:43], s72 offen offset:2048 nt lds\n"
"v_mul_f32 v197, s62, v197\n"
"v_mfma_f32_16x16x32_bf16 v[200:203], a[96:99], a[64:67], 0\n"
"v_mul_f32 v198, s62, v198\n"
"s_add_u32 m0, s56, 40960\n"
"v_mfma_f32_16x16x32_bf16 v[200:203], a[100:103], a[68:71], v[200:203]\n"
"buffer_load_dwordx4 v226, s[44:47], s72 offen nt lds\n"
"v_mul_f32 v199, s62, v199\n"
"v_mfma_f32_16x16x32_bf16 v[200:203], a[104:107], a[72:75], v[200:203]\n"
"buffer_load_dwordx4 v226, s[44:47], s72 offen offset:2048 nt lds\n"
"v_exp_f32 v194, v194\n"
"v_mfma_f32_16x16x32_bf16 v[200:203], a[108:111], a[76:79], v[200:203]\n"
"v_exp_f32 v215, v215\n"
"s_add_u32 s60, s59, 114688\n"
"s_add_u32 m0, s58, 16384\n"
"v_mfma_f32_16x16x32_bf16 v[204:207], a[112:115], a[64:67], 0\n"
"buffer_load_dwordx4 v226, s[36:39], s60 offen nt lds\n"
"v_cvt_pk_bf16_f32 v211, v214, v215\n"
"v_mfma_f32_16x16x32_bf16 v[204:207], a[116:119], a[68:71], v[204:207]\n"
"buffer_load_dwordx4 v226, s[36:39], s60 offen offset:2048 nt lds\n"
"v_exp_f32 v195, v195\n"
"v_mfma_f32_16x16x32_bf16 v[204:207], a[120:123], a[72:75], v[204:207]\n"
"s_add_u32 s60, s59, 114816\n"
"s_add_u32 m0, s58, 17408\n"
"v_mfma_f32_16x16x32_bf16 v[204:207], a[124:127], a[76:79], v[204:207]\n"
"buffer_load_dwordx4 v226, s[36:39], s60 offen nt lds\n"
"v_cvt_pk_bf16_f32 v193, v194, v195\n"
"v_mfma_f32_16x16x32_bf16 v[64:67], a[128:131], v[208:211], v[64:67]\n"
"buffer_load_dwordx4 v226, s[36:39], s60 offen offset:2048 nt lds\n"
"v_mul_f32 v200, s62, v200\n"
"v_mfma_f32_16x16x32_bf16 v[68:71], a[132:135], v[208:211], v[68:71]\n"
"v_exp_f32 v196, v196\n"
"v_mfma_f32_16x16x32_bf16 v[72:75], a[136:139], v[208:211], v[72:75]\n"
"v_exp_f32 v200, v200\n"
"v_mfma_f32_16x16x32_bf16 v[76:79], a[140:143], v[208:211], v[76:79]\n"
"v_exp_f32 v197, v197\n"
"v_mfma_f32_16x16x32_bf16 v[80:83], a[144:147], v[208:211], v[80:83]\n"
"v_mul_f32 v201, s62, v201\n"
"v_cvt_pk_bf16_f32 v194, v196, v197\n"
"v_mfma_f32_16x16x32_bf16 v[84:87], a[148:151], v[208:211], v[84:87]\n"
"v_exp_f32 v201, v201\n"
"v_mfma_f32_16x16x32_bf16 v[88:91], a[152:155], v[208:211], v[88:91]\n"
"v_exp_f32 v198, v198\n"
"v_mfma_f32_16x16x32_bf16 v[92:95], a[156:159], v[208:211], v[92:95]\n"
"v_mul_f32 v202, s62, v202\n"
"v_cvt_pk_bf16_f32 v200, v200, v201\n"
"v_mfma_f32_16x16x32_bf16 a[168:171], a[184:187], v[208:211], a[168:171]\n"
"v_exp_f32 v202, v202\n"
"v_mfma_f32_16x16x32_bf16 v[208:211], a[96:99], a[80:83], 0\n"
"ds_read_b128 a[96:99], v224 offset:16384\n"
"v_mul_f32 v203, s62, v203\n"
"v_mfma_f32_16x16x32_bf16 v[208:211], a[100:103], a[84:87], v[208:211]\n"
"v_exp_f32 v199, v199\n"
"v_mfma_f32_16x16x32_bf16 v[208:211], a[104:107], a[88:91], v[208:211]\n"
"ds_read_b128 a[100:103], v224 offset:16896\n"
"v_cvt_pk_bf16_f32 v195, v198, v199\n"
"v_mfma_f32_16x16x32_bf16 v[208:211], a[108:111], a[92:95], v[208:211]\n"
"v_exp_f32 v203, v203\n"
"v_mfma_f32_16x16x32_bf16 v[212:215], a[112:115], a[80:83], 0\n"
"ds_read_b128 a[104:107], v224 offset:17408\n"
"v_mul_f32 v204, s62, v204\n"
"v_mfma_f32_16x16x32_bf16 v[212:215], a[116:119], a[84:87], v[212:215]\n"
"v_cvt_pk_bf16_f32 v201, v202, v203\n"
"v_mul_f32 v205, s62, v205\n"
"v_mfma_f32_16x16x32_bf16 v[212:215], a[120:123], a[88:91], v[212:215]\n"
"ds_read_b128 a[108:111], v224 offset:17920\n"
"v_mul_f32 v208, s62, v208\n"
"v_mfma_f32_16x16x32_bf16 v[212:215], a[124:127], a[92:95], v[212:215]\n"
"v_exp_f32 v204, v204\n"
"v_mfma_f32_16x16x32_bf16 v[96:99], a[128:131], v[192:195], v[96:99]\n"
"ds_read_b128 a[112:115], v224 offset:20480\n"
"v_mul_f32 v209, s62, v209\n"
"v_mfma_f32_16x16x32_bf16 v[100:103], a[132:135], v[192:195], v[100:103]\n"
"v_exp_f32 v208, v208\n"
"v_mfma_f32_16x16x32_bf16 v[104:107], a[136:139], v[192:195], v[104:107]\n"
"ds_read_b128 a[116:119], v224 offset:20992\n"
"v_mul_f32 v206, s62, v206\n"
"v_mfma_f32_16x16x32_bf16 v[108:111], a[140:143], v[192:195], v[108:111]\n"
"v_exp_f32 v205, v205\n"
"v_mfma_f32_16x16x32_bf16 v[112:115], a[144:147], v[192:195], v[112:115]\n"
"ds_read_b128 a[120:123], v224 offset:21504\n"
"v_cvt_pk_bf16_f32 v202, v204, v205\n"
"v_mfma_f32_16x16x32_bf16 v[116:119], a[148:151], v[192:195], v[116:119]\n"
"v_exp_f32 v209, v209\n"
"v_mfma_f32_16x16x32_bf16 v[120:123], a[152:155], v[192:195], v[120:123]\n"
"ds_read_b128 a[124:127], v224 offset:22016\n"
"v_mul_f32 v210, s62, v210\n"
"v_mfma_f32_16x16x32_bf16 v[124:127], a[156:159], v[192:195], v[124:127]\n"
"v_exp_f32 v206, v206\n"
"v_mfma_f32_16x16x32_bf16 a[172:175], a[184:187], v[192:195], a[172:175]\n"
"v_cvt_pk_bf16_f32 v208, v208, v209\n"
"s_waitcnt lgkmcnt(2)\n"
"v_mfma_f32_16x16x32_bf16 v[192:195], a[96:99], a[0:3], 0\n"
"v_mul_f32 v207, s62, v207\n"
"v_mul_f32 v211, s62, v211\n"
"v_mfma_f32_16x16x32_bf16 v[192:195], a[100:103], a[4:7], v[192:195]\n"
"v_exp_f32 v210, v210\n"
"v_mfma_f32_16x16x32_bf16 v[192:195], a[104:107], a[8:11], v[192:195]\n"
"v_exp_f32 v207, v207\n"
"v_mfma_f32_16x16x32_bf16 v[192:195], a[108:111], a[12:15], v[192:195]\n"
"v_cvt_pk_bf16_f32 v203, v206, v207\n"
"v_mul_f32 v212, s62, v212\n"
"v_mfma_f32_16x16x32_bf16 v[196:199], a[112:115], a[0:3], 0\n"
"v_exp_f32 v211, v211\n"
"v_mfma_f32_16x16x32_bf16 v[196:199], a[116:119], a[4:7], v[196:199]\n"
"v_cvt_pk_bf16_f32 v209, v210, v211\n"
"s_waitcnt lgkmcnt(0)\n"
"v_mfma_f32_16x16x32_bf16 v[196:199], a[120:123], a[8:11], v[196:199]\n"
"v_exp_f32 v212, v212\n"
"v_mfma_f32_16x16x32_bf16 v[196:199], a[124:127], a[12:15], v[196:199]\n"
"v_mul_f32 v192, s62, v192\n"
"v_mul_f32 v213, s62, v213\n"
"v_mfma_f32_16x16x32_bf16 v[128:131], a[128:131], v[200:203], v[128:131]\n"
"v_exp_f32 v192, v192\n"
"v_mfma_f32_16x16x32_bf16 v[132:135], a[132:135], v[200:203], v[132:135]\n"
"v_exp_f32 v213, v213\n"
"v_mfma_f32_16x16x32_bf16 v[136:139], a[136:139], v[200:203], v[136:139]\n"
"v_mul_f32 v193, s62, v193\n"
"v_mul_f32 v214, s62, v214\n"
"v_mfma_f32_16x16x32_bf16 v[140:143], a[140:143], v[200:203], v[140:143]\n"
"v_cvt_pk_bf16_f32 v210, v212, v213\n"
"v_mul_f32 v194, s62, v194\n"
"v_mfma_f32_16x16x32_bf16 v[144:147], a[144:147], v[200:203], v[144:147]\n"
"v_exp_f32 v193, v193\n"
"v_mfma_f32_16x16x32_bf16 v[148:151], a[148:151], v[200:203], v[148:151]\n"
"v_exp_f32 v214, v214\n"
"v_mfma_f32_16x16x32_bf16 v[152:155], a[152:155], v[200:203], v[152:155]\n"
"v_cvt_pk_bf16_f32 v192, v192, v193\n"
"v_mul_f32 v215, s62, v215\n"
"v_mfma_f32_16x16x32_bf16 v[156:159], a[156:159], v[200:203], v[156:159]\n"
"v_exp_f32 v194, v194\n"
"v_mfma_f32_16x16x32_bf16 a[176:179], a[184:187], v[200:203], a[176:179]\n"
"v_exp_f32 v215, v215\n"
"v_mfma_f32_16x16x32_bf16 v[200:203], a[96:99], a[16:19], 0\n"
"v_mul_f32 v195, s62, v195\n"
"v_cvt_pk_bf16_f32 v211, v214, v215\n"
"v_mfma_f32_16x16x32_bf16 v[200:203], a[100:103], a[20:23], v[200:203]\n"
"v_exp_f32 v195, v195\n"
"v_mfma_f32_16x16x32_bf16 v[200:203], a[104:107], a[24:27], v[200:203]\n"
"v_mul_f32 v196, s62, v196\n"
"v_cvt_pk_bf16_f32 v193, v194, v195\n"
"v_mfma_f32_16x16x32_bf16 v[200:203], a[108:111], a[28:31], v[200:203]\n"
"v_exp_f32 v196, v196\n"
"v_mfma_f32_16x16x32_bf16 v[204:207], a[112:115], a[16:19], 0\n"
"v_mul_f32 v197, s62, v197\n"
"v_mul_f32 v198, s62, v198\n"
"v_mfma_f32_16x16x32_bf16 v[204:207], a[116:119], a[20:23], v[204:207]\n"
"v_exp_f32 v197, v197\n"
"v_mfma_f32_16x16x32_bf16 v[204:207], a[120:123], a[24:27], v[204:207]\n"
"v_cvt_pk_bf16_f32 v194, v196, v197\n"
"v_mul_f32 v200, s62, v200\n"
"v_mfma_f32_16x16x32_bf16 v[204:207], a[124:127], a[28:31], v[204:207]\n"
"v_exp_f32 v200, v200\n"
"v_mfma_f32_16x16x32_bf16 v[160:163], a[128:131], v[208:211], v[160:163]\n"
"ds_read_b64_tr_b16 a[128:129], v228 offset:16384\n"
"v_mul_f32 v201, s62, v201\n"
"v_mfma_f32_16x16x32_bf16 v[164:167], a[132:135], v[208:211], v[164:167]\n"
"ds_read_b64_tr_b16 a[130:131], v228 offset:20480\n"
"v_mul_f32 v202, s62, v202\n"
"v_mfma_f32_16x16x32_bf16 v[168:171], a[136:139], v[208:211], v[168:171]\n"
"ds_read_b64_tr_b16 a[132:133], v229 offset:16384\n"
"v_mul_f32 v199, s62, v199\n"
"v_mfma_f32_16x16x32_bf16 v[172:175], a[140:143], v[208:211], v[172:175]\n"
"ds_read_b64_tr_b16 a[134:135], v229 offset:20480\n"
"v_mul_f32 v203, s62, v203\n"
"v_mfma_f32_16x16x32_bf16 v[176:179], a[144:147], v[208:211], v[176:179]\n"
"ds_read_b64_tr_b16 a[136:137], v228 offset:16896\n"
"v_mul_f32 v204, s62, v204\n"
"v_mfma_f32_16x16x32_bf16 v[180:183], a[148:151], v[208:211], v[180:183]\n"
"ds_read_b64_tr_b16 a[138:139], v228 offset:20992\n"
"v_mul_f32 v205, s62, v205\n"
"v_mfma_f32_16x16x32_bf16 v[184:187], a[152:155], v[208:211], v[184:187]\n"
"ds_read_b64_tr_b16 a[140:141], v229 offset:16896\n"
"v_mul_f32 v206, s62, v206\n"
"v_mfma_f32_16x16x32_bf16 v[188:191], a[156:159], v[208:211], v[188:191]\n"
"ds_read_b64_tr_b16 a[142:143], v229 offset:20992\n"
"v_exp_f32 v201, v201\n"
"v_mfma_f32_16x16x32_bf16 a[180:183], a[184:187], v[208:211], a[180:183]\n"
"ds_read_b64_tr_b16 a[144:145], v228 offset:17408\n"
"v_exp_f32 v198, v198\n"
"v_mfma_f32_16x16x32_bf16 v[208:211], a[96:99], a[32:35], 0\n"
"ds_read_b64_tr_b16 a[146:147], v228 offset:21504\n"
"v_cvt_pk_bf16_f32 v200, v200, v201\n"
"v_mfma_f32_16x16x32_bf16 v[208:211], a[100:103], a[36:39], v[208:211]\n"
"ds_read_b64_tr_b16 a[148:149], v229 offset:17408\n"
"v_mul_f32 v207, s62, v207\n"
"v_mfma_f32_16x16x32_bf16 v[208:211], a[104:107], a[40:43], v[208:211]\n"
"ds_read_b64_tr_b16 a[150:151], v229 offset:21504\n"
"v_exp_f32 v202, v202\n"
"v_mfma_f32_16x16x32_bf16 v[208:211], a[108:111], a[44:47], v[208:211]\n"
"ds_read_b64_tr_b16 a[152:153], v228 offset:17920\n"
"v_exp_f32 v199, v199\n"
"v_mfma_f32_16x16x32_bf16 v[212:215], a[112:115], a[32:35], 0\n"
"ds_read_b64_tr_b16 a[154:155], v228 offset:22016\n"
"v_cvt_pk_bf16_f32 v195, v198, v199\n"
"v_mfma_f32_16x16x32_bf16 v[212:215], a[116:119], a[36:39], v[212:215]\n"
"ds_read_b64_tr_b16 a[156:157], v229 offset:17920\n"
"v_exp_f32 v203, v203\n"
"v_mfma_f32_16x16x32_bf16 v[212:215], a[120:123], a[40:43], v[212:215]\n"
"ds_read_b64_tr_b16 a[158:159], v229 offset:22016\n"
"v_cvt_pk_bf16_f32 v201, v202, v203\n"
"v_mfma_f32_16x16x32_bf16 v[212:215], a[124:127], a[44:47], v[212:215]\n"
"v_mul_f32 v208, s62, v208\n"
"s_waitcnt lgkmcnt(5)\n"
"v_mfma_f32_16x16x32_bf16 v[0:3], a[128:131], v[192:195], v[0:3]\n"
"v_exp_f32 v204, v204\n"
"v_mfma_f32_16x16x32_bf16 v[4:7], a[132:135], v[192:195], v[4:7]\n"
"v_exp_f32 v208, v208\n"
"v_mfma_f32_16x16x32_bf16 v[8:11], a[136:139], v[192:195], v[8:11]\n"
"v_exp_f32 v205, v205\n"
"v_mfma_f32_16x16x32_bf16 v[12:15], a[140:143], v[192:195], v[12:15]\n"
"v_mul_f32 v209, s62, v209\n"
"v_cvt_pk_bf16_f32 v202, v204, v205\n"
"v_mfma_f32_16x16x32_bf16 v[16:19], a[144:147], v[192:195], v[16:19]\n"
"v_mul_f32 v210, s62, v210\n"
"s_waitcnt lgkmcnt(0)\n"
"v_mfma_f32_16x16x32_bf16 v[20:23], a[148:151], v[192:195], v[20:23]\n"
"v_exp_f32 v209, v209\n"
"v_mfma_f32_16x16x32_bf16 v[24:27], a[152:155], v[192:195], v[24:27]\n"
"v_exp_f32 v206, v206\n"
"v_mfma_f32_16x16x32_bf16 v[28:31], a[156:159], v[192:195], v[28:31]\n"
"v_cvt_pk_bf16_f32 v208, v208, v209\n"
"v_mul_f32 v211, s62, v211\n"
"v_mfma_f32_16x16x32_bf16 a[160:163], a[184:187], v[192:195], a[160:163]\n"
"v_exp_f32 v210, v210\n"
"v_mfma_f32_16x16x32_bf16 v[192:195], a[96:99], a[48:51], 0\n"
"v_exp_f32 v207, v207\n"
"v_mfma_f32_16x16x32_bf16 v[192:195], a[100:103], a[52:55], v[192:195]\n"
"v_cvt_pk_bf16_f32 v203, v206, v207\n"
"v_mul_f32 v212, s62, v212\n"
"v_mfma_f32_16x16x32_bf16 v[192:195], a[104:107], a[56:59], v[192:195]\n"
"v_exp_f32 v211, v211\n"
"v_mfma_f32_16x16x32_bf16 v[192:195], a[108:111], a[60:63], v[192:195]\n"
"v_cvt_pk_bf16_f32 v209, v210, v211\n"
"v_mul_f32 v213, s62, v213\n"
"v_mfma_f32_16x16x32_bf16 v[196:199], a[112:115], a[48:51], 0\n"
"v_exp_f32 v212, v212\n"
"v_mfma_f32_16x16x32_bf16 v[196:199], a[116:119], a[52:55], v[196:199]\n"
"v_exp_f32 v213, v213\n"
"v_mfma_f32_16x16x32_bf16 v[196:199], a[120:123], a[56:59], v[196:199]\n"
"v_mul_f32 v214, s62, v214\n"
"v_mul_f32 v192, s62, v192\n"
"v_mfma_f32_16x16x32_bf16 v[196:199], a[124:127], a[60:63], v[196:199]\n"
"v_exp_f32 v192, v192\n"
"v_mfma_f32_16x16x32_bf16 v[32:35], a[128:131], v[200:203], v[32:35]\n"
"v_mul_f32 v193, s62, v193\n"
"v_cvt_pk_bf16_f32 v210, v212, v213\n"
"v_mfma_f32_16x16x32_bf16 v[36:39], a[132:135], v[200:203], v[36:39]\n"
"v_exp_f32 v193, v193\n"
"v_mfma_f32_16x16x32_bf16 v[40:43], a[136:139], v[200:203], v[40:43]\n"
"v_exp_f32 v214, v214\n"
"v_mfma_f32_16x16x32_bf16 v[44:47], a[140:143], v[200:203], v[44:47]\n"
"v_mul_f32 v194, s62, v194\n"
"v_cvt_pk_bf16_f32 v192, v192, v193\n"
"v_mfma_f32_16x16x32_bf16 v[48:51], a[144:147], v[200:203], v[48:51]\n"
"s_waitcnt vmcnt(20)\n"
"s_barrier\n"
"v_mfma_f32_16x16x32_bf16 v[52:55], a[148:151], v[200:203], v[52:55]\n"
"v_mul_f32 v215, s62, v215\n"
"v_mul_f32 v195, s62, v195\n"
"v_mfma_f32_16x16x32_bf16 v[56:59], a[152:155], v[200:203], v[56:59]\n"
"s_add_u32 m0, s56, 16384\n"
"s_add_u32 s72, s57, 114688\n"
"v_mfma_f32_16x16x32_bf16 v[60:63], a[156:159], v[200:203], v[60:63]\n"
"buffer_load_dwordx4 v226, s[40:43], s72 offen nt lds\n"
"v_mul_f32 v196, s62, v196\n"
"v_mfma_f32_16x16x32_bf16 a[164:167], a[184:187], v[200:203], a[164:167]\n"
"buffer_load_dwordx4 v226, s[40:43], s72 offen offset:2048 nt lds\n"
"v_mul_f32 v197, s62, v197\n"
"v_mfma_f32_16x16x32_bf16 v[200:203], a[96:99], a[64:67], 0\n"
"v_mul_f32 v198, s62, v198\n"
"s_add_u32 m0, s56, 49152\n"
"v_mfma_f32_16x16x32_bf16 v[200:203], a[100:103], a[68:71], v[200:203]\n"
"buffer_load_dwordx4 v226, s[44:47], s72 offen nt lds\n"
"v_mul_f32 v199, s62, v199\n"
"v_mfma_f32_16x16x32_bf16 v[200:203], a[104:107], a[72:75], v[200:203]\n"
"buffer_load_dwordx4 v226, s[44:47], s72 offen offset:2048 nt lds\n"
"v_exp_f32 v194, v194\n"
"v_mfma_f32_16x16x32_bf16 v[200:203], a[108:111], a[76:79], v[200:203]\n"
"v_exp_f32 v215, v215\n"
"s_add_u32 s60, s59, 118784\n"
"s_add_u32 m0, s58, 20480\n"
"v_mfma_f32_16x16x32_bf16 v[204:207], a[112:115], a[64:67], 0\n"
"buffer_load_dwordx4 v226, s[36:39], s60 offen nt lds\n"
"v_cvt_pk_bf16_f32 v211, v214, v215\n"
"v_mfma_f32_16x16x32_bf16 v[204:207], a[116:119], a[68:71], v[204:207]\n"
"buffer_load_dwordx4 v226, s[36:39], s60 offen offset:2048 nt lds\n"
"v_exp_f32 v195, v195\n"
"v_mfma_f32_16x16x32_bf16 v[204:207], a[120:123], a[72:75], v[204:207]\n"
"s_add_u32 s60, s59, 118912\n"
"s_add_u32 m0, s58, 21504\n"
"v_mfma_f32_16x16x32_bf16 v[204:207], a[124:127], a[76:79], v[204:207]\n"
"buffer_load_dwordx4 v226, s[36:39], s60 offen nt lds\n"
"v_cvt_pk_bf16_f32 v193, v194, v195\n"
"v_mfma_f32_16x16x32_bf16 v[64:67], a[128:131], v[208:211], v[64:67]\n"
"buffer_load_dwordx4 v226, s[36:39], s60 offen offset:2048 nt lds\n"
"v_mul_f32 v200, s62, v200\n"
"v_mfma_f32_16x16x32_bf16 v[68:71], a[132:135], v[208:211], v[68:71]\n"
"v_exp_f32 v196, v196\n"
"v_mfma_f32_16x16x32_bf16 v[72:75], a[136:139], v[208:211], v[72:75]\n"
"v_exp_f32 v200, v200\n"
"v_mfma_f32_16x16x32_bf16 v[76:79], a[140:143], v[208:211], v[76:79]\n"
"v_exp_f32 v197, v197\n"
"v_mfma_f32_16x16x32_bf16 v[80:83], a[144:147], v[208:211], v[80:83]\n"
"v_mul_f32 v201, s62, v201\n"
"v_cvt_pk_bf16_f32 v194, v196, v197\n"
"v_mfma_f32_16x16x32_bf16 v[84:87], a[148:151], v[208:211], v[84:87]\n"
"v_exp_f32 v201, v201\n"
"v_mfma_f32_16x16x32_bf16 v[88:91], a[152:155], v[208:211], v[88:91]\n"
"v_exp_f32 v198, v198\n"
"v_mfma_f32_16x16x32_bf16 v[92:95], a[156:159], v[208:211], v[92:95]\n"
"v_mul_f32 v202, s62, v202\n"
"v_cvt_pk_bf16_f32 v200, v200, v201\n"
"v_mfma_f32_16x16x32_bf16 a[168:171], a[184:187], v[208:211], a[168:171]\n"
"v_exp_f32 v202, v202\n"
"v_mfma_f32_16x16x32_bf16 v[208:211], a[96:99], a[80:83], 0\n"
"ds_read_b128 a[96:99], v224 offset:24576\n"
"v_mul_f32 v203, s62, v203\n"
"v_mfma_f32_16x16x32_bf16 v[208:211], a[100:103], a[84:87], v[208:211]\n"
"v_exp_f32 v199, v199\n"
"v_mfma_f32_16x16x32_bf16 v[208:211], a[104:107], a[88:91], v[208:211]\n"
"ds_read_b128 a[100:103], v224 offset:25088\n"
"v_cvt_pk_bf16_f32 v195, v198, v199\n"
"v_mfma_f32_16x16x32_bf16 v[208:211], a[108:111], a[92:95], v[208:211]\n"
"v_exp_f32 v203, v203\n"
"v_mfma_f32_16x16x32_bf16 v[212:215], a[112:115], a[80:83], 0\n"
"ds_read_b128 a[104:107], v224 offset:25600\n"
"v_mul_f32 v204, s62, v204\n"
"v_mfma_f32_16x16x32_bf16 v[212:215], a[116:119], a[84:87], v[212:215]\n"
"v_cvt_pk_bf16_f32 v201, v202, v203\n"
"v_mul_f32 v205, s62, v205\n"
"v_mfma_f32_16x16x32_bf16 v[212:215], a[120:123], a[88:91], v[212:215]\n"
"ds_read_b128 a[108:111], v224 offset:26112\n"
"v_mul_f32 v208, s62, v208\n"
"v_mfma_f32_16x16x32_bf16 v[212:215], a[124:127], a[92:95], v[212:215]\n"
"v_exp_f32 v204, v204\n"
"v_mfma_f32_16x16x32_bf16 v[96:99], a[128:131], v[192:195], v[96:99]\n"
"ds_read_b128 a[112:115], v224 offset:28672\n"
"v_mul_f32 v209, s62, v209\n"
"v_mfma_f32_16x16x32_bf16 v[100:103], a[132:135], v[192:195], v[100:103]\n"
"v_exp_f32 v208, v208\n"
"v_mfma_f32_16x16x32_bf16 v[104:107], a[136:139], v[192:195], v[104:107]\n"
"ds_read_b128 a[116:119], v224 offset:29184\n"
"v_mul_f32 v206, s62, v206\n"
"v_mfma_f32_16x16x32_bf16 v[108:111], a[140:143], v[192:195], v[108:111]\n"
"v_exp_f32 v205, v205\n"
"v_mfma_f32_16x16x32_bf16 v[112:115], a[144:147], v[192:195], v[112:115]\n"
"ds_read_b128 a[120:123], v224 offset:29696\n"
"v_cvt_pk_bf16_f32 v202, v204, v205\n"
"v_mfma_f32_16x16x32_bf16 v[116:119], a[148:151], v[192:195], v[116:119]\n"
"v_exp_f32 v209, v209\n"
"v_mfma_f32_16x16x32_bf16 v[120:123], a[152:155], v[192:195], v[120:123]\n"
"ds_read_b128 a[124:127], v224 offset:30208\n"
"v_mul_f32 v210, s62, v210\n"
"v_mfma_f32_16x16x32_bf16 v[124:127], a[156:159], v[192:195], v[124:127]\n"
"v_exp_f32 v206, v206\n"
"v_mfma_f32_16x16x32_bf16 a[172:175], a[184:187], v[192:195], a[172:175]\n"
"v_cvt_pk_bf16_f32 v208, v208, v209\n"
"s_waitcnt lgkmcnt(2)\n"
"v_mfma_f32_16x16x32_bf16 v[192:195], a[96:99], a[0:3], 0\n"
"v_mul_f32 v207, s62, v207\n"
"v_mul_f32 v211, s62, v211\n"
"v_mfma_f32_16x16x32_bf16 v[192:195], a[100:103], a[4:7], v[192:195]\n"
"v_exp_f32 v210, v210\n"
"v_mfma_f32_16x16x32_bf16 v[192:195], a[104:107], a[8:11], v[192:195]\n"
"v_exp_f32 v207, v207\n"
"v_mfma_f32_16x16x32_bf16 v[192:195], a[108:111], a[12:15], v[192:195]\n"
"v_cvt_pk_bf16_f32 v203, v206, v207\n"
"v_mul_f32 v212, s62, v212\n"
"v_mfma_f32_16x16x32_bf16 v[196:199], a[112:115], a[0:3], 0\n"
"v_exp_f32 v211, v211\n"
"v_mfma_f32_16x16x32_bf16 v[196:199], a[116:119], a[4:7], v[196:199]\n"
"v_cvt_pk_bf16_f32 v209, v210, v211\n"
"s_waitcnt lgkmcnt(0)\n"
"v_mfma_f32_16x16x32_bf16 v[196:199], a[120:123], a[8:11], v[196:199]\n"
"v_exp_f32 v212, v212\n"
"v_mfma_f32_16x16x32_bf16 v[196:199], a[124:127], a[12:15], v[196:199]\n"
"v_mul_f32 v192, s62, v192\n"
"v_mul_f32 v213, s62, v213\n"
"v_mfma_f32_16x16x32_bf16 v[128:131], a[128:131], v[200:203], v[128:131]\n"
"v_exp_f32 v192, v192\n"
"v_mfma_f32_16x16x32_bf16 v[132:135], a[132:135], v[200:203], v[132:135]\n"
"ds_read_b128 a[0:3], v225 offset:0\n"
"v_mul_f32 v193, s62, v193\n"
"v_mfma_f32_16x16x32_bf16 v[136:139], a[136:139], v[200:203], v[136:139]\n"
"v_exp_f32 v213, v213\n"
"v_mfma_f32_16x16x32_bf16 v[140:143], a[140:143], v[200:203], v[140:143]\n"
"ds_read_b128 a[4:7], v225 offset:512\n"
"v_mul_f32 v214, s62, v214\n"
"v_mfma_f32_16x16x32_bf16 v[144:147], a[144:147], v[200:203], v[144:147]\n"
"v_cvt_pk_bf16_f32 v210, v212, v213\n"
"v_mul_f32 v194, s62, v194\n"
"v_mfma_f32_16x16x32_bf16 v[148:151], a[148:151], v[200:203], v[148:151]\n"
"ds_read_b128 a[8:11], v225 offset:1024\n"
"v_mul_f32 v215, s62, v215\n"
"v_mfma_f32_16x16x32_bf16 v[152:155], a[152:155], v[200:203], v[152:155]\n"
"v_exp_f32 v193, v193\n"
"v_mfma_f32_16x16x32_bf16 v[156:159], a[156:159], v[200:203], v[156:159]\n"
"ds_read_b128 a[12:15], v225 offset:1536\n"
"v_cvt_pk_bf16_f32 v192, v192, v193\n"
"v_mfma_f32_16x16x32_bf16 a[176:179], a[184:187], v[200:203], a[176:179]\n"
"v_exp_f32 v214, v214\n"
"v_mfma_f32_16x16x32_bf16 v[200:203], a[96:99], a[16:19], 0\n"
"v_exp_f32 v194, v194\n"
"v_mfma_f32_16x16x32_bf16 v[200:203], a[100:103], a[20:23], v[200:203]\n"
"v_exp_f32 v215, v215\n"
"v_mfma_f32_16x16x32_bf16 v[200:203], a[104:107], a[24:27], v[200:203]\n"
"v_mul_f32 v195, s62, v195\n"
"v_cvt_pk_bf16_f32 v211, v214, v215\n"
"v_mfma_f32_16x16x32_bf16 v[200:203], a[108:111], a[28:31], v[200:203]\n"
"v_exp_f32 v195, v195\n"
"v_mfma_f32_16x16x32_bf16 v[204:207], a[112:115], a[16:19], 0\n"
"v_mul_f32 v196, s62, v196\n"
"v_cvt_pk_bf16_f32 v193, v194, v195\n"
"v_mfma_f32_16x16x32_bf16 v[204:207], a[116:119], a[20:23], v[204:207]\n"
"v_exp_f32 v196, v196\n"
"v_mfma_f32_16x16x32_bf16 v[204:207], a[120:123], a[24:27], v[204:207]\n"
"v_mul_f32 v197, s62, v197\n"
"v_mul_f32 v200, s62, v200\n"
"v_mfma_f32_16x16x32_bf16 v[204:207], a[124:127], a[28:31], v[204:207]\n"
"v_exp_f32 v200, v200\n"
"v_mfma_f32_16x16x32_bf16 v[160:163], a[128:131], v[208:211], v[160:163]\n"
"ds_read_b64_tr_b16 a[128:129], v228 offset:24576\n"
"v_mul_f32 v201, s62, v201\n"
"v_mfma_f32_16x16x32_bf16 v[164:167], a[132:135], v[208:211], v[164:167]\n"
"ds_read_b64_tr_b16 a[130:131], v228 offset:28672\n"
"ds_read_b128 a[16:19], v225 offset:4096\n"
"v_mfma_f32_16x16x32_bf16 v[168:171], a[136:139], v[208:211], v[168:171]\n"
"ds_read_b64_tr_b16 a[132:133], v229 offset:24576\n"
"v_mul_f32 v198, s62, v198\n"
"v_mfma_f32_16x16x32_bf16 v[172:175], a[140:143], v[208:211], v[172:175]\n"
"ds_read_b64_tr_b16 a[134:135], v229 offset:28672\n"
"ds_read_b128 a[20:23], v225 offset:4608\n"
"v_mfma_f32_16x16x32_bf16 v[176:179], a[144:147], v[208:211], v[176:179]\n"
"ds_read_b64_tr_b16 a[136:137], v228 offset:25088\n"
"v_mul_f32 v202, s62, v202\n"
"v_mfma_f32_16x16x32_bf16 v[180:183], a[148:151], v[208:211], v[180:183]\n"
"ds_read_b64_tr_b16 a[138:139], v228 offset:29184\n"
"ds_read_b128 a[24:27], v225 offset:5120\n"
"v_exp_f32 v197, v197\n"
"v_mfma_f32_16x16x32_bf16 v[184:187], a[152:155], v[208:211], v[184:187]\n"
"ds_read_b64_tr_b16 a[140:141], v229 offset:25088\n"
"v_cvt_pk_bf16_f32 v194, v196, v197\n"
"v_mfma_f32_16x16x32_bf16 v[188:191], a[156:159], v[208:211], v[188:191]\n"
"ds_read_b64_tr_b16 a[142:143], v229 offset:29184\n"
"ds_read_b128 a[28:31], v225 offset:5632\n"
"v_exp_f32 v201, v201\n"
"v_mfma_f32_16x16x32_bf16 a[180:183], a[184:187], v[208:211], a[180:183]\n"
"ds_read_b64_tr_b16 a[144:145], v228 offset:25600\n"
"v_exp_f32 v198, v198\n"
"v_mfma_f32_16x16x32_bf16 v[208:211], a[96:99], a[32:35], 0\n"
"ds_read_b64_tr_b16 a[146:147], v228 offset:29696\n"
"v_cvt_pk_bf16_f32 v200, v200, v201\n"
"v_mfma_f32_16x16x32_bf16 v[208:211], a[100:103], a[36:39], v[208:211]\n"
"ds_read_b64_tr_b16 a[148:149], v229 offset:25600\n"
"v_mul_f32 v199, s62, v199\n"
"v_mfma_f32_16x16x32_bf16 v[208:211], a[104:107], a[40:43], v[208:211]\n"
"ds_read_b64_tr_b16 a[150:151], v229 offset:29696\n"
"v_exp_f32 v202, v202\n"
"v_mfma_f32_16x16x32_bf16 v[208:211], a[108:111], a[44:47], v[208:211]\n"
"ds_read_b64_tr_b16 a[152:153], v228 offset:26112\n"
"v_exp_f32 v199, v199\n"
"v_mul_f32 v203, s62, v203\n"
"v_mfma_f32_16x16x32_bf16 v[212:215], a[112:115], a[32:35], 0\n"
"ds_read_b64_tr_b16 a[154:155], v228 offset:30208\n"
"v_cvt_pk_bf16_f32 v195, v198, v199\n"
"v_mfma_f32_16x16x32_bf16 v[212:215], a[116:119], a[36:39], v[212:215]\n"
"ds_read_b64_tr_b16 a[156:157], v229 offset:26112\n"
"v_exp_f32 v203, v203\n"
"v_mfma_f32_16x16x32_bf16 v[212:215], a[120:123], a[40:43], v[212:215]\n"
"ds_read_b64_tr_b16 a[158:159], v229 offset:30208\n"
"v_mul_f32 v204, s62, v204\n"
"v_mfma_f32_16x16x32_bf16 v[212:215], a[124:127], a[44:47], v[212:215]\n"
"v_cvt_pk_bf16_f32 v201, v202, v203\n"
"s_waitcnt lgkmcnt(5)\n"
"v_mfma_f32_16x16x32_bf16 v[0:3], a[128:131], v[192:195], v[0:3]\n"
"v_exp_f32 v204, v204\n"
"v_mfma_f32_16x16x32_bf16 v[4:7], a[132:135], v[192:195], v[4:7]\n"
"s_waitcnt vmcnt(24)\n"
"ds_read_b128 a[32:35], v225 offset:8192\n"
"v_mfma_f32_16x16x32_bf16 v[8:11], a[136:139], v[192:195], v[8:11]\n"
"v_mul_f32 v208, s62, v208\n"
"v_mul_f32 v205, s62, v205\n"
"v_mfma_f32_16x16x32_bf16 v[12:15], a[140:143], v[192:195], v[12:15]\n"
"ds_read_b128 a[36:39], v225 offset:8704\n"
"v_mul_f32 v209, s62, v209\n"
"v_mfma_f32_16x16x32_bf16 v[16:19], a[144:147], v[192:195], v[16:19]\n"
"v_exp_f32 v208, v208\n"
"s_waitcnt lgkmcnt(2)\n"
"v_mfma_f32_16x16x32_bf16 v[20:23], a[148:151], v[192:195], v[20:23]\n"
"ds_read_b128 a[40:43], v225 offset:9216\n"
"v_exp_f32 v205, v205\n"
"v_mfma_f32_16x16x32_bf16 v[24:27], a[152:155], v[192:195], v[24:27]\n"
"v_mul_f32 v206, s62, v206\n"
"v_cvt_pk_bf16_f32 v202, v204, v205\n"
"v_mfma_f32_16x16x32_bf16 v[28:31], a[156:159], v[192:195], v[28:31]\n"
"ds_read_b128 a[44:47], v225 offset:9728\n"
"v_exp_f32 v209, v209\n"
"v_mfma_f32_16x16x32_bf16 a[160:163], a[184:187], v[192:195], a[160:163]\n"
"v_exp_f32 v206, v206\n"
"v_mfma_f32_16x16x32_bf16 v[192:195], a[96:99], a[48:51], 0\n"
"v_mul_f32 v210, s62, v210\n"
"v_cvt_pk_bf16_f32 v208, v208, v209\n"
"v_mfma_f32_16x16x32_bf16 v[192:195], a[100:103], a[52:55], v[192:195]\n"
"v_mul_f32 v207, s62, v207\n"
"s_nop 1\n"
"v_accvgpr_read_b32 v234, a160\n"
"v_rcp_f32 v236, v234\n"
"v_mfma_f32_16x16x32_bf16 v[192:195], a[104:107], a[56:59], v[192:195]\n"
"v_exp_f32 v210, v210\n"
"v_cmp_nle_f32 vcc, s63, v234\n"
"s_or_b64 s[66:67], s[66:67], vcc\n"
"v_mfma_f32_16x16x32_bf16 v[192:195], a[108:111], a[60:63], v[192:195]\n"
"v_exp_f32 v207, v207\n"
"v_mul_f32 v211, s62, v211\n"
"v_cmp_nge_f32 vcc, s64, v234\n"
"s_or_b64 s[66:67], s[66:67], vcc\n"
"v_add_u32 v233, 0, v232\n"
"v_mfma_f32_16x16x32_bf16 v[196:199], a[112:115], a[48:51], 0\n"
"v_mul_f32 v0, v236, v0\n"
"v_cvt_pk_bf16_f32 v203, v206, v207\n"
"v_mfma_f32_16x16x32_bf16 v[196:199], a[116:119], a[52:55], v[196:199]\n"
"v_exp_f32 v211, v211\n"
"v_mfma_f32_16x16x32_bf16 v[196:199], a[120:123], a[56:59], v[196:199]\n"
"v_mul_f32 v1, v236, v1\n"
"v_mul_f32 v212, s62, v212\n"
"v_mfma_f32_16x16x32_bf16 v[196:199], a[124:127], a[60:63], v[196:199]\n"
"v_cvt_pk_bf16_f32 v209, v210, v211\n"
"v_mul_f32 v2, v236, v2\n"
"v_mfma_f32_16x16x32_bf16 v[32:35], a[128:131], v[200:203], v[32:35]\n"
"v_exp_f32 v212, v212\n"
"v_mfma_f32_16x16x32_bf16 v[36:39], a[132:135], v[200:203], v[36:39]\n"
"s_waitcnt vmcnt(16)\n"
"ds_read_b128 a[48:51], v225 offset:12288\n"
"v_mfma_f32_16x16x32_bf16 v[40:43], a[136:139], v[200:203], v[40:43]\n"
"v_mul_f32 v192, s62, v192\n"
"v_mul_f32 v3, v236, v3\n"
"v_cvt_pk_bf16_f32 v0, v0, v1\n"
"v_mfma_f32_16x16x32_bf16 v[44:47], a[140:143], v[200:203], v[44:47]\n"
"ds_read_b128 a[52:55], v225 offset:12800\n"
"v_mul_f32 v213, s62, v213\n"
"v_cvt_pk_bf16_f32 v1, v2, v3\n"
"v_mfma_f32_16x16x32_bf16 v[48:51], a[144:147], v[200:203], v[48:51]\n"
"s_barrier\n"
"v_mfma_f32_16x16x32_bf16 v[52:55], a[148:151], v[200:203], v[52:55]\n"
"ds_read_b128 a[56:59], v225 offset:13312\n"
"v_exp_f32 v192, v192\n"
"v_exp_f32 v213, v213\n"
"v_mul_f32 v193, s62, v193\n"
"v_mul_f32 v16, v236, v16\n"
"v_mfma_f32_16x16x32_bf16 v[56:59], a[152:155], v[200:203], v[56:59]\n"
"v_mul_f32 v214, s62, v214\n"
"s_add_u32 m0, s56, 24576\n"
"s_add_u32 s72, s57, 122880\n"
"v_mfma_f32_16x16x32_bf16 v[60:63], a[156:159], v[200:203], v[60:63]\n"
"buffer_load_dwordx4 v226, s[40:43], s72 offen nt lds\n"
"ds_read_b128 a[60:63], v225 offset:13824\n"
"v_cvt_pk_bf16_f32 v210, v212, v213\n"
"v_exp_f32 v193, v193\n"
"v_mul_f32 v17, v236, v17\n"
"v_mfma_f32_16x16x32_bf16 a[164:167], a[184:187], v[200:203], a[164:167]\n"
"buffer_load_dwordx4 v226, s[40:43], s72 offen offset:2048 nt lds\n"
"v_exp_f32 v214, v214\n"
"v_mfma_f32_16x16x32_bf16 v[200:203], a[96:99], a[64:67], 0\n"
"v_mul_f32 v194, s62, v194\n"
"v_cvt_pk_bf16_f32 v192, v192, v193\n"
"v_mul_f32 v18, v236, v18\n"
"s_add_u32 m0, s56, 57344\n"
"v_mfma_f32_16x16x32_bf16 v[200:203], a[100:103], a[68:71], v[200:203]\n"
"buffer_load_dwordx4 v226, s[44:47], s72 offen nt lds\n"
"v_mul_f32 v215, s62, v215\n"
"v_accvgpr_read_b32 v235, a164\n"
"v_rcp_f32 v237, v235\n"
"v_mfma_f32_16x16x32_bf16 v[200:203], a[104:107], a[72:75], v[200:203]\n"
"buffer_load_dwordx4 v226, s[44:47], s72 offen offset:2048 nt lds\n"
"v_exp_f32 v194, v194\n"
"v_cmp_nle_f32 vcc, s63, v235\n"
"s_or_b64 s[66:67], s[66:67], vcc\n"
"v_mfma_f32_16x16x32_bf16 v[200:203], a[108:111], a[76:79], v[200:203]\n"
"v_exp_f32 v215, v215\n"
"v_mul_f32 v195, s62, v195\n"
"v_mul_f32 v19, v236, v19\n"
"v_cvt_pk_bf16_f32 v16, v16, v17\n"
"v_cmp_nge_f32 vcc, s64, v235\n"
"s_or_b64 s[66:67], s[66:67], vcc\n"
"v_mfma_f32_16x16x32_bf16 v[204:207], a[112:115], a[64:67], 0\n"
"v_cvt_pk_bf16_f32 v17, v18, v19\n"
"v_mul_f32 v32, v237, v32\n"
"v_mfma_f32_16x16x32_bf16 v[204:207], a[116:119], a[68:71], v[204:207]\n"
"ds_write2_b64 v233, v[0:1], v[16:17] offset0:0 offset1:16\n"
"v_cvt_pk_bf16_f32 v211, v214, v215\n"
"v_exp_f32 v195, v195\n"
"v_mul_f32 v4, v236, v4\n"
"v_mfma_f32_16x16x32_bf16 v[204:207], a[120:123], a[72:75], v[204:207]\n"
"v_mul_f32 v33, v237, v33\n"
"v_mul_f32 v196, s62, v196\n"
"v_mfma_f32_16x16x32_bf16 v[204:207], a[124:127], a[76:79], v[204:207]\n"
"v_cvt_pk_bf16_f32 v193, v194, v195\n"
"v_mul_f32 v5, v236, v5\n"
"v_mfma_f32_16x16x32_bf16 v[64:67], a[128:131], v[208:211], v[64:67]\n"
"v_mul_f32 v34, v237, v34\n"
"v_mul_f32 v200, s62, v200\n"
"v_mfma_f32_16x16x32_bf16 v[68:71], a[132:135], v[208:211], v[68:71]\n"
"s_waitcnt vmcnt(12)\n"
"ds_read_b128 a[64:67], v225 offset:16384\n"
"v_exp_f32 v196, v196\n"
"v_mfma_f32_16x16x32_bf16 v[72:75], a[136:139], v[208:211], v[72:75]\n"
"v_mul_f32 v6, v236, v6\n"
"v_mul_f32 v35, v237, v35\n"
"v_cvt_pk_bf16_f32 v32, v32, v33\n"
"v_mfma_f32_16x16x32_bf16 v[76:79], a[140:143], v[208:211], v[76:79]\n"
"ds_read_b128 a[68:71], v225 offset:16896\n"
"v_mul_f32 v197, s62, v197\n"
"v_cvt_pk_bf16_f32 v33, v34, v35\n"
"v_mfma_f32_16x16x32_bf16 v[80:83], a[144:147], v[208:211], v[80:83]\n"
"v_exp_f32 v200, v200\n"
"v_mul_f32 v7, v236, v7\n"
"v_cvt_pk_bf16_f32 v4, v4, v5\n"
"v_mfma_f32_16x16x32_bf16 v[84:87], a[148:151], v[208:211], v[84:87]\n"
"ds_read_b128 a[72:75], v225 offset:17408\n"
"v_exp_f32 v197, v197\n"
"v_mul_f32 v201, s62, v201\n"
"v_cvt_pk_bf16_f32 v5, v6, v7\n"
"v_mul_f32 v48, v237, v48\n"
"v_mfma_f32_16x16x32_bf16 v[88:91], a[152:155], v[208:211], v[88:91]\n"
"v_mul_f32 v198, s62, v198\n"
"v_mul_f32 v20, v236, v20\n"
"v_mfma_f32_16x16x32_bf16 v[92:95], a[156:159], v[208:211], v[92:95]\n"
"ds_read_b128 a[76:79], v225 offset:17920\n"
"v_cvt_pk_bf16_f32 v194, v196, v197\n"
"v_exp_f32 v201, v201\n"
"v_mul_f32 v49, v237, v49\n"
"v_mfma_f32_16x16x32_bf16 a[168:171], a[184:187], v[208:211], a[168:171]\n"
"v_exp_f32 v198, v198\n"
"v_mul_f32 v21, v236, v21\n"
"v_mfma_f32_16x16x32_bf16 v[208:211], a[96:99], a[80:83], 0\n"
"ds_read_b128 a[96:99], v224 offset:0\n"
"v_mul_f32 v202, s62, v202\n"
"v_cvt_pk_bf16_f32 v200, v200, v201\n"
"v_mul_f32 v50, v237, v50\n"
"v_mfma_f32_16x16x32_bf16 v[208:211], a[100:103], a[84:87], v[208:211]\n"
"v_mul_f32 v199, s62, v199\n"
"v_mul_f32 v22, v236, v22\n"
"v_mfma_f32_16x16x32_bf16 v[208:211], a[104:107], a[88:91], v[208:211]\n"
"ds_read_b128 a[100:103], v224 offset:512\n"
"v_exp_f32 v202, v202\n"
"v_mfma_f32_16x16x32_bf16 v[208:211], a[108:111], a[92:95], v[208:211]\n"
"v_exp_f32 v199, v199\n"
"v_mul_f32 v203, s62, v203\n"
"v_mul_f32 v51, v237, v51\n"
"v_cvt_pk_bf16_f32 v48, v48, v49\n"
"v_mfma_f32_16x16x32_bf16 v[212:215], a[112:115], a[80:83], 0\n"
"ds_read_b128 a[104:107], v224 offset:1024\n"
"v_mul_f32 v23, v236, v23\n"
"v_cvt_pk_bf16_f32 v20, v20, v21\n"
"v_xor_b32 v239, 32, v233\n"
"v_cvt_pk_bf16_f32 v49, v50, v51\n"
"v_mfma_f32_16x16x32_bf16 v[212:215], a[116:119], a[84:87], v[212:215]\n"
"v_cvt_pk_bf16_f32 v195, v198, v199\n"
"v_exp_f32 v203, v203\n"
"v_cvt_pk_bf16_f32 v21, v22, v23\n"
"v_mul_f32 v36, v237, v36\n"
"v_mfma_f32_16x16x32_bf16 v[212:215], a[120:123], a[88:91], v[212:215]\n"
"ds_read_b128 a[108:111], v224 offset:1536\n"
"ds_write2_b64 v239, v[4:5], v[20:21] offset0:0 offset1:16\n"
"v_mul_f32 v8, v236, v8\n"
"v_mfma_f32_16x16x32_bf16 v[212:215], a[124:127], a[92:95], v[212:215]\n"
"v_mul_f32 v204, s62, v204\n"
"v_cvt_pk_bf16_f32 v201, v202, v203\n"
"v_mul_f32 v37, v237, v37\n"
"v_mfma_f32_16x16x32_bf16 v[96:99], a[128:131], v[192:195], v[96:99]\n"
"ds_read_b128 a[112:115], v224 offset:4096\n"
"v_mul_f32 v9, v236, v9\n"
"v_mfma_f32_16x16x32_bf16 v[100:103], a[132:135], v[192:195], v[100:103]\n"
"s_waitcnt vmcnt(4)\n"
"ds_read_b128 a[80:83], v225 offset:20480\n"
"v_exp_f32 v204, v204\n"
"v_mfma_f32_16x16x32_bf16 v[104:107], a[136:139], v[192:195], v[104:107]\n"
"ds_read_b128 a[116:119], v224 offset:4608\n"
"v_mul_f32 v208, s62, v208\n"
"v_mul_f32 v38, v237, v38\n"
"v_mfma_f32_16x16x32_bf16 v[108:111], a[140:143], v[192:195], v[108:111]\n"
"ds_read_b128 a[84:87], v225 offset:20992\n"
"v_mul_f32 v205, s62, v205\n"
"v_mul_f32 v10, v236, v10\n"
"v_mfma_f32_16x16x32_bf16 v[112:115], a[144:147], v[192:195], v[112:115]\n"
"ds_read_b128 a[120:123], v224 offset:5120\n"
"v_exp_f32 v208, v208\n"
"v_mul_f32 v39, v237, v39\n"
"v_cvt_pk_bf16_f32 v36, v36, v37\n"
"v_mfma_f32_16x16x32_bf16 v[116:119], a[148:151], v[192:195], v[116:119]\n"
"ds_read_b128 a[88:91], v225 offset:21504\n"
"v_exp_f32 v205, v205\n"
"v_mul_f32 v209, s62, v209\n"
"v_mul_f32 v11, v236, v11\n"
"v_cvt_pk_bf16_f32 v8, v8, v9\n"
"v_cvt_pk_bf16_f32 v37, v38, v39\n"
"v_mfma_f32_16x16x32_bf16 v[120:123], a[152:155], v[192:195], v[120:123]\n"
"ds_read_b128 a[124:127], v224 offset:5632\n"
"v_mul_f32 v206, s62, v206\n"
"v_cvt_pk_bf16_f32 v9, v10, v11\n"
"v_mul_f32 v52, v237, v52\n"
"v_mfma_f32_16x16x32_bf16 v[124:127], a[156:159], v[192:195], v[124:127]\n"
"ds_read_b128 a[92:95], v225 offset:22016\n"
"v_cvt_pk_bf16_f32 v202, v204, v205\n"
"v_exp_f32 v209, v209\n"
"v_mfma_f32_16x16x32_bf16 a[172:175], a[184:187], v[192:195], a[172:175]\n"
"v_exp_f32 v206, v206\n"
"v_mul_f32 v24, v236, v24\n"
"v_mul_f32 v53, v237, v53\n"
"s_waitcnt lgkmcnt(5)\n"
"v_mfma_f32_16x16x32_bf16 v[192:195], a[96:99], a[0:3], 0\n"
"v_mul_f32 v210, s62, v210\n"
"v_cvt_pk_bf16_f32 v208, v208, v209\n"
"v_mfma_f32_16x16x32_bf16 v[192:195], a[100:103], a[4:7], v[192:195]\n"
"v_mul_f32 v207, s62, v207\n"
"v_mul_f32 v25, v236, v25\n"
"v_mfma_f32_16x16x32_bf16 v[192:195], a[104:107], a[8:11], v[192:195]\n"
"v_exp_f32 v210, v210\n"
"v_mul_f32 v54, v237, v54\n"
"v_mfma_f32_16x16x32_bf16 v[192:195], a[108:111], a[12:15], v[192:195]\n"
"v_exp_f32 v207, v207\n"
"v_mul_f32 v211, s62, v211\n"
"v_mul_f32 v26, v236, v26\n"
"v_mfma_f32_16x16x32_bf16 v[196:199], a[112:115], a[0:3], 0\n"
"v_mul_f32 v55, v237, v55\n"
"v_cvt_pk_bf16_f32 v52, v52, v53\n"
"v_mfma_f32_16x16x32_bf16 v[196:199], a[116:119], a[4:7], v[196:199]\n"
"v_cvt_pk_bf16_f32 v203, v206, v207\n"
"v_exp_f32 v211, v211\n"
"v_cvt_pk_bf16_f32 v53, v54, v55\n"
"s_waitcnt lgkmcnt(0)\n"
"v_mfma_f32_16x16x32_bf16 v[196:199], a[120:123], a[8:11], v[196:199]\n"
"v_mul_f32 v27, v236, v27\n"
"v_cvt_pk_bf16_f32 v24, v24, v25\n"
"v_xor_b32 v240, 64, v233\n"
"v_mul_f32 v40, v237, v40\n"
"v_mfma_f32_16x16x32_bf16 v[196:199], a[124:127], a[12:15], v[196:199]\n"
"v_mul_f32 v212, s62, v212\n"
"v_cvt_pk_bf16_f32 v209, v210, v211\n"
"v_cvt_pk_bf16_f32 v25, v26, v27\n"
"v_mfma_f32_16x16x32_bf16 v[128:131], a[128:131], v[200:203], v[128:131]\n"
"ds_write2_b64 v240, v[8:9], v[24:25] offset0:0 offset1:16\n"
"v_mul_f32 v12, v236, v12\n"
"v_mfma_f32_16x16x32_bf16 v[132:135], a[132:135], v[200:203], v[132:135]\n"
"v_exp_f32 v212, v212\n"
"v_mul_f32 v41, v237, v41\n"
"v_mfma_f32_16x16x32_bf16 v[136:139], a[136:139], v[200:203], v[136:139]\n"
"v_mul_f32 v192, s62, v192\n"
"v_mul_f32 v13, v236, v13\n"
"v_mfma_f32_16x16x32_bf16 v[140:143], a[140:143], v[200:203], v[140:143]\n"
"v_mul_f32 v213, s62, v213\n"
"v_mul_f32 v42, v237, v42\n"
"v_mfma_f32_16x16x32_bf16 v[144:147], a[144:147], v[200:203], v[144:147]\n"
"v_exp_f32 v192, v192\n"
"v_mul_f32 v14, v236, v14\n"
"v_mfma_f32_16x16x32_bf16 v[148:151], a[148:151], v[200:203], v[148:151]\n"
"v_exp_f32 v213, v213\n"
"v_mul_f32 v193, s62, v193\n"
"v_mul_f32 v43, v237, v43\n"
"v_cvt_pk_bf16_f32 v40, v40, v41\n"
"v_mfma_f32_16x16x32_bf16 v[152:155], a[152:155], v[200:203], v[152:155]\n"
"v_mul_f32 v214, s62, v214\n"
"v_cvt_pk_bf16_f32 v41, v42, v43\n"
"v_mfma_f32_16x16x32_bf16 v[156:159], a[156:159], v[200:203], v[156:159]\n"
"v_cvt_pk_bf16_f32 v210, v212, v213\n"
"v_exp_f32 v193, v193\n"
"v_mul_f32 v15, v236, v15\n"
"v_cvt_pk_bf16_f32 v12, v12, v13\n"
"v_mfma_f32_16x16x32_bf16 a[176:179], a[184:187], v[200:203], a[176:179]\n"
"v_exp_f32 v214, v214\n"
"v_cvt_pk_bf16_f32 v13, v14, v15\n"
"v_mul_f32 v56, v237, v56\n"
"v_mfma_f32_16x16x32_bf16 v[200:203], a[96:99], a[16:19], 0\n"
"v_mul_f32 v194, s62, v194\n"
"v_cvt_pk_bf16_f32 v192, v192, v193\n"
"v_mul_f32 v28, v236, v28\n"
"v_mfma_f32_16x16x32_bf16 v[200:203], a[100:103], a[20:23], v[200:203]\n"
"v_mul_f32 v215, s62, v215\n"
"v_mul_f32 v57, v237, v57\n"
"v_mfma_f32_16x16x32_bf16 v[200:203], a[104:107], a[24:27], v[200:203]\n"
"v_exp_f32 v194, v194\n"
"v_mul_f32 v29, v236, v29\n"
"v_mfma_f32_16x16x32_bf16 v[200:203], a[108:111], a[28:31], v[200:203]\n"
"v_exp_f32 v215, v215\n"
"v_mul_f32 v195, s62, v195\n"
"v_mul_f32 v58, v237, v58\n"
"v_mfma_f32_16x16x32_bf16 v[204:207], a[112:115], a[16:19], 0\n"
"v_cvt_pk_bf16_f32 v211, v214, v215\n"
"v_mul_f32 v30, v236, v30\n"
"v_mfma_f32_16x16x32_bf16 v[204:207], a[116:119], a[20:23], v[204:207]\n"
"v_exp_f32 v195, v195\n"
"v_mfma_f32_16x16x32_bf16 v[204:207], a[120:123], a[24:27], v[204:207]\n"
"v_mul_f32 v59, v237, v59\n"
"v_cvt_pk_bf16_f32 v56, v56, v57\n"
"v_mfma_f32_16x16x32_bf16 v[204:207], a[124:127], a[28:31], v[204:207]\n"
"v_mul_f32 v196, s62, v196\n"
"v_cvt_pk_bf16_f32 v193, v194, v195\n"
"v_mul_f32 v31, v236, v31\n"
"v_accvgpr_read_b32 v234, a168\n"
"v_rcp_f32 v236, v234\n"
"v_cmp_nle_f32 vcc, s63, v234\n"
"s_or_b64 s[66:67], s[66:67], vcc\n"
"v_cmp_nge_f32 vcc, s64, v234\n"
"s_or_b64 s[66:67], s[66:67], vcc\n"
"v_mul_f32 v64, v236, v64\n"
"v_mul_f32 v65, v236, v65\n"
"v_mul_f32 v66, v236, v66\n"
"v_mul_f32 v67, v236, v67\n"
"v_cvt_pk_bf16_f32 v64, v64, v65\n"
"v_cvt_pk_bf16_f32 v65, v66, v67\n"
"v_mul_f32 v80, v236, v80\n"
"v_mul_f32 v81, v236, v81\n"
"v_mul_f32 v82, v236, v82\n"
"v_mul_f32 v83, v236, v83\n"
"v_cvt_pk_bf16_f32 v80, v80, v81\n"
"v_cvt_pk_bf16_f32 v81, v82, v83\n"
"v_mul_f32 v68, v236, v68\n"
"v_mul_f32 v69, v236, v69\n"
"v_mul_f32 v70, v236, v70\n"
"v_mul_f32 v71, v236, v71\n"
"v_cvt_pk_bf16_f32 v68, v68, v69\n"
"v_cvt_pk_bf16_f32 v69, v70, v71\n"
"v_mul_f32 v84, v236, v84\n"
"v_mul_f32 v85, v236, v85\n"
"v_mul_f32 v86, v236, v86\n"
"v_mul_f32 v87, v236, v87\n"
"v_cvt_pk_bf16_f32 v84, v84, v85\n"
"v_cvt_pk_bf16_f32 v85, v86, v87\n"
"v_mul_f32 v72, v236, v72\n"
"v_cvt_pk_bf16_f32 v28, v28, v29\n"
"v_xor_b32 v241, 96, v233\n"
"v_add_u32 v233, 4096, v232\n"
"ds_write2_b64 v233, v[32:33], v[48:49] offset0:0 offset1:16\n"
"v_xor_b32 v239, 32, v233\n"
"ds_write2_b64 v239, v[36:37], v[52:53] offset0:0 offset1:16\n"
"v_xor_b32 v240, 64, v233\n"
"v_cvt_pk_bf16_f32 v57, v58, v59\n"
"v_mfma_f32_16x16x32_bf16 v[160:163], a[128:131], v[208:211], v[160:163]\n"
"ds_read_b64_tr_b16 a[128:129], v228 offset:0\n"
"ds_write2_b64 v240, v[40:41], v[56:57] offset0:0 offset1:16\n"
"v_cvt_pk_bf16_f32 v29, v30, v31\n"
"v_mul_f32 v44, v237, v44\n"
"v_mfma_f32_16x16x32_bf16 v[164:167], a[132:135], v[208:211], v[164:167]\n"
"ds_read_b64_tr_b16 a[130:131], v228 offset:4096\n"
"ds_write2_b64 v241, v[12:13], v[28:29] offset0:0 offset1:16\n"
"v_exp_f32 v196, v196\n"
"v_mul_f32 v73, v236, v73\n"
"v_mfma_f32_16x16x32_bf16 v[168:171], a[136:139], v[208:211], v[168:171]\n"
"ds_read_b64_tr_b16 a[132:133], v229 offset:0\n"
"v_mul_f32 v200, s62, v200\n"
"v_mul_f32 v45, v237, v45\n"
"v_mfma_f32_16x16x32_bf16 v[172:175], a[140:143], v[208:211], v[172:175]\n"
"ds_read_b64_tr_b16 a[134:135], v229 offset:4096\n"
"v_mul_f32 v197, s62, v197\n"
"v_mul_f32 v74, v236, v74\n"
"v_mfma_f32_16x16x32_bf16 v[176:179], a[144:147], v[208:211], v[176:179]\n"
"ds_read_b64_tr_b16 a[136:137], v228 offset:512\n"
"v_exp_f32 v200, v200\n"
"v_mul_f32 v46, v237, v46\n"
"v_mfma_f32_16x16x32_bf16 v[180:183], a[148:151], v[208:211], v[180:183]\n"
"ds_read_b64_tr_b16 a[138:139], v228 offset:4608\n"
"v_exp_f32 v197, v197\n"
"v_mul_f32 v201, s62, v201\n"
"v_mul_f32 v75, v236, v75\n"
"v_cvt_pk_bf16_f32 v72, v72, v73\n"
"v_mfma_f32_16x16x32_bf16 v[184:187], a[152:155], v[208:211], v[184:187]\n"
"ds_read_b64_tr_b16 a[140:141], v229 offset:512\n"
"v_mul_f32 v198, s62, v198\n"
"v_cvt_pk_bf16_f32 v73, v74, v75\n"
"v_mfma_f32_16x16x32_bf16 v[188:191], a[156:159], v[208:211], v[188:191]\n"
"ds_read_b64_tr_b16 a[142:143], v229 offset:4608\n"
"v_cvt_pk_bf16_f32 v194, v196, v197\n"
"v_exp_f32 v201, v201\n"
"v_mul_f32 v47, v237, v47\n"
"v_cvt_pk_bf16_f32 v44, v44, v45\n"
"v_mfma_f32_16x16x32_bf16 a[180:183], a[184:187], v[208:211], a[180:183]\n"
"ds_read_b64_tr_b16 a[144:145], v228 offset:1024\n"
"v_exp_f32 v198, v198\n"
"v_cvt_pk_bf16_f32 v45, v46, v47\n"
"v_mul_f32 v88, v236, v88\n"
"v_mfma_f32_16x16x32_bf16 v[208:211], a[96:99], a[32:35], 0\n"
"ds_read_b64_tr_b16 a[146:147], v228 offset:5120\n"
"v_mul_f32 v202, s62, v202\n"
"v_cvt_pk_bf16_f32 v200, v200, v201\n"
"v_mul_f32 v60, v237, v60\n"
"v_mfma_f32_16x16x32_bf16 v[208:211], a[100:103], a[36:39], v[208:211]\n"
"ds_read_b64_tr_b16 a[148:149], v229 offset:1024\n"
"v_mul_f32 v199, s62, v199\n"
"v_mul_f32 v89, v236, v89\n"
"v_mfma_f32_16x16x32_bf16 v[208:211], a[104:107], a[40:43], v[208:211]\n"
"ds_read_b64_tr_b16 a[150:151], v229 offset:5120\n"
"v_exp_f32 v202, v202\n"
"v_mul_f32 v61, v237, v61\n"
"v_mfma_f32_16x16x32_bf16 v[208:211], a[108:111], a[44:47], v[208:211]\n"
"ds_read_b64_tr_b16 a[152:153], v228 offset:1536\n"
"v_exp_f32 v199, v199\n"
"v_mul_f32 v203, s62, v203\n"
"v_mul_f32 v90, v236, v90\n"
"v_mfma_f32_16x16x32_bf16 v[212:215], a[112:115], a[32:35], 0\n"
"ds_read_b64_tr_b16 a[154:155], v228 offset:5632\n"
"v_cvt_pk_bf16_f32 v195, v198, v199\n"
"v_mfma_f32_16x16x32_bf16 v[212:215], a[116:119], a[36:39], v[212:215]\n"
"ds_read_b64_tr_b16 a[156:157], v229 offset:1536\n"
"v_exp_f32 v203, v203\n"
"v_mul_f32 v62, v237, v62\n"
"v_mfma_f32_16x16x32_bf16 v[212:215], a[120:123], a[40:43], v[212:215]\n"
"ds_read_b64_tr_b16 a[158:159], v229 offset:5632\n"
"v_mul_f32 v91, v236, v91\n"
"v_cvt_pk_bf16_f32 v88, v88, v89\n"
"v_mfma_f32_16x16x32_bf16 v[212:215], a[124:127], a[44:47], v[212:215]\n"
"v_mul_f32 v204, s62, v204\n"
"v_cvt_pk_bf16_f32 v201, v202, v203\n"
"v_mul_f32 v63, v237, v63\n"
"v_accvgpr_read_b32 v235, a172\n"
"v_rcp_f32 v237, v235\n"
"v_cmp_nle_f32 vcc, s63, v235\n"
"s_or_b64 s[66:67], s[66:67], vcc\n"
"v_cmp_nge_f32 vcc, s64, v235\n"
"s_or_b64 s[66:67], s[66:67], vcc\n"
"v_mul_f32 v96, v237, v96\n"
"v_mul_f32 v97, v237, v97\n"
"v_mul_f32 v98, v237, v98\n"
"v_mul_f32 v99, v237, v99\n"
"v_cvt_pk_bf16_f32 v96, v96, v97\n"
"v_cvt_pk_bf16_f32 v97, v98, v99\n"
"v_mul_f32 v112, v237, v112\n"
"v_mul_f32 v113, v237, v113\n"
"v_mul_f32 v114, v237, v114\n"
"v_mul_f32 v115, v237, v115\n"
"v_cvt_pk_bf16_f32 v112, v112, v113\n"
"v_cvt_pk_bf16_f32 v113, v114, v115\n"
"v_mul_f32 v100, v237, v100\n"
"v_mul_f32 v101, v237, v101\n"
"v_mul_f32 v102, v237, v102\n"
"v_mul_f32 v103, v237, v103\n"
"v_cvt_pk_bf16_f32 v100, v100, v101\n"
"v_cvt_pk_bf16_f32 v101, v102, v103\n"
"v_mul_f32 v116, v237, v116\n"
"v_mul_f32 v117, v237, v117\n"
"v_mul_f32 v118, v237, v118\n"
"v_mul_f32 v119, v237, v119\n"
"v_cvt_pk_bf16_f32 v116, v116, v117\n"
"v_cvt_pk_bf16_f32 v117, v118, v119\n"
"v_mul_f32 v104, v237, v104\n"
"v_cvt_pk_bf16_f32 v60, v60, v61\n"
"v_xor_b32 v241, 96, v233\n"
"v_add_u32 v233, 8192, v232\n"
"ds_write2_b64 v233, v[64:65], v[80:81] offset0:0 offset1:16\n"
"v_xor_b32 v239, 32, v233\n"
"ds_write2_b64 v239, v[68:69], v[84:85] offset0:0 offset1:16\n"
"v_xor_b32 v240, 64, v233\n"
"v_cvt_pk_bf16_f32 v89, v90, v91\n"
"s_waitcnt lgkmcnt(7)\n"
"v_mfma_f32_16x16x32_bf16 v[0:3], a[128:131], v[192:195], 0\n"
"ds_write2_b64 v240, v[72:73], v[88:89] offset0:0 offset1:16\n"
"v_cvt_pk_bf16_f32 v61, v62, v63\n"
"v_mul_f32 v76, v236, v76\n"
"v_mfma_f32_16x16x32_bf16 v[4:7], a[132:135], v[192:195], 0\n"
"ds_write2_b64 v241, v[44:45], v[60:61] offset0:0 offset1:16\n"
"v_exp_f32 v204, v204\n"
"v_mul_f32 v105, v237, v105\n"
"v_mfma_f32_16x16x32_bf16 v[8:11], a[136:139], v[192:195], 0\n"
"v_mul_f32 v208, s62, v208\n"
"v_mul_f32 v77, v236, v77\n"
"v_mfma_f32_16x16x32_bf16 v[12:15], a[140:143], v[192:195], 0\n"
"v_mul_f32 v205, s62, v205\n"
"v_mul_f32 v106, v237, v106\n"
"v_mfma_f32_16x16x32_bf16 v[16:19], a[144:147], v[192:195], 0\n"
"v_exp_f32 v208, v208\n"
"v_mul_f32 v78, v236, v78\n"
"s_waitcnt lgkmcnt(4)\n"
"v_mfma_f32_16x16x32_bf16 v[20:23], a[148:151], v[192:195], 0\n"
"v_exp_f32 v205, v205\n"
"v_mul_f32 v209, s62, v209\n"
"v_mul_f32 v107, v237, v107\n"
"v_cvt_pk_bf16_f32 v104, v104, v105\n"
"v_mfma_f32_16x16x32_bf16 v[24:27], a[152:155], v[192:195], 0\n"
"v_mul_f32 v206, s62, v206\n"
"v_cvt_pk_bf16_f32 v105, v106, v107\n"
"v_mfma_f32_16x16x32_bf16 v[28:31], a[156:159], v[192:195], 0\n"
"v_cvt_pk_bf16_f32 v202, v204, v205\n"
"v_exp_f32 v209, v209\n"
"v_mul_f32 v79, v236, v79\n"
"v_cvt_pk_bf16_f32 v76, v76, v77\n"
"v_mfma_f32_16x16x32_bf16 a[160:163], a[184:187], v[192:195], 0\n"
"v_exp_f32 v206, v206\n"
"v_cvt_pk_bf16_f32 v77, v78, v79\n"
"v_mul_f32 v120, v237, v120\n"
"v_mfma_f32_16x16x32_bf16 v[192:195], a[96:99], a[48:51], 0\n"
"v_mul_f32 v210, s62, v210\n"
"v_cvt_pk_bf16_f32 v208, v208, v209\n"
"v_mul_f32 v92, v236, v92\n"
"v_mfma_f32_16x16x32_bf16 v[192:195], a[100:103], a[52:55], v[192:195]\n"
"v_mul_f32 v207, s62, v207\n"
"v_mul_f32 v121, v237, v121\n"
"v_mfma_f32_16x16x32_bf16 v[192:195], a[104:107], a[56:59], v[192:195]\n"
"v_exp_f32 v210, v210\n"
"v_mul_f32 v93, v236, v93\n"
"v_mfma_f32_16x16x32_bf16 v[192:195], a[108:111], a[60:63], v[192:195]\n"
"v_exp_f32 v207, v207\n"
"v_mul_f32 v211, s62, v211\n"
"v_mul_f32 v122, v237, v122\n"
"v_mfma_f32_16x16x32_bf16 v[196:199], a[112:115], a[48:51], 0\n"
"v_cvt_pk_bf16_f32 v203, v206, v207\n"
"v_mul_f32 v94, v236, v94\n"
"v_mfma_f32_16x16x32_bf16 v[196:199], a[116:119], a[52:55], v[196:199]\n"
"v_exp_f32 v211, v211\n"
"v_mfma_f32_16x16x32_bf16 v[196:199], a[120:123], a[56:59], v[196:199]\n"
"v_mul_f32 v123, v237, v123\n"
"v_cvt_pk_bf16_f32 v120, v120, v121\n"
"v_mfma_f32_16x16x32_bf16 v[196:199], a[124:127], a[60:63], v[196:199]\n"
"v_mul_f32 v212, s62, v212\n"
"v_cvt_pk_bf16_f32 v209, v210, v211\n"
"v_mul_f32 v95, v236, v95\n"
"v_accvgpr_read_b32 v234, a176\n"
"v_rcp_f32 v236, v234\n"
"v_cmp_nle_f32 vcc, s63, v234\n"
"s_or_b64 s[66:67], s[66:67], vcc\n"
"v_cmp_nge_f32 vcc, s64, v234\n"
"s_or_b64 s[66:67], s[66:67], vcc\n"
"v_mul_f32 v128, v236, v128\n"
"v_mul_f32 v129, v236, v129\n"
"v_mul_f32 v130, v236, v130\n"
"v_mul_f32 v131, v236, v131\n"
"v_cvt_pk_bf16_f32 v128, v128, v129\n"
"v_cvt_pk_bf16_f32 v129, v130, v131\n"
"v_mul_f32 v144, v236, v144\n"
"v_mul_f32 v145, v236, v145\n"
"v_mul_f32 v146, v236, v146\n"
"v_mul_f32 v147, v236, v147\n"
"v_cvt_pk_bf16_f32 v144, v144, v145\n"
"v_cvt_pk_bf16_f32 v145, v146, v147\n"
"v_mul_f32 v132, v236, v132\n"
"v_mul_f32 v133, v236, v133\n"
"v_mul_f32 v134, v236, v134\n"
"v_mul_f32 v135, v236, v135\n"
"v_cvt_pk_bf16_f32 v132, v132, v133\n"
"v_cvt_pk_bf16_f32 v133, v134, v135\n"
"v_mul_f32 v148, v236, v148\n"
"v_mul_f32 v149, v236, v149\n"
"v_mul_f32 v150, v236, v150\n"
"v_mul_f32 v151, v236, v151\n"
"v_cvt_pk_bf16_f32 v148, v148, v149\n"
"v_cvt_pk_bf16_f32 v149, v150, v151\n"
"v_mul_f32 v136, v236, v136\n"
"v_cvt_pk_bf16_f32 v92, v92, v93\n"
"v_xor_b32 v241, 96, v233\n"
"v_add_u32 v233, 12288, v232\n"
"ds_write2_b64 v233, v[96:97], v[112:113] offset0:0 offset1:16\n"
"v_xor_b32 v239, 32, v233\n"
"ds_write2_b64 v239, v[100:101], v[116:117] offset0:0 offset1:16\n"
"v_xor_b32 v240, 64, v233\n"
"v_cvt_pk_bf16_f32 v121, v122, v123\n"
"v_mfma_f32_16x16x32_bf16 v[32:35], a[128:131], v[200:203], 0\n"
"ds_write2_b64 v240, v[104:105], v[120:121] offset0:0 offset1:16\n"
"v_cvt_pk_bf16_f32 v93, v94, v95\n"
"v_mul_f32 v108, v237, v108\n"
"v_mfma_f32_16x16x32_bf16 v[36:39], a[132:135], v[200:203], 0\n"
"ds_write2_b64 v241, v[76:77], v[92:93] offset0:0 offset1:16\n"
"v_exp_f32 v212, v212\n"
"v_mul_f32 v137, v236, v137\n"
"v_mfma_f32_16x16x32_bf16 v[40:43], a[136:139], v[200:203], 0\n"
"v_mul_f32 v192, s62, v192\n"
"v_mul_f32 v109, v237, v109\n"
"v_mfma_f32_16x16x32_bf16 v[44:47], a[140:143], v[200:203], 0\n"
"v_mul_f32 v213, s62, v213\n"
"v_mul_f32 v138, v236, v138\n"
"v_mfma_f32_16x16x32_bf16 v[48:51], a[144:147], v[200:203], 0\n"
"s_barrier\n"
"v_mfma_f32_16x16x32_bf16 v[52:55], a[148:151], v[200:203], 0\n"
"v_exp_f32 v192, v192\n"
"v_mul_f32 v110, v237, v110\n"
"v_exp_f32 v213, v213\n"
"v_mul_f32 v193, s62, v193\n"
"v_mul_f32 v139, v236, v139\n"
"v_cvt_pk_bf16_f32 v136, v136, v137\n"
"v_mfma_f32_16x16x32_bf16 v[56:59], a[152:155], v[200:203], 0\n"
"v_mul_f32 v214, s62, v214\n"
"v_cvt_pk_bf16_f32 v137, v138, v139\n"
"s_add_u32 m0, s56, 0\n"
"s_add_u32 s72, s57, 131072\n"
"v_mfma_f32_16x16x32_bf16 v[60:63], a[156:159], v[200:203], 0\n"
"buffer_load_dwordx4 v226, s[40:43], s72 offen nt lds\n"
"v_cvt_pk_bf16_f32 v210, v212, v213\n"
"v_exp_f32 v193, v193\n"
"v_mul_f32 v111, v237, v111\n"
"v_cvt_pk_bf16_f32 v108, v108, v109\n"
"v_mfma_f32_16x16x32_bf16 a[164:167], a[184:187], v[200:203], 0\n"
"buffer_load_dwordx4 v226, s[40:43], s72 offen offset:2048 nt lds\n"
"v_exp_f32 v214, v214\n"
"v_cvt_pk_bf16_f32 v109, v110, v111\n"
"v_mul_f32 v152, v236, v152\n"
"v_mfma_f32_16x16x32_bf16 v[200:203], a[96:99], a[64:67], 0\n"
"v_mul_f32 v194, s62, v194\n"
"v_cvt_pk_bf16_f32 v192, v192, v193\n"
"v_mul_f32 v124, v237, v124\n"
"s_add_u32 m0, s56, 32768\n"
"v_mfma_f32_16x16x32_bf16 v[200:203], a[100:103], a[68:71], v[200:203]\n"
"buffer_load_dwordx4 v226, s[44:47], s72 offen nt lds\n"
"v_mul_f32 v215, s62, v215\n"
"v_mul_f32 v153, v236, v153\n"
"v_mfma_f32_16x16x32_bf16 v[200:203], a[104:107], a[72:75], v[200:203]\n"
"buffer_load_dwordx4 v226, s[44:47], s72 offen offset:2048 nt lds\n"
"v_exp_f32 v194, v194\n"
"v_mul_f32 v125, v237, v125\n"
"v_mfma_f32_16x16x32_bf16 v[200:203], a[108:111], a[76:79], v[200:203]\n"
"v_exp_f32 v215, v215\n"
"v_mul_f32 v195, s62, v195\n"
"v_mul_f32 v154, v236, v154\n"
"v_mfma_f32_16x16x32_bf16 v[204:207], a[112:115], a[64:67], 0\n"
"v_cvt_pk_bf16_f32 v211, v214, v215\n"
"v_mul_f32 v126, v237, v126\n"
"v_mfma_f32_16x16x32_bf16 v[204:207], a[116:119], a[68:71], v[204:207]\n"
"v_exp_f32 v195, v195\n"
"v_mfma_f32_16x16x32_bf16 v[204:207], a[120:123], a[72:75], v[204:207]\n"
"v_mul_f32 v155, v236, v155\n"
"v_cvt_pk_bf16_f32 v152, v152, v153\n"
"v_mfma_f32_16x16x32_bf16 v[204:207], a[124:127], a[76:79], v[204:207]\n"
"v_mul_f32 v196, s62, v196\n"
"v_cvt_pk_bf16_f32 v193, v194, v195\n"
"v_mul_f32 v127, v237, v127\n"
"v_accvgpr_read_b32 v235, a180\n"
"v_rcp_f32 v237, v235\n"
"v_cmp_nle_f32 vcc, s63, v235\n"
"s_or_b64 s[66:67], s[66:67], vcc\n"
"v_cmp_nge_f32 vcc, s64, v235\n"
"s_or_b64 s[66:67], s[66:67], vcc\n"
"v_mul_f32 v160, v237, v160\n"
"v_mul_f32 v161, v237, v161\n"
"v_mul_f32 v162, v237, v162\n"
"v_mul_f32 v163, v237, v163\n"
"v_cvt_pk_bf16_f32 v160, v160, v161\n"
"v_cvt_pk_bf16_f32 v161, v162, v163\n"
"v_mul_f32 v176, v237, v176\n"
"v_mul_f32 v177, v237, v177\n"
"v_mul_f32 v178, v237, v178\n"
"v_mul_f32 v179, v237, v179\n"
"v_cvt_pk_bf16_f32 v176, v176, v177\n"
"v_cvt_pk_bf16_f32 v177, v178, v179\n"
"v_mul_f32 v164, v237, v164\n"
"v_mul_f32 v165, v237, v165\n"
"v_mul_f32 v166, v237, v166\n"
"v_mul_f32 v167, v237, v167\n"
"v_cvt_pk_bf16_f32 v164, v164, v165\n"
"v_cvt_pk_bf16_f32 v165, v166, v167\n"
"v_mul_f32 v180, v237, v180\n"
"v_mul_f32 v181, v237, v181\n"
"v_mul_f32 v182, v237, v182\n"
"v_mul_f32 v183, v237, v183\n"
"v_cvt_pk_bf16_f32 v180, v180, v181\n"
"v_cvt_pk_bf16_f32 v181, v182, v183\n"
"v_mul_f32 v168, v237, v168\n"
"v_cvt_pk_bf16_f32 v124, v124, v125\n"
"v_xor_b32 v241, 96, v233\n"
"v_add_u32 v233, 16384, v232\n"
"ds_write2_b64 v233, v[128:129], v[144:145] offset0:0 offset1:16\n"
"v_xor_b32 v239, 32, v233\n"
"ds_write2_b64 v239, v[132:133], v[148:149] offset0:0 offset1:16\n"
"v_xor_b32 v240, 64, v233\n"
"v_cvt_pk_bf16_f32 v153, v154, v155\n"
"v_mfma_f32_16x16x32_bf16 v[64:67], a[128:131], v[208:211], 0\n"
"ds_write2_b64 v240, v[136:137], v[152:153] offset0:0 offset1:16\n"
"v_cvt_pk_bf16_f32 v125, v126, v127\n"
"v_mul_f32 v140, v236, v140\n"
"v_mfma_f32_16x16x32_bf16 v[68:71], a[132:135], v[208:211], 0\n"
"ds_write2_b64 v241, v[108:109], v[124:125] offset0:0 offset1:16\n"
"v_exp_f32 v196, v196\n"
"v_mul_f32 v169, v237, v169\n"
"v_mfma_f32_16x16x32_bf16 v[72:75], a[136:139], v[208:211], 0\n"
"v_mul_f32 v200, s62, v200\n"
"v_mul_f32 v141, v236, v141\n"
"v_mfma_f32_16x16x32_bf16 v[76:79], a[140:143], v[208:211], 0\n"
"v_mul_f32 v197, s62, v197\n"
"v_mul_f32 v170, v237, v170\n"
"v_mfma_f32_16x16x32_bf16 v[80:83], a[144:147], v[208:211], 0\n"
"v_exp_f32 v200, v200\n"
"v_mul_f32 v142, v236, v142\n"
"v_mfma_f32_16x16x32_bf16 v[84:87], a[148:151], v[208:211], 0\n"
"v_exp_f32 v197, v197\n"
"v_mul_f32 v201, s62, v201\n"
"v_mul_f32 v171, v237, v171\n"
"v_cvt_pk_bf16_f32 v168, v168, v169\n"
"v_mfma_f32_16x16x32_bf16 v[88:91], a[152:155], v[208:211], 0\n"
"v_mul_f32 v198, s62, v198\n"
"v_cvt_pk_bf16_f32 v169, v170, v171\n"
"v_mfma_f32_16x16x32_bf16 v[92:95], a[156:159], v[208:211], 0\n"
"v_cvt_pk_bf16_f32 v194, v196, v197\n"
"v_exp_f32 v201, v201\n"
"v_mul_f32 v143, v236, v143\n"
"v_cvt_pk_bf16_f32 v140, v140, v141\n"
"v_mfma_f32_16x16x32_bf16 a[168:171], a[184:187], v[208:211], 0\n"
"v_exp_f32 v198, v198\n"
"v_cvt_pk_bf16_f32 v141, v142, v143\n"
"v_mul_f32 v184, v237, v184\n"
"v_mfma_f32_16x16x32_bf16 v[208:211], a[96:99], a[80:83], 0\n"
"ds_read_b128 a[96:99], v224 offset:8192\n"
"v_mul_f32 v202, s62, v202\n"
"v_cvt_pk_bf16_f32 v200, v200, v201\n"
"v_mul_f32 v156, v236, v156\n"
"v_mfma_f32_16x16x32_bf16 v[208:211], a[100:103], a[84:87], v[208:211]\n"
"v_mul_f32 v199, s62, v199\n"
"v_mul_f32 v185, v237, v185\n"
"v_mfma_f32_16x16x32_bf16 v[208:211], a[104:107], a[88:91], v[208:211]\n"
"ds_read_b128 a[100:103], v224 offset:8704\n"
"v_exp_f32 v202, v202\n"
"v_mul_f32 v157, v236, v157\n"
"v_mfma_f32_16x16x32_bf16 v[208:211], a[108:111], a[92:95], v[208:211]\n"
"v_exp_f32 v199, v199\n"
"v_mul_f32 v203, s62, v203\n"
"v_mul_f32 v186, v237, v186\n"
"v_mfma_f32_16x16x32_bf16 v[212:215], a[112:115], a[80:83], 0\n"
"ds_read_b128 a[104:107], v224 offset:9216\n"
"v_cvt_pk_bf16_f32 v195, v198, v199\n"
"v_mfma_f32_16x16x32_bf16 v[212:215], a[116:119], a[84:87], v[212:215]\n"
"v_exp_f32 v203, v203\n"
"v_mul_f32 v158, v236, v158\n"
"v_mfma_f32_16x16x32_bf16 v[212:215], a[120:123], a[88:91], v[212:215]\n"
"ds_read_b128 a[108:111], v224 offset:9728\n"
"v_mul_f32 v187, v237, v187\n"
"v_cvt_pk_bf16_f32 v184, v184, v185\n"
"v_mfma_f32_16x16x32_bf16 v[212:215], a[124:127], a[92:95], v[212:215]\n"
"v_mul_f32 v204, s62, v204\n"
"v_cvt_pk_bf16_f32 v201, v202, v203\n"
"v_mul_f32 v159, v236, v159\n"
"v_cvt_pk_bf16_f32 v156, v156, v157\n"
"v_xor_b32 v241, 96, v233\n"
"v_add_u32 v233, 20480, v232\n"
"ds_write2_b64 v233, v[160:161], v[176:177] offset0:0 offset1:16\n"
"v_xor_b32 v239, 32, v233\n"
"ds_write2_b64 v239, v[164:165], v[180:181] offset0:0 offset1:16\n"
"v_xor_b32 v240, 64, v233\n"
"v_cvt_pk_bf16_f32 v185, v186, v187\n"
"v_mfma_f32_16x16x32_bf16 v[96:99], a[128:131], v[192:195], 0\n"
"ds_read_b128 a[112:115], v224 offset:12288\n"
"ds_write2_b64 v240, v[168:169], v[184:185] offset0:0 offset1:16\n"
"v_cvt_pk_bf16_f32 v157, v158, v159\n"
"v_mul_f32 v172, v237, v172\n"
"v_mfma_f32_16x16x32_bf16 v[100:103], a[132:135], v[192:195], 0\n"
"ds_write2_b64 v241, v[140:141], v[156:157] offset0:0 offset1:16\n"
"v_exp_f32 v204, v204\n"
"v_mfma_f32_16x16x32_bf16 v[104:107], a[136:139], v[192:195], 0\n"
"ds_read_b128 a[116:119], v224 offset:12800\n"
"v_mul_f32 v208, s62, v208\n"
"v_mul_f32 v173, v237, v173\n"
"v_mfma_f32_16x16x32_bf16 v[108:111], a[140:143], v[192:195], 0\n"
"v_mul_f32 v205, s62, v205\n"
"v_mul_f32 v174, v237, v174\n"
"v_mfma_f32_16x16x32_bf16 v[112:115], a[144:147], v[192:195], 0\n"
"ds_read_b128 a[120:123], v224 offset:13312\n"
"v_exp_f32 v208, v208\n"
"v_mfma_f32_16x16x32_bf16 v[116:119], a[148:151], v[192:195], 0\n"
"v_exp_f32 v205, v205\n"
"v_mul_f32 v209, s62, v209\n"
"v_mfma_f32_16x16x32_bf16 v[120:123], a[152:155], v[192:195], 0\n"
"ds_read_b128 a[124:127], v224 offset:13824\n"
"v_mul_f32 v206, s62, v206\n"
"v_mfma_f32_16x16x32_bf16 v[124:127], a[156:159], v[192:195], 0\n"
"v_cvt_pk_bf16_f32 v202, v204, v205\n"
"v_exp_f32 v209, v209\n"
"v_mul_f32 v175, v237, v175\n"
"v_cvt_pk_bf16_f32 v172, v172, v173\n"
"v_mfma_f32_16x16x32_bf16 a[172:175], a[184:187], v[192:195], 0\n"
"v_exp_f32 v206, v206\n"
"v_cvt_pk_bf16_f32 v173, v174, v175\n"
"s_waitcnt lgkmcnt(2)\n"
"v_mfma_f32_16x16x32_bf16 v[192:195], a[96:99], a[0:3], 0\n"
"v_mul_f32 v210, s62, v210\n"
"v_cvt_pk_bf16_f32 v208, v208, v209\n"
"v_mul_f32 v188, v237, v188\n"
"v_mfma_f32_16x16x32_bf16 v[192:195], a[100:103], a[4:7], v[192:195]\n"
"ds_read_b128 a[188:191], v230 offset:0\n"
"v_mul_f32 v207, s62, v207\n"
"v_mfma_f32_16x16x32_bf16 v[192:195], a[104:107], a[8:11], v[192:195]\n"
"v_exp_f32 v210, v210\n"
"v_mul_f32 v189, v237, v189\n"
"v_mfma_f32_16x16x32_bf16 v[192:195], a[108:111], a[12:15], v[192:195]\n"
"ds_read_b128 a[192:195], v231 offset:1024\n"
"v_exp_f32 v207, v207\n"
"v_mul_f32 v211, s62, v211\n"
"v_mfma_f32_16x16x32_bf16 v[196:199], a[112:115], a[0:3], 0\n"
"v_cvt_pk_bf16_f32 v203, v206, v207\n"
"v_mul_f32 v190, v237, v190\n"
"v_mfma_f32_16x16x32_bf16 v[196:199], a[116:119], a[4:7], v[196:199]\n"
"v_exp_f32 v211, v211\n"
"s_waitcnt lgkmcnt(2)\n"
"v_mfma_f32_16x16x32_bf16 v[196:199], a[120:123], a[8:11], v[196:199]\n"
"v_mul_f32 v212, s62, v212\n"
"v_cvt_pk_bf16_f32 v209, v210, v211\n"
"v_mfma_f32_16x16x32_bf16 v[196:199], a[124:127], a[12:15], v[196:199]\n"
"v_mul_f32 v191, v237, v191\n"
"v_cvt_pk_bf16_f32 v188, v188, v189\n"
"v_xor_b32 v241, 96, v233\n"
"v_mfma_f32_16x16x32_bf16 v[128:131], a[128:131], v[200:203], 0\n"
"v_cvt_pk_bf16_f32 v189, v190, v191\n"
"v_mul_f32 v192, s62, v192\n"
"v_mfma_f32_16x16x32_bf16 v[132:135], a[132:135], v[200:203], 0\n"
"ds_write2_b64 v241, v[172:173], v[188:189] offset0:0 offset1:16\n"
"v_exp_f32 v212, v212\n"
"v_mfma_f32_16x16x32_bf16 v[136:139], a[136:139], v[200:203], 0\n"
"v_mul_f32 v213, s62, v213\n"
"v_mul_f32 v193, s62, v193\n"
"v_mfma_f32_16x16x32_bf16 v[140:143], a[140:143], v[200:203], 0\n"
"v_exp_f32 v192, v192\n"
"v_mfma_f32_16x16x32_bf16 v[144:147], a[144:147], v[200:203], 0\n"
"v_exp_f32 v213, v213\n"
"v_mfma_f32_16x16x32_bf16 v[148:151], a[148:151], v[200:203], 0\n"
"v_mul_f32 v214, s62, v214\n"
"v_cvt_pk_bf16_f32 v210, v212, v213\n"
"v_mfma_f32_16x16x32_bf16 v[152:155], a[152:155], v[200:203], 0\n"
"v_exp_f32 v193, v193\n"
"v_mfma_f32_16x16x32_bf16 v[156:159], a[156:159], v[200:203], 0\n"
"v_exp_f32 v214, v214\n"
"v_mfma_f32_16x16x32_bf16 a[176:179], a[184:187], v[200:203], 0\n"
"v_mul_f32 v194, s62, v194\n"
"v_cvt_pk_bf16_f32 v192, v192, v193\n"
"v_mfma_f32_16x16x32_bf16 v[200:203], a[96:99], a[16:19], 0\n"
"v_mul_f32 v215, s62, v215\n"
"v_mul_f32 v195, s62, v195\n"
"v_mfma_f32_16x16x32_bf16 v[200:203], a[100:103], a[20:23], v[200:203]\n"
"v_exp_f32 v194, v194\n"
"v_mfma_f32_16x16x32_bf16 v[200:203], a[104:107], a[24:27], v[200:203]\n"
"v_exp_f32 v215, v215\n"
"v_mfma_f32_16x16x32_bf16 v[200:203], a[108:111], a[28:31], v[200:203]\n"
"v_cvt_pk_bf16_f32 v211, v214, v215\n"
"v_mul_f32 v196, s62, v196\n"
"v_mfma_f32_16x16x32_bf16 v[204:207], a[112:115], a[16:19], 0\n"
"v_exp_f32 v195, v195\n"
"v_mfma_f32_16x16x32_bf16 v[204:207], a[116:119], a[20:23], v[204:207]\n"
"v_cvt_pk_bf16_f32 v193, v194, v195\n"
"v_mul_f32 v197, s62, v197\n"
"v_mfma_f32_16x16x32_bf16 v[204:207], a[120:123], a[24:27], v[204:207]\n"
"v_exp_f32 v196, v196\n"
"v_mfma_f32_16x16x32_bf16 v[204:207], a[124:127], a[28:31], v[204:207]\n"
"v_mul_f32 v200, s62, v200\n"
"v_mul_f32 v201, s62, v201\n"
"v_mfma_f32_16x16x32_bf16 v[160:163], a[128:131], v[208:211], 0\n"
"ds_read_b64_tr_b16 a[128:129], v228 offset:8192\n"
"v_mul_f32 v198, s62, v198\n"
"v_mfma_f32_16x16x32_bf16 v[164:167], a[132:135], v[208:211], 0\n"
"ds_read_b64_tr_b16 a[130:131], v228 offset:12288\n"
"v_mul_f32 v202, s62, v202\n"
"v_mfma_f32_16x16x32_bf16 v[168:171], a[136:139], v[208:211], 0\n"
"ds_read_b64_tr_b16 a[132:133], v229 offset:8192\n"
"v_mul_f32 v199, s62, v199\n"
"v_mfma_f32_16x16x32_bf16 v[172:175], a[140:143], v[208:211], 0\n"
"ds_read_b64_tr_b16 a[134:135], v229 offset:12288\n"
"v_mul_f32 v203, s62, v203\n"
"v_mfma_f32_16x16x32_bf16 v[176:179], a[144:147], v[208:211], 0\n"
"ds_read_b64_tr_b16 a[136:137], v228 offset:8704\n"
"v_exp_f32 v200, v200\n"
"v_mfma_f32_16x16x32_bf16 v[180:183], a[148:151], v[208:211], 0\n"
"ds_read_b64_tr_b16 a[138:139], v228 offset:12800\n"
"v_exp_f32 v197, v197\n"
"v_mfma_f32_16x16x32_bf16 v[184:187], a[152:155], v[208:211], 0\n"
"ds_read_b64_tr_b16 a[140:141], v229 offset:8704\n"
"v_cvt_pk_bf16_f32 v194, v196, v197\n"
"v_mfma_f32_16x16x32_bf16 v[188:191], a[156:159], v[208:211], 0\n"
"ds_read_b64_tr_b16 a[142:143], v229 offset:12800\n"
"v_exp_f32 v201, v201\n"
"v_mfma_f32_16x16x32_bf16 a[180:183], a[184:187], v[208:211], 0\n"
"ds_read_b64_tr_b16 a[144:145], v228 offset:9216\n"
"v_exp_f32 v198, v198\n"
"v_mfma_f32_16x16x32_bf16 v[208:211], a[96:99], a[32:35], 0\n"
"ds_read_b64_tr_b16 a[146:147], v228 offset:13312\n"
"v_cvt_pk_bf16_f32 v200, v200, v201\n"
"v_mfma_f32_16x16x32_bf16 v[208:211], a[100:103], a[36:39], v[208:211]\n"
"ds_read_b64_tr_b16 a[148:149], v229 offset:9216\n"
"v_mul_f32 v204, s62, v204\n"
"v_mfma_f32_16x16x32_bf16 v[208:211], a[104:107], a[40:43], v[208:211]\n"
"ds_read_b64_tr_b16 a[150:151], v229 offset:13312\n"
"v_exp_f32 v202, v202\n"
"v_mfma_f32_16x16x32_bf16 v[208:211], a[108:111], a[44:47], v[208:211]\n"
"ds_read_b64_tr_b16 a[152:153], v228 offset:9728\n"
"v_exp_f32 v199, v199\n"
"v_mfma_f32_16x16x32_bf16 v[212:215], a[112:115], a[32:35], 0\n"
"ds_read_b64_tr_b16 a[154:155], v228 offset:13824\n"
"v_cvt_pk_bf16_f32 v195, v198, v199\n"
"v_mfma_f32_16x16x32_bf16 v[212:215], a[116:119], a[36:39], v[212:215]\n"
"ds_read_b64_tr_b16 a[156:157], v229 offset:9728\n"
"v_exp_f32 v203, v203\n"
"v_mfma_f32_16x16x32_bf16 v[212:215], a[120:123], a[40:43], v[212:215]\n"
"ds_read_b64_tr_b16 a[158:159], v229 offset:13824\n"
"v_cvt_pk_bf16_f32 v201, v202, v203\n"
"v_mfma_f32_16x16x32_bf16 v[212:215], a[124:127], a[44:47], v[212:215]\n"
"v_mul_f32 v208, s62, v208\n"
"s_waitcnt lgkmcnt(5)\n"
"v_mfma_f32_16x16x32_bf16 v[0:3], a[128:131], v[192:195], v[0:3]\n"
"v_mul_f32 v205, s62, v205\n"
"s_add_u32 s61, s59, 0\n"
"v_mfma_f32_16x16x32_bf16 v[4:7], a[132:135], v[192:195], v[4:7]\n"
"buffer_store_dwordx4 a[188:191], v227, s[48:51], s61 offen sc1 nt\n"
"v_exp_f32 v204, v204\n"
"v_mfma_f32_16x16x32_bf16 v[8:11], a[136:139], v[192:195], v[8:11]\n"
"v_exp_f32 v208, v208\n"
"v_mfma_f32_16x16x32_bf16 v[12:15], a[140:143], v[192:195], v[12:15]\n"
"buffer_store_dwordx4 a[192:195], v227, s[48:51], s61 offen offset:1024 sc1 nt\n"
"v_mul_f32 v209, s62, v209\n"
"v_mfma_f32_16x16x32_bf16 v[16:19], a[144:147], v[192:195], v[16:19]\n"
"v_mul_f32 v206, s62, v206\n"
"s_waitcnt lgkmcnt(0)\n"
"v_mfma_f32_16x16x32_bf16 v[20:23], a[148:151], v[192:195], v[20:23]\n"
"v_exp_f32 v205, v205\n"
"v_mfma_f32_16x16x32_bf16 v[24:27], a[152:155], v[192:195], v[24:27]\n"
"v_cvt_pk_bf16_f32 v202, v204, v205\n"
"v_mul_f32 v210, s62, v210\n"
"v_mfma_f32_16x16x32_bf16 v[28:31], a[156:159], v[192:195], v[28:31]\n"
"v_exp_f32 v209, v209\n"
"v_mfma_f32_16x16x32_bf16 a[160:163], a[184:187], v[192:195], a[160:163]\n"
"v_exp_f32 v206, v206\n"
"v_mfma_f32_16x16x32_bf16 v[192:195], a[96:99], a[48:51], 0\n"
"v_cvt_pk_bf16_f32 v208, v208, v209\n"
"v_mul_f32 v207, s62, v207\n"
"v_mfma_f32_16x16x32_bf16 v[192:195], a[100:103], a[52:55], v[192:195]\n"
"v_exp_f32 v210, v210\n"
"v_mfma_f32_16x16x32_bf16 v[192:195], a[104:107], a[56:59], v[192:195]\n"
"v_exp_f32 v207, v207\n"
"v_mfma_f32_16x16x32_bf16 v[192:195], a[108:111], a[60:63], v[192:195]\n"
"v_mul_f32 v211, s62, v211\n"
"v_cvt_pk_bf16_f32 v203, v206, v207\n"
"v_mfma_f32_16x16x32_bf16 v[196:199], a[112:115], a[48:51], 0\n"
"v_exp_f32 v211, v211\n"
"v_mfma_f32_16x16x32_bf16 v[196:199], a[116:119], a[52:55], v[196:199]\n"
"v_mul_f32 v212, s62, v212\n"
"v_cvt_pk_bf16_f32 v209, v210, v211\n"
"v_mfma_f32_16x16x32_bf16 v[196:199], a[120:123], a[56:59], v[196:199]\n"
"v_exp_f32 v212, v212\n"
"v_mfma_f32_16x16x32_bf16 v[196:199], a[124:127], a[60:63], v[196:199]\n"
"v_mul_f32 v192, s62, v192\n"
"v_mul_f32 v213, s62, v213\n"
"v_mfma_f32_16x16x32_bf16 v[32:35], a[128:131], v[200:203], v[32:35]\n"
"v_exp_f32 v192, v192\n"
"v_mfma_f32_16x16x32_bf16 v[36:39], a[132:135], v[200:203], v[36:39]\n"
"v_exp_f32 v213, v213\n"
"v_mfma_f32_16x16x32_bf16 v[40:43], a[136:139], v[200:203], v[40:43]\n"
"v_mul_f32 v193, s62, v193\n"
"v_mul_f32 v214, s62, v214\n"
"v_mfma_f32_16x16x32_bf16 v[44:47], a[140:143], v[200:203], v[44:47]\n"
"v_cvt_pk_bf16_f32 v210, v212, v213\n"
"v_mul_f32 v194, s62, v194\n"
"v_mfma_f32_16x16x32_bf16 v[48:51], a[144:147], v[200:203], v[48:51]\n"
"s_barrier\n"
"v_mfma_f32_16x16x32_bf16 v[52:55], a[148:151], v[200:203], v[52:55]\n"
"v_exp_f32 v193, v193\n"
"v_mfma_f32_16x16x32_bf16 v[56:59], a[152:155], v[200:203], v[56:59]\n"
"s_add_u32 m0, s56, 8192\n"
"s_add_u32 s72, s57, 139264\n"
"v_mfma_f32_16x16x32_bf16 v[60:63], a[156:159], v[200:203], v[60:63]\n"
"buffer_load_dwordx4 v226, s[40:43], s72 offen nt lds\n"
"v_cvt_pk_bf16_f32 v192, v192, v193\n"
"v_mfma_f32_16x16x32_bf16 a[164:167], a[184:187], v[200:203], a[164:167]\n"
"buffer_load_dwordx4 v226, s[40:43], s72 offen offset:2048 nt lds\n"
"v_exp_f32 v214, v214\n"
"v_mfma_f32_16x16x32_bf16 v[200:203], a[96:99], a[64:67], 0\n"
"v_mul_f32 v215, s62, v215\n"
"s_add_u32 m0, s56, 40960\n"
"v_mfma_f32_16x16x32_bf16 v[200:203], a[100:103], a[68:71], v[200:203]\n"
"buffer_load_dwordx4 v226, s[44:47], s72 offen nt lds\n"
"v_mul_f32 v195, s62, v195\n"
"v_mfma_f32_16x16x32_bf16 v[200:203], a[104:107], a[72:75], v[200:203]\n"
"buffer_load_dwordx4 v226, s[44:47], s72 offen offset:2048 nt lds\n"
"v_exp_f32 v194, v194\n"
"v_mfma_f32_16x16x32_bf16 v[200:203], a[108:111], a[76:79], v[200:203]\n"
"v_exp_f32 v215, v215\n"
"v_mfma_f32_16x16x32_bf16 v[204:207], a[112:115], a[64:67], 0\n"
"v_cvt_pk_bf16_f32 v211, v214, v215\n"
"v_mul_f32 v196, s62, v196\n"
"v_mfma_f32_16x16x32_bf16 v[204:207], a[116:119], a[68:71], v[204:207]\n"
"v_exp_f32 v195, v195\n"
"v_mfma_f32_16x16x32_bf16 v[204:207], a[120:123], a[72:75], v[204:207]\n"
"v_cvt_pk_bf16_f32 v193, v194, v195\n"
"v_mul_f32 v200, s62, v200\n"
"v_mfma_f32_16x16x32_bf16 v[204:207], a[124:127], a[76:79], v[204:207]\n"
"v_exp_f32 v196, v196\n"
"v_mfma_f32_16x16x32_bf16 v[64:67], a[128:131], v[208:211], v[64:67]\n"
"v_mul_f32 v197, s62, v197\n"
"v_mul_f32 v201, s62, v201\n"
"v_mfma_f32_16x16x32_bf16 v[68:71], a[132:135], v[208:211], v[68:71]\n"
"v_exp_f32 v200, v200\n"
"v_mfma_f32_16x16x32_bf16 v[72:75], a[136:139], v[208:211], v[72:75]\n"
"v_exp_f32 v197, v197\n"
"v_mfma_f32_16x16x32_bf16 v[76:79], a[140:143], v[208:211], v[76:79]\n"
"v_mul_f32 v198, s62, v198\n"
"v_cvt_pk_bf16_f32 v194, v196, v197\n"
"v_mfma_f32_16x16x32_bf16 v[80:83], a[144:147], v[208:211], v[80:83]\n"
"v_exp_f32 v201, v201\n"
"v_mfma_f32_16x16x32_bf16 v[84:87], a[148:151], v[208:211], v[84:87]\n"
"v_exp_f32 v198, v198\n"
"v_mfma_f32_16x16x32_bf16 v[88:91], a[152:155], v[208:211], v[88:91]\n"
"v_mul_f32 v202, s62, v202\n"
"v_cvt_pk_bf16_f32 v200, v200, v201\n"
"v_mfma_f32_16x16x32_bf16 v[92:95], a[156:159], v[208:211], v[92:95]\n"
"v_mul_f32 v199, s62, v199\n"
"v_mul_f32 v203, s62, v203\n"
"v_mfma_f32_16x16x32_bf16 a[168:171], a[184:187], v[208:211], a[168:171]\n"
"v_exp_f32 v202, v202\n"
"v_mfma_f32_16x16x32_bf16 v[208:211], a[96:99], a[80:83], 0\n"
"ds_read_b128 a[96:99], v224 offset:16384\n"
"v_mul_f32 v204, s62, v204\n"
"v_mfma_f32_16x16x32_bf16 v[208:211], a[100:103], a[84:87], v[208:211]\n"
"v_exp_f32 v199, v199\n"
"v_mfma_f32_16x16x32_bf16 v[208:211], a[104:107], a[88:91], v[208:211]\n"
"ds_read_b128 a[100:103], v224 offset:16896\n"
"v_cvt_pk_bf16_f32 v195, v198, v199\n"
"v_mfma_f32_16x16x32_bf16 v[208:211], a[108:111], a[92:95], v[208:211]\n"
"v_exp_f32 v203, v203\n"
"v_mfma_f32_16x16x32_bf16 v[212:215], a[112:115], a[80:83], 0\n"
"ds_read_b128 a[104:107], v224 offset:17408\n"
"v_cvt_pk_bf16_f32 v201, v202, v203\n"
"v_mfma_f32_16x16x32_bf16 v[212:215], a[116:119], a[84:87], v[212:215]\n"
"v_exp_f32 v204, v204\n"
"v_mfma_f32_16x16x32_bf16 v[212:215], a[120:123], a[88:91], v[212:215]\n"
"ds_read_b128 a[108:111], v224 offset:17920\n"
"v_mul_f32 v208, s62, v208\n"
"v_mfma_f32_16x16x32_bf16 v[212:215], a[124:127], a[92:95], v[212:215]\n"
"v_mul_f32 v205, s62, v205\n"
"v_mul_f32 v209, s62, v209\n"
"v_mfma_f32_16x16x32_bf16 v[96:99], a[128:131], v[192:195], v[96:99]\n"
"ds_read_b128 a[112:115], v224 offset:20480\n"
"v_mul_f32 v206, s62, v206\n"
"v_mfma_f32_16x16x32_bf16 v[100:103], a[132:135], v[192:195], v[100:103]\n"
"v_exp_f32 v208, v208\n"
"v_mfma_f32_16x16x32_bf16 v[104:107], a[136:139], v[192:195], v[104:107]\n"
"ds_read_b128 a[116:119], v224 offset:20992\n"
"v_mul_f32 v210, s62, v210\n"
"v_mfma_f32_16x16x32_bf16 v[108:111], a[140:143], v[192:195], v[108:111]\n"
"v_exp_f32 v205, v205\n"
"v_mfma_f32_16x16x32_bf16 v[112:115], a[144:147], v[192:195], v[112:115]\n"
"ds_read_b128 a[120:123], v224 offset:21504\n"
"v_cvt_pk_bf16_f32 v202, v204, v205\n"
"v_mfma_f32_16x16x32_bf16 v[116:119], a[148:151], v[192:195], v[116:119]\n"
"v_exp_f32 v209, v209\n"
"v_mfma_f32_16x16x32_bf16 v[120:123], a[152:155], v[192:195], v[120:123]\n"
"ds_read_b128 a[124:127], v224 offset:22016\n"
"v_cvt_pk_bf16_f32 v208, v208, v209\n"
"v_mfma_f32_16x16x32_bf16 v[124:127], a[156:159], v[192:195], v[124:127]\n"
"v_exp_f32 v206, v206\n"
"v_mfma_f32_16x16x32_bf16 a[172:175], a[184:187], v[192:195], a[172:175]\n"
"v_mul_f32 v207, s62, v207\n"
"s_waitcnt lgkmcnt(2)\n"
"v_mfma_f32_16x16x32_bf16 v[192:195], a[96:99], a[0:3], 0\n"
"v_exp_f32 v210, v210\n"
"v_mfma_f32_16x16x32_bf16 v[192:195], a[100:103], a[4:7], v[192:195]\n"
"ds_read_b128 a[196:199], v230 offset:2048\n"
"v_mul_f32 v211, s62, v211\n"
"v_mfma_f32_16x16x32_bf16 v[192:195], a[104:107], a[8:11], v[192:195]\n"
"v_exp_f32 v207, v207\n"
"v_mfma_f32_16x16x32_bf16 v[192:195], a[108:111], a[12:15], v[192:195]\n"
"ds_read_b128 a[200:203], v231 offset:3072\n"
"v_cvt_pk_bf16_f32 v203, v206, v207\n"
"v_mfma_f32_16x16x32_bf16 v[196:199], a[112:115], a[0:3], 0\n"
"v_exp_f32 v211, v211\n"
"v_mfma_f32_16x16x32_bf16 v[196:199], a[116:119], a[4:7], v[196:199]\n"
"v_mul_f32 v212, s62, v212\n"
"s_waitcnt lgkmcnt(2)\n"
"v_mfma_f32_16x16x32_bf16 v[196:199], a[120:123], a[8:11], v[196:199]\n"
"v_cvt_pk_bf16_f32 v209, v210, v211\n"
"v_mul_f32 v192, s62, v192\n"
"v_mfma_f32_16x16x32_bf16 v[196:199], a[124:127], a[12:15], v[196:199]\n"
"v_exp_f32 v212, v212\n"
"v_mfma_f32_16x16x32_bf16 v[128:131], a[128:131], v[200:203], v[128:131]\n"
"v_mul_f32 v213, s62, v213\n"
"v_mul_f32 v193, s62, v193\n"
"v_mfma_f32_16x16x32_bf16 v[132:135], a[132:135], v[200:203], v[132:135]\n"
"v_exp_f32 v192, v192\n"
"v_mfma_f32_16x16x32_bf16 v[136:139], a[136:139], v[200:203], v[136:139]\n"
"v_exp_f32 v213, v213\n"
"v_mfma_f32_16x16x32_bf16 v[140:143], a[140:143], v[200:203], v[140:143]\n"
"v_mul_f32 v214, s62, v214\n"
"v_cvt_pk_bf16_f32 v210, v212, v213\n"
"v_mfma_f32_16x16x32_bf16 v[144:147], a[144:147], v[200:203], v[144:147]\n"
"v_exp_f32 v193, v193\n"
"v_mfma_f32_16x16x32_bf16 v[148:151], a[148:151], v[200:203], v[148:151]\n"
"v_exp_f32 v214, v214\n"
"v_mfma_f32_16x16x32_bf16 v[152:155], a[152:155], v[200:203], v[152:155]\n"
"v_mul_f32 v194, s62, v194\n"
"v_cvt_pk_bf16_f32 v192, v192, v193\n"
"v_mfma_f32_16x16x32_bf16 v[156:159], a[156:159], v[200:203], v[156:159]\n"
"v_mul_f32 v215, s62, v215\n"
"v_mul_f32 v195, s62, v195\n"
"v_mfma_f32_16x16x32_bf16 a[176:179], a[184:187], v[200:203], a[176:179]\n"
"v_exp_f32 v194, v194\n"
"v_mfma_f32_16x16x32_bf16 v[200:203], a[96:99], a[16:19], 0\n"
"v_exp_f32 v215, v215\n"
"v_mfma_f32_16x16x32_bf16 v[200:203], a[100:103], a[20:23], v[200:203]\n"
"v_cvt_pk_bf16_f32 v211, v214, v215\n"
"v_mul_f32 v196, s62, v196\n"
"v_mfma_f32_16x16x32_bf16 v[200:203], a[104:107], a[24:27], v[200:203]\n"
"v_exp_f32 v195, v195\n"
"v_mfma_f32_16x16x32_bf16 v[200:203], a[108:111], a[28:31], v[200:203]\n"
"v_cvt_pk_bf16_f32 v193, v194, v195\n"
"v_mul_f32 v197, s62, v197\n"
"v_mfma_f32_16x16x32_bf16 v[204:207], a[112:115], a[16:19], 0\n"
"v_exp_f32 v196, v196\n"
"v_mfma_f32_16x16x32_bf16 v[204:207], a[116:119], a[20:23], v[204:207]\n"
"v_exp_f32 v197, v197\n"
"v_mfma_f32_16x16x32_bf16 v[204:207], a[120:123], a[24:27], v[204:207]\n"
"v_mul_f32 v198, s62, v198\n"
"v_mul_f32 v200, s62, v200\n"
"v_mfma_f32_16x16x32_bf16 v[204:207], a[124:127], a[28:31], v[204:207]\n"
"v_exp_f32 v200, v200\n"
"v_mfma_f32_16x16x32_bf16 v[160:163], a[128:131], v[208:211], v[160:163]\n"
"ds_read_b64_tr_b16 a[128:129], v228 offset:16384\n"
"v_mul_f32 v201, s62, v201\n"
"v_mfma_f32_16x16x32_bf16 v[164:167], a[132:135], v[208:211], v[164:167]\n"
"ds_read_b64_tr_b16 a[130:131], v228 offset:20480\n"
"v_cvt_pk_bf16_f32 v194, v196, v197\n"
"v_mfma_f32_16x16x32_bf16 v[168:171], a[136:139], v[208:211], v[168:171]\n"
"ds_read_b64_tr_b16 a[132:133], v229 offset:16384\n"
"v_mul_f32 v202, s62, v202\n"
"v_mfma_f32_16x16x32_bf16 v[172:175], a[140:143], v[208:211], v[172:175]\n"
"ds_read_b64_tr_b16 a[134:135], v229 offset:20480\n"
"v_mul_f32 v199, s62, v199\n"
"v_mfma_f32_16x16x32_bf16 v[176:179], a[144:147], v[208:211], v[176:179]\n"
"ds_read_b64_tr_b16 a[136:137], v228 offset:16896\n"
"v_mul_f32 v203, s62, v203\n"
"v_mfma_f32_16x16x32_bf16 v[180:183], a[148:151], v[208:211], v[180:183]\n"
"ds_read_b64_tr_b16 a[138:139], v228 offset:20992\n"
"v_mul_f32 v204, s62, v204\n"
"v_mfma_f32_16x16x32_bf16 v[184:187], a[152:155], v[208:211], v[184:187]\n"
"ds_read_b64_tr_b16 a[140:141], v229 offset:16896\n"
"v_mul_f32 v205, s62, v205\n"
"v_mfma_f32_16x16x32_bf16 v[188:191], a[156:159], v[208:211], v[188:191]\n"
"ds_read_b64_tr_b16 a[142:143], v229 offset:20992\n"
"v_exp_f32 v201, v201\n"
"v_mfma_f32_16x16x32_bf16 a[180:183], a[184:187], v[208:211], a[180:183]\n"
"ds_read_b64_tr_b16 a[144:145], v228 offset:17408\n"
"v_exp_f32 v198, v198\n"
"v_mfma_f32_16x16x32_bf16 v[208:211], a[96:99], a[32:35], 0\n"
"ds_read_b64_tr_b16 a[146:147], v228 offset:21504\n"
"v_cvt_pk_bf16_f32 v200, v200, v201\n"
"v_mfma_f32_16x16x32_bf16 v[208:211], a[100:103], a[36:39], v[208:211]\n"
"ds_read_b64_tr_b16 a[148:149], v229 offset:17408\n"
"v_mul_f32 v206, s62, v206\n"
"v_mfma_f32_16x16x32_bf16 v[208:211], a[104:107], a[40:43], v[208:211]\n"
"ds_read_b64_tr_b16 a[150:151], v229 offset:21504\n"
"v_exp_f32 v202, v202\n"
"v_mfma_f32_16x16x32_bf16 v[208:211], a[108:111], a[44:47], v[208:211]\n"
"ds_read_b64_tr_b16 a[152:153], v228 offset:17920\n"
"v_exp_f32 v199, v199\n"
"v_mfma_f32_16x16x32_bf16 v[212:215], a[112:115], a[32:35], 0\n"
"ds_read_b64_tr_b16 a[154:155], v228 offset:22016\n"
"v_cvt_pk_bf16_f32 v195, v198, v199\n"
"v_mfma_f32_16x16x32_bf16 v[212:215], a[116:119], a[36:39], v[212:215]\n"
"ds_read_b64_tr_b16 a[156:157], v229 offset:17920\n"
"v_exp_f32 v203, v203\n"
"v_mfma_f32_16x16x32_bf16 v[212:215], a[120:123], a[40:43], v[212:215]\n"
"ds_read_b64_tr_b16 a[158:159], v229 offset:22016\n"
"v_cvt_pk_bf16_f32 v201, v202, v203\n"
"v_mfma_f32_16x16x32_bf16 v[212:215], a[124:127], a[44:47], v[212:215]\n"
"v_mul_f32 v208, s62, v208\n"
"s_waitcnt lgkmcnt(5)\n"
"v_mfma_f32_16x16x32_bf16 v[0:3], a[128:131], v[192:195], v[0:3]\n"
"v_exp_f32 v204, v204\n"
"v_mfma_f32_16x16x32_bf16 v[4:7], a[132:135], v[192:195], v[4:7]\n"
"buffer_store_dwordx4 a[196:199], v227, s[48:51], s61 offen offset:2048 sc1 nt\n"
"v_mul_f32 v209, s62, v209\n"
"v_mfma_f32_16x16x32_bf16 v[8:11], a[136:139], v[192:195], v[8:11]\n"
"v_exp_f32 v208, v208\n"
"v_mfma_f32_16x16x32_bf16 v[12:15], a[140:143], v[192:195], v[12:15]\n"
"buffer_store_dwordx4 a[200:203], v227, s[48:51], s61 offen offset:3072 sc1 nt\n"
"v_mul_f32 v210, s62, v210\n"
"v_mfma_f32_16x16x32_bf16 v[16:19], a[144:147], v[192:195], v[16:19]\n"
"v_mul_f32 v207, s62, v207\n"
"s_waitcnt lgkmcnt(0)\n"
"v_mfma_f32_16x16x32_bf16 v[20:23], a[148:151], v[192:195], v[20:23]\n"
"v_exp_f32 v205, v205\n"
"v_mfma_f32_16x16x32_bf16 v[24:27], a[152:155], v[192:195], v[24:27]\n"
"v_cvt_pk_bf16_f32 v202, v204, v205\n"
"v_mul_f32 v211, s62, v211\n"
"v_mfma_f32_16x16x32_bf16 v[28:31], a[156:159], v[192:195], v[28:31]\n"
"v_exp_f32 v209, v209\n"
"v_mfma_f32_16x16x32_bf16 a[160:163], a[184:187], v[192:195], a[160:163]\n"
"v_exp_f32 v206, v206\n"
"v_mfma_f32_16x16x32_bf16 v[192:195], a[96:99], a[48:51], 0\n"
"v_cvt_pk_bf16_f32 v208, v208, v209\n"
"v_mul_f32 v212, s62, v212\n"
"v_mfma_f32_16x16x32_bf16 v[192:195], a[100:103], a[52:55], v[192:195]\n"
"v_exp_f32 v210, v210\n"
"v_mfma_f32_16x16x32_bf16 v[192:195], a[104:107], a[56:59], v[192:195]\n"
"v_exp_f32 v207, v207\n"
"v_mfma_f32_16x16x32_bf16 v[192:195], a[108:111], a[60:63], v[192:195]\n"
"v_cvt_pk_bf16_f32 v203, v206, v207\n"
"v_mul_f32 v213, s62, v213\n"
"v_mfma_f32_16x16x32_bf16 v[196:199], a[112:115], a[48:51], 0\n"
"v_exp_f32 v211, v211\n"
"v_mfma_f32_16x16x32_bf16 v[196:199], a[116:119], a[52:55], v[196:199]\n"
"v_cvt_pk_bf16_f32 v209, v210, v211\n"
"v_mul_f32 v214, s62, v214\n"
"v_mfma_f32_16x16x32_bf16 v[196:199], a[120:123], a[56:59], v[196:199]\n"
"v_exp_f32 v212, v212\n"
"v_mfma_f32_16x16x32_bf16 v[196:199], a[124:127], a[60:63], v[196:199]\n"
"v_mul_f32 v192, s62, v192\n"
"v_mul_f32 v193, s62, v193\n"
"v_mfma_f32_16x16x32_bf16 v[32:35], a[128:131], v[200:203], v[32:35]\n"
"v_exp_f32 v192, v192\n"
"v_mfma_f32_16x16x32_bf16 v[36:39], a[132:135], v[200:203], v[36:39]\n"
"v_exp_f32 v213, v213\n"
"v_mfma_f32_16x16x32_bf16 v[40:43], a[136:139], v[200:203], v[40:43]\n"
"v_cvt_pk_bf16_f32 v210, v212, v213\n"
"v_mul_f32 v194, s62, v194\n"
"v_mfma_f32_16x16x32_bf16 v[44:47], a[140:143], v[200:203], v[44:47]\n"
"v_exp_f32 v193, v193\n"
"v_mfma_f32_16x16x32_bf16 v[48:51], a[144:147], v[200:203], v[48:51]\n"
"s_waitcnt vmcnt(12)\n"
"s_barrier\n"
"v_mfma_f32_16x16x32_bf16 v[52:55], a[148:151], v[200:203], v[52:55]\n"
"v_exp_f32 v214, v214\n"
"v_mfma_f32_16x16x32_bf16 v[56:59], a[152:155], v[200:203], v[56:59]\n"
"s_add_u32 m0, s56, 16384\n"
"s_add_u32 s72, s57, 147456\n"
"v_mfma_f32_16x16x32_bf16 v[60:63], a[156:159], v[200:203], v[60:63]\n"
"buffer_load_dwordx4 v226, s[40:43], s72 offen nt lds\n"
"v_cvt_pk_bf16_f32 v192, v192, v193\n"
"v_mfma_f32_16x16x32_bf16 a[164:167], a[184:187], v[200:203], a[164:167]\n"
"buffer_load_dwordx4 v226, s[40:43], s72 offen offset:2048 nt lds\n"
"v_mul_f32 v215, s62, v215\n"
"v_mfma_f32_16x16x32_bf16 v[200:203], a[96:99], a[64:67], 0\n"
"v_mul_f32 v195, s62, v195\n"
"s_add_u32 m0, s56, 49152\n"
"v_mfma_f32_16x16x32_bf16 v[200:203], a[100:103], a[68:71], v[200:203]\n"
"buffer_load_dwordx4 v226, s[44:47], s72 offen nt lds\n"
"v_mul_f32 v196, s62, v196\n"
"v_mfma_f32_16x16x32_bf16 v[200:203], a[104:107], a[72:75], v[200:203]\n"
"buffer_load_dwordx4 v226, s[44:47], s72 offen offset:2048 nt lds\n"
"v_exp_f32 v194, v194\n"
"v_mfma_f32_16x16x32_bf16 v[200:203], a[108:111], a[76:79], v[200:203]\n"
"v_exp_f32 v215, v215\n"
"v_mfma_f32_16x16x32_bf16 v[204:207], a[112:115], a[64:67], 0\n"
"v_cvt_pk_bf16_f32 v211, v214, v215\n"
"v_mul_f32 v197, s62, v197\n"
"v_mfma_f32_16x16x32_bf16 v[204:207], a[116:119], a[68:71], v[204:207]\n"
"v_exp_f32 v195, v195\n"
"v_mfma_f32_16x16x32_bf16 v[204:207], a[120:123], a[72:75], v[204:207]\n"
"v_cvt_pk_bf16_f32 v193, v194, v195\n"
"v_mul_f32 v200, s62, v200\n"
"v_mfma_f32_16x16x32_bf16 v[204:207], a[124:127], a[76:79], v[204:207]\n"
"v_exp_f32 v196, v196\n"
"v_mfma_f32_16x16x32_bf16 v[64:67], a[128:131], v[208:211], v[64:67]\n"
"v_exp_f32 v200, v200\n"
"v_mfma_f32_16x16x32_bf16 v[68:71], a[132:135], v[208:211], v[68:71]\n"
"v_exp_f32 v197, v197\n"
"v_mfma_f32_16x16x32_bf16 v[72:75], a[136:139], v[208:211], v[72:75]\n"
"v_mul_f32 v201, s62, v201\n"
"v_mul_f32 v198, s62, v198\n"
"v_mfma_f32_16x16x32_bf16 v[76:79], a[140:143], v[208:211], v[76:79]\n"
"v_cvt_pk_bf16_f32 v194, v196, v197\n"
"v_mul_f32 v202, s62, v202\n"
"v_mfma_f32_16x16x32_bf16 v[80:83], a[144:147], v[208:211], v[80:83]\n"
"v_exp_f32 v201, v201\n"
"v_mfma_f32_16x16x32_bf16 v[84:87], a[148:151], v[208:211], v[84:87]\n"
"v_exp_f32 v198, v198\n"
"v_mfma_f32_16x16x32_bf16 v[88:91], a[152:155], v[208:211], v[88:91]\n"
"v_cvt_pk_bf16_f32 v200, v200, v201\n"
"v_mul_f32 v199, s62, v199\n"
"v_mfma_f32_16x16x32_bf16 v[92:95], a[156:159], v[208:211], v[92:95]\n"
"v_exp_f32 v202, v202\n"
"v_mfma_f32_16x16x32_bf16 a[168:171], a[184:187], v[208:211], a[168:171]\n"
"v_exp_f32 v199, v199\n"
"v_mfma_f32_16x16x32_bf16 v[208:211], a[96:99], a[80:83], 0\n"
"ds_read_b128 a[96:99], v224 offset:24576\n"
"v_mul_f32 v203, s62, v203\n"
"v_mfma_f32_16x16x32_bf16 v[208:211], a[100:103], a[84:87], v[208:211]\n"
"v_cvt_pk_bf16_f32 v195, v198, v199\n"
"v_mul_f32 v204, s62, v204\n"
"v_mfma_f32_16x16x32_bf16 v[208:211], a[104:107], a[88:91], v[208:211]\n"
"ds_read_b128 a[100:103], v224 offset:25088\n"
"v_mul_f32 v205, s62, v205\n"
"v_mfma_f32_16x16x32_bf16 v[208:211], a[108:111], a[92:95], v[208:211]\n"
"v_exp_f32 v203, v203\n"
"v_mfma_f32_16x16x32_bf16 v[212:215], a[112:115], a[80:83], 0\n"
"ds_read_b128 a[104:107], v224 offset:25600\n"
"v_cvt_pk_bf16_f32 v201, v202, v203\n"
"v_mfma_f32_16x16x32_bf16 v[212:215], a[116:119], a[84:87], v[212:215]\n"
"v_exp_f32 v204, v204\n"
"v_mfma_f32_16x16x32_bf16 v[212:215], a[120:123], a[88:91], v[212:215]\n"
"ds_read_b128 a[108:111], v224 offset:26112\n"
"v_mul_f32 v208, s62, v208\n"
"v_mfma_f32_16x16x32_bf16 v[212:215], a[124:127], a[92:95], v[212:215]\n"
"v_exp_f32 v208, v208\n"
"v_mfma_f32_16x16x32_bf16 v[96:99], a[128:131], v[192:195], v[96:99]\n"
"ds_read_b128 a[112:115], v224 offset:28672\n"
"v_mul_f32 v209, s62, v209\n"
"v_mfma_f32_16x16x32_bf16 v[100:103], a[132:135], v[192:195], v[100:103]\n"
"v_exp_f32 v205, v205\n"
"v_mfma_f32_16x16x32_bf16 v[104:107], a[136:139], v[192:195], v[104:107]\n"
"ds_read_b128 a[116:119], v224 offset:29184\n"
"v_mul_f32 v206, s62, v206\n"
"v_mfma_f32_16x16x32_bf16 v[108:111], a[140:143], v[192:195], v[108:111]\n"
"v_cvt_pk_bf16_f32 v202, v204, v205\n"
"v_mul_f32 v210, s62, v210\n"
"v_mfma_f32_16x16x32_bf16 v[112:115], a[144:147], v[192:195], v[112:115]\n"
"ds_read_b128 a[120:123], v224 offset:29696\n"
"v_mul_f32 v207, s62, v207\n"
"v_mfma_f32_16x16x32_bf16 v[116:119], a[148:151], v[192:195], v[116:119]\n"
"v_exp_f32 v209, v209\n"
"v_mfma_f32_16x16x32_bf16 v[120:123], a[152:155], v[192:195], v[120:123]\n"
"ds_read_b128 a[124:127], v224 offset:30208\n"
"v_cvt_pk_bf16_f32 v208, v208, v209\n"
"v_mfma_f32_16x16x32_bf16 v[124:127], a[156:159], v[192:195], v[124:127]\n"
"v_exp_f32 v206, v206\n"
"v_mfma_f32_16x16x32_bf16 a[172:175], a[184:187], v[192:195], a[172:175]\n"
"v_mul_f32 v211, s62, v211\n"
"s_waitcnt lgkmcnt(2)\n"
"v_mfma_f32_16x16x32_bf16 v[192:195], a[96:99], a[0:3], 0\n"
"v_exp_f32 v210, v210\n"
"v_mfma_f32_16x16x32_bf16 v[192:195], a[100:103], a[4:7], v[192:195]\n"
"ds_read_b128 a[204:207], v230 offset:4096\n"
"v_mul_f32 v212, s62, v212\n"
"v_mfma_f32_16x16x32_bf16 v[192:195], a[104:107], a[8:11], v[192:195]\n"
"v_exp_f32 v207, v207\n"
"v_mfma_f32_16x16x32_bf16 v[192:195], a[108:111], a[12:15], v[192:195]\n"
"ds_read_b128 a[208:211], v231 offset:5120\n"
"v_cvt_pk_bf16_f32 v203, v206, v207\n"
"v_mfma_f32_16x16x32_bf16 v[196:199], a[112:115], a[0:3], 0\n"
"v_exp_f32 v211, v211\n"
"v_mfma_f32_16x16x32_bf16 v[196:199], a[116:119], a[4:7], v[196:199]\n"
"v_cvt_pk_bf16_f32 v209, v210, v211\n"
"s_waitcnt lgkmcnt(2)\n"
"v_mfma_f32_16x16x32_bf16 v[196:199], a[120:123], a[8:11], v[196:199]\n"
"v_exp_f32 v212, v212\n"
"v_mfma_f32_16x16x32_bf16 v[196:199], a[124:127], a[12:15], v[196:199]\n"
"v_mul_f32 v192, s62, v192\n"
"v_mul_f32 v213, s62, v213\n"
"v_mfma_f32_16x16x32_bf16 v[128:131], a[128:131], v[200:203], v[128:131]\n"
"v_exp_f32 v192, v192\n"
"v_mfma_f32_16x16x32_bf16 v[132:135], a[132:135], v[200:203], v[132:135]\n"
"v_exp_f32 v213, v213\n"
"v_mfma_f32_16x16x32_bf16 v[136:139], a[136:139], v[200:203], v[136:139]\n"
"v_mul_f32 v193, s62, v193\n"
"v_mul_f32 v214, s62, v214\n"
"v_mfma_f32_16x16x32_bf16 v[140:143], a[140:143], v[200:203], v[140:143]\n"
"v_cvt_pk_bf16_f32 v210, v212, v213\n"
"v_mul_f32 v194, s62, v194\n"
"v_mfma_f32_16x16x32_bf16 v[144:147], a[144:147], v[200:203], v[144:147]\n"
"v_exp_f32 v193, v193\n"
"v_mfma_f32_16x16x32_bf16 v[148:151], a[148:151], v[200:203], v[148:151]\n"
"v_exp_f32 v214, v214\n"
"v_mfma_f32_16x16x32_bf16 v[152:155], a[152:155], v[200:203], v[152:155]\n"
"v_cvt_pk_bf16_f32 v192, v192, v193\n"
"v_mul_f32 v215, s62, v215\n"
"v_mfma_f32_16x16x32_bf16 v[156:159], a[156:159], v[200:203], v[156:159]\n"
"v_exp_f32 v194, v194\n"
"v_mfma_f32_16x16x32_bf16 a[176:179], a[184:187], v[200:203], a[176:179]\n"
"v_exp_f32 v215, v215\n"
"v_mfma_f32_16x16x32_bf16 v[200:203], a[96:99], a[16:19], 0\n"
"v_mul_f32 v195, s62, v195\n"
"v_cvt_pk_bf16_f32 v211, v214, v215\n"
"v_mfma_f32_16x16x32_bf16 v[200:203], a[100:103], a[20:23], v[200:203]\n"
"v_exp_f32 v195, v195\n"
"v_mfma_f32_16x16x32_bf16 v[200:203], a[104:107], a[24:27], v[200:203]\n"
"v_mul_f32 v196, s62, v196\n"
"v_cvt_pk_bf16_f32 v193, v194, v195\n"
"v_mfma_f32_16x16x32_bf16 v[200:203], a[108:111], a[28:31], v[200:203]\n"
"v_exp_f32 v196, v196\n"
"v_mfma_f32_16x16x32_bf16 v[204:207], a[112:115], a[16:19], 0\n"
"v_mul_f32 v197, s62, v197\n"
"v_mul_f32 v198, s62, v198\n"
"v_mfma_f32_16x16x32_bf16 v[204:207], a[116:119], a[20:23], v[204:207]\n"
"v_exp_f32 v197, v197\n"
"v_mfma_f32_16x16x32_bf16 v[204:207], a[120:123], a[24:27], v[204:207]\n"
"v_cvt_pk_bf16_f32 v194, v196, v197\n"
"v_mul_f32 v200, s62, v200\n"
"v_mfma_f32_16x16x32_bf16 v[204:207], a[124:127], a[28:31], v[204:207]\n"
"v_exp_f32 v200, v200\n"
"v_mfma_f32_16x16x32_bf16 v[160:163], a[128:131], v[208:211], v[160:163]\n"
"ds_read_b64_tr_b16 a[128:129], v228 offset:24576\n"
"v_mul_f32 v201, s62, v201\n"
"v_mfma_f32_16x16x32_bf16 v[164:167], a[132:135], v[208:211], v[164:167]\n"
"ds_read_b64_tr_b16 a[130:131], v228 offset:28672\n"
"v_mul_f32 v202, s62, v202\n"
"v_mfma_f32_16x16x32_bf16 v[168:171], a[136:139], v[208:211], v[168:171]\n"
"ds_read_b64_tr_b16 a[132:133], v229 offset:24576\n"
"v_mul_f32 v199, s62, v199\n"
"v_mfma_f32_16x16x32_bf16 v[172:175], a[140:143], v[208:211], v[172:175]\n"
"ds_read_b64_tr_b16 a[134:135], v229 offset:28672\n"
"v_mul_f32 v203, s62, v203\n"
"v_mfma_f32_16x16x32_bf16 v[176:179], a[144:147], v[208:211], v[176:179]\n"
"ds_read_b64_tr_b16 a[136:137], v228 offset:25088\n"
"v_mul_f32 v204, s62, v204\n"
"v_mfma_f32_16x16x32_bf16 v[180:183], a[148:151], v[208:211], v[180:183]\n"
"ds_read_b64_tr_b16 a[138:139], v228 offset:29184\n"
"v_mul_f32 v205, s62, v205\n"
"v_mfma_f32_16x16x32_bf16 v[184:187], a[152:155], v[208:211], v[184:187]\n"
"ds_read_b64_tr_b16 a[140:141], v229 offset:25088\n"
"v_mul_f32 v206, s62, v206\n"
"v_mfma_f32_16x16x32_bf16 v[188:191], a[156:159], v[208:211], v[188:191]\n"
"ds_read_b64_tr_b16 a[142:143], v229 offset:29184\n"
"v_exp_f32 v201, v201\n"
"v_mfma_f32_16x16x32_bf16 a[180:183], a[184:187], v[208:211], a[180:183]\n"
"ds_read_b64_tr_b16 a[144:145], v228 offset:25600\n"
"v_exp_f32 v198, v198\n"
"v_mfma_f32_16x16x32_bf16 v[208:211], a[96:99], a[32:35], 0\n"
"ds_read_b64_tr_b16 a[146:147], v228 offset:29696\n"
"v_cvt_pk_bf16_f32 v200, v200, v201\n"
"v_mfma_f32_16x16x32_bf16 v[208:211], a[100:103], a[36:39], v[208:211]\n"
"ds_read_b64_tr_b16 a[148:149], v229 offset:25600\n"
"v_mul_f32 v207, s62, v207\n"
"v_mfma_f32_16x16x32_bf16 v[208:211], a[104:107], a[40:43], v[208:211]\n"
"ds_read_b64_tr_b16 a[150:151], v229 offset:29696\n"
"v_exp_f32 v202, v202\n"
"v_mfma_f32_16x16x32_bf16 v[208:211], a[108:111], a[44:47], v[208:211]\n"
"ds_read_b64_tr_b16 a[152:153], v228 offset:26112\n"
"v_exp_f32 v199, v199\n"
"v_mfma_f32_16x16x32_bf16 v[212:215], a[112:115], a[32:35], 0\n"
"ds_read_b64_tr_b16 a[154:155], v228 offset:30208\n"
"v_cvt_pk_bf16_f32 v195, v198, v199\n"
"v_mfma_f32_16x16x32_bf16 v[212:215], a[116:119], a[36:39], v[212:215]\n"
"ds_read_b64_tr_b16 a[156:157], v229 offset:26112\n"
"v_exp_f32 v203, v203\n"
"v_mfma_f32_16x16x32_bf16 v[212:215], a[120:123], a[40:43], v[212:215]\n"
"ds_read_b64_tr_b16 a[158:159], v229 offset:30208\n"
"v_cvt_pk_bf16_f32 v201, v202, v203\n"
"v_mfma_f32_16x16x32_bf16 v[212:215], a[124:127], a[44:47], v[212:215]\n"
"v_mul_f32 v208, s62, v208\n"
"s_waitcnt lgkmcnt(5)\n"
"v_mfma_f32_16x16x32_bf16 v[0:3], a[128:131], v[192:195], v[0:3]\n"
"v_mul_f32 v209, s62, v209\n"
"s_add_u32 s61, s59, 4096\n"
"v_mfma_f32_16x16x32_bf16 v[4:7], a[132:135], v[192:195], v[4:7]\n"
"buffer_store_dwordx4 a[204:207], v227, s[48:51], s61 offen sc1 nt\n"
"v_exp_f32 v204, v204\n"
"v_mfma_f32_16x16x32_bf16 v[8:11], a[136:139], v[192:195], v[8:11]\n"
"v_exp_f32 v208, v208\n"
"v_mfma_f32_16x16x32_bf16 v[12:15], a[140:143], v[192:195], v[12:15]\n"
"buffer_store_dwordx4 a[208:211], v227, s[48:51], s61 offen offset:1024 sc1 nt\n"
"v_mul_f32 v210, s62, v210\n"
"v_mfma_f32_16x16x32_bf16 v[16:19], a[144:147], v[192:195], v[16:19]\n"
"v_mul_f32 v211, s62, v211\n"
"s_waitcnt lgkmcnt(0)\n"
"v_mfma_f32_16x16x32_bf16 v[20:23], a[148:151], v[192:195], v[20:23]\n"
"v_exp_f32 v205, v205\n"
"v_mfma_f32_16x16x32_bf16 v[24:27], a[152:155], v[192:195], v[24:27]\n"
"v_cvt_pk_bf16_f32 v202, v204, v205\n"
"v_mul_f32 v212, s62, v212\n"
"v_mfma_f32_16x16x32_bf16 v[28:31], a[156:159], v[192:195], v[28:31]\n"
"v_exp_f32 v209, v209\n"
"v_mfma_f32_16x16x32_bf16 a[160:163], a[184:187], v[192:195], a[160:163]\n"
"v_exp_f32 v206, v206\n"
"v_mfma_f32_16x16x32_bf16 v[192:195], a[96:99], a[48:51], 0\n"
"v_cvt_pk_bf16_f32 v208, v208, v209\n"
"v_mul_f32 v213, s62, v213\n"
"v_mfma_f32_16x16x32_bf16 v[192:195], a[100:103], a[52:55], v[192:195]\n"
"v_exp_f32 v210, v210\n"
"v_mfma_f32_16x16x32_bf16 v[192:195], a[104:107], a[56:59], v[192:195]\n"
"v_exp_f32 v207, v207\n"
"v_mfma_f32_16x16x32_bf16 v[192:195], a[108:111], a[60:63], v[192:195]\n"
"v_cvt_pk_bf16_f32 v203, v206, v207\n"
"v_mul_f32 v214, s62, v214\n"
"v_mfma_f32_16x16x32_bf16 v[196:199], a[112:115], a[48:51], 0\n"
"v_exp_f32 v211, v211\n"
"v_mfma_f32_16x16x32_bf16 v[196:199], a[116:119], a[52:55], v[196:199]\n"
"v_cvt_pk_bf16_f32 v209, v210, v211\n"
"v_mul_f32 v215, s62, v215\n"
"v_mfma_f32_16x16x32_bf16 v[196:199], a[120:123], a[56:59], v[196:199]\n"
"v_exp_f32 v212, v212\n"
"v_mfma_f32_16x16x32_bf16 v[196:199], a[124:127], a[60:63], v[196:199]\n"
"v_mul_f32 v192, s62, v192\n"
"v_mul_f32 v193, s62, v193\n"
"v_mfma_f32_16x16x32_bf16 v[32:35], a[128:131], v[200:203], v[32:35]\n"
"v_exp_f32 v192, v192\n"
"v_mfma_f32_16x16x32_bf16 v[36:39], a[132:135], v[200:203], v[36:39]\n"
"v_exp_f32 v213, v213\n"
"v_mfma_f32_16x16x32_bf16 v[40:43], a[136:139], v[200:203], v[40:43]\n"
"v_cvt_pk_bf16_f32 v210, v212, v213\n"
"v_mul_f32 v194, s62, v194\n"
"v_mfma_f32_16x16x32_bf16 v[44:47], a[140:143], v[200:203], v[44:47]\n"
"v_exp_f32 v193, v193\n"
"v_mfma_f32_16x16x32_bf16 v[48:51], a[144:147], v[200:203], v[48:51]\n"
"s_waitcnt vmcnt(14)\n"
"s_barrier\n"
"v_mfma_f32_16x16x32_bf16 v[52:55], a[148:151], v[200:203], v[52:55]\n"
"v_exp_f32 v214, v214\n"
"v_mfma_f32_16x16x32_bf16 v[56:59], a[152:155], v[200:203], v[56:59]\n"
"s_add_u32 m0, s56, 24576\n"
"s_add_u32 s72, s57, 155648\n"
"v_mfma_f32_16x16x32_bf16 v[60:63], a[156:159], v[200:203], v[60:63]\n"
"buffer_load_dwordx4 v226, s[40:43], s72 offen nt lds\n"
"v_cvt_pk_bf16_f32 v192, v192, v193\n"
"v_mfma_f32_16x16x32_bf16 a[164:167], a[184:187], v[200:203], a[164:167]\n"
"buffer_load_dwordx4 v226, s[40:43], s72 offen offset:2048 nt lds\n"
"v_mul_f32 v195, s62, v195\n"
"v_mfma_f32_16x16x32_bf16 v[200:203], a[96:99], a[64:67], 0\n"
"v_mul_f32 v196, s62, v196\n"
"s_add_u32 m0, s56, 57344\n"
"v_mfma_f32_16x16x32_bf16 v[200:203], a[100:103], a[68:71], v[200:203]\n"
"buffer_load_dwordx4 v226, s[44:47], s72 offen nt lds\n"
"v_mul_f32 v197, s62, v197\n"
"v_mfma_f32_16x16x32_bf16 v[200:203], a[104:107], a[72:75], v[200:203]\n"
"buffer_load_dwordx4 v226, s[44:47], s72 offen offset:2048 nt lds\n"
"v_exp_f32 v194, v194\n"
"v_mfma_f32_16x16x32_bf16 v[200:203], a[108:111], a[76:79], v[200:203]\n"
"v_exp_f32 v215, v215\n"
"v_mfma_f32_16x16x32_bf16 v[204:207], a[112:115], a[64:67], 0\n"
"v_cvt_pk_bf16_f32 v211, v214, v215\n"
"v_mul_f32 v198, s62, v198\n"
"v_mfma_f32_16x16x32_bf16 v[204:207], a[116:119], a[68:71], v[204:207]\n"
"v_exp_f32 v195, v195\n"
"v_mfma_f32_16x16x32_bf16 v[204:207], a[120:123], a[72:75], v[204:207]\n"
"v_cvt_pk_bf16_f32 v193, v194, v195\n"
"v_mul_f32 v200, s62, v200\n"
"v_mfma_f32_16x16x32_bf16 v[204:207], a[124:127], a[76:79], v[204:207]\n"
"v_exp_f32 v196, v196\n"
"v_mfma_f32_16x16x32_bf16 v[64:67], a[128:131], v[208:211], v[64:67]\n"
"v_exp_f32 v200, v200\n"
"v_mfma_f32_16x16x32_bf16 v[68:71], a[132:135], v[208:211], v[68:71]\n"
"v_exp_f32 v197, v197\n"
"v_mfma_f32_16x16x32_bf16 v[72:75], a[136:139], v[208:211], v[72:75]\n"
"v_mul_f32 v201, s62, v201\n"
"v_cvt_pk_bf16_f32 v194, v196, v197\n"
"v_mfma_f32_16x16x32_bf16 v[76:79], a[140:143], v[208:211], v[76:79]\n"
"v_exp_f32 v201, v201\n"
"v_mfma_f32_16x16x32_bf16 v[80:83], a[144:147], v[208:211], v[80:83]\n"
"v_exp_f32 v198, v198\n"
"v_mfma_f32_16x16x32_bf16 v[84:87], a[148:151], v[208:211], v[84:87]\n"
"v_mul_f32 v202, s62, v202\n"
"v_cvt_pk_bf16_f32 v200, v200, v201\n"
"v_mfma_f32_16x16x32_bf16 v[88:91], a[152:155], v[208:211], v[88:91]\n"
"v_mul_f32 v199, s62, v199\n"
"v_mul_f32 v203, s62, v203\n"
"v_mfma_f32_16x16x32_bf16 v[92:95], a[156:159], v[208:211], v[92:95]\n"
"v_exp_f32 v202, v202\n"
"v_mfma_f32_16x16x32_bf16 a[168:171], a[184:187], v[208:211], a[168:171]\n"
"v_exp_f32 v199, v199\n"
"v_mfma_f32_16x16x32_bf16 v[208:211], a[96:99], a[80:83], 0\n"
"ds_read_b128 a[96:99], v224 offset:0\n"
"v_cvt_pk_bf16_f32 v195, v198, v199\n"
"v_mfma_f32_16x16x32_bf16 v[208:211], a[100:103], a[84:87], v[208:211]\n"
"v_exp_f32 v203, v203\n"
"v_mfma_f32_16x16x32_bf16 v[208:211], a[104:107], a[88:91], v[208:211]\n"
"ds_read_b128 a[100:103], v224 offset:512\n"
"v_mul_f32 v204, s62, v204\n"
"v_mfma_f32_16x16x32_bf16 v[208:211], a[108:111], a[92:95], v[208:211]\n"
"v_cvt_pk_bf16_f32 v201, v202, v203\n"
"v_mul_f32 v205, s62, v205\n"
"v_mfma_f32_16x16x32_bf16 v[212:215], a[112:115], a[80:83], 0\n"
"ds_read_b128 a[104:107], v224 offset:1024\n"
"v_mul_f32 v206, s62, v206\n"
"v_mfma_f32_16x16x32_bf16 v[212:215], a[116:119], a[84:87], v[212:215]\n"
"v_exp_f32 v204, v204\n"
"v_mfma_f32_16x16x32_bf16 v[212:215], a[120:123], a[88:91], v[212:215]\n"
"ds_read_b128 a[108:111], v224 offset:1536\n"
"v_mul_f32 v208, s62, v208\n"
"v_mfma_f32_16x16x32_bf16 v[212:215], a[124:127], a[92:95], v[212:215]\n"
"v_exp_f32 v208, v208\n"
"v_mfma_f32_16x16x32_bf16 v[96:99], a[128:131], v[192:195], v[96:99]\n"
"ds_read_b128 a[112:115], v224 offset:4096\n"
"v_mul_f32 v209, s62, v209\n"
"v_mfma_f32_16x16x32_bf16 v[100:103], a[132:135], v[192:195], v[100:103]\n"
"v_exp_f32 v205, v205\n"
"v_mfma_f32_16x16x32_bf16 v[104:107], a[136:139], v[192:195], v[104:107]\n"
"ds_read_b128 a[116:119], v224 offset:4608\n"
"v_cvt_pk_bf16_f32 v202, v204, v205\n"
"v_mfma_f32_16x16x32_bf16 v[108:111], a[140:143], v[192:195], v[108:111]\n"
"v_exp_f32 v209, v209\n"
"v_mfma_f32_16x16x32_bf16 v[112:115], a[144:147], v[192:195], v[112:115]\n"
"ds_read_b128 a[120:123], v224 offset:5120\n"
"v_mul_f32 v210, s62, v210\n"
"v_mfma_f32_16x16x32_bf16 v[116:119], a[148:151], v[192:195], v[116:119]\n"
"v_exp_f32 v206, v206\n"
"v_mfma_f32_16x16x32_bf16 v[120:123], a[152:155], v[192:195], v[120:123]\n"
"ds_read_b128 a[124:127], v224 offset:5632\n"
"v_cvt_pk_bf16_f32 v208, v208, v209\n"
"v_mfma_f32_16x16x32_bf16 v[124:127], a[156:159], v[192:195], v[124:127]\n"
"v_mul_f32 v207, s62, v207\n"
"v_mul_f32 v211, s62, v211\n"
"v_mfma_f32_16x16x32_bf16 a[172:175], a[184:187], v[192:195], a[172:175]\n"
"v_mul_f32 v212, s62, v212\n"
"s_waitcnt lgkmcnt(2)\n"
"v_mfma_f32_16x16x32_bf16 v[192:195], a[96:99], a[0:3], 0\n"
"v_exp_f32 v210, v210\n"
"v_mfma_f32_16x16x32_bf16 v[192:195], a[100:103], a[4:7], v[192:195]\n"
"ds_read_b128 a[212:215], v230 offset:6144\n"
"v_mul_f32 v213, s62, v213\n"
"v_mfma_f32_16x16x32_bf16 v[192:195], a[104:107], a[8:11], v[192:195]\n"
"v_exp_f32 v207, v207\n"
"v_mfma_f32_16x16x32_bf16 v[192:195], a[108:111], a[12:15], v[192:195]\n"
"ds_read_b128 a[216:219], v231 offset:7168\n"
"v_cvt_pk_bf16_f32 v203, v206, v207\n"
"v_mfma_f32_16x16x32_bf16 v[196:199], a[112:115], a[0:3], 0\n"
"v_exp_f32 v211, v211\n"
"v_mfma_f32_16x16x32_bf16 v[196:199], a[116:119], a[4:7], v[196:199]\n"
"v_cvt_pk_bf16_f32 v209, v210, v211\n"
"s_waitcnt lgkmcnt(2)\n"
"v_mfma_f32_16x16x32_bf16 v[196:199], a[120:123], a[8:11], v[196:199]\n"
"v_exp_f32 v212, v212\n"
"v_mfma_f32_16x16x32_bf16 v[196:199], a[124:127], a[12:15], v[196:199]\n"
"v_mul_f32 v192, s62, v192\n"
"v_mul_f32 v193, s62, v193\n"
"v_mfma_f32_16x16x32_bf16 v[128:131], a[128:131], v[200:203], v[128:131]\n"
"v_exp_f32 v192, v192\n"
"v_mfma_f32_16x16x32_bf16 v[132:135], a[132:135], v[200:203], v[132:135]\n"
"v_exp_f32 v213, v213\n"
"v_mfma_f32_16x16x32_bf16 v[136:139], a[136:139], v[200:203], v[136:139]\n"
"v_mul_f32 v214, s62, v214\n"
"v_cvt_pk_bf16_f32 v210, v212, v213\n"
"v_mfma_f32_16x16x32_bf16 v[140:143], a[140:143], v[200:203], v[140:143]\n"
"v_exp_f32 v193, v193\n"
"v_mfma_f32_16x16x32_bf16 v[144:147], a[144:147], v[200:203], v[144:147]\n"
"v_exp_f32 v214, v214\n"
"v_mfma_f32_16x16x32_bf16 v[148:151], a[148:151], v[200:203], v[148:151]\n"
"v_mul_f32 v194, s62, v194\n"
"v_cvt_pk_bf16_f32 v192, v192, v193\n"
"v_mfma_f32_16x16x32_bf16 v[152:155], a[152:155], v[200:203], v[152:155]\n"
"v_mul_f32 v215, s62, v215\n"
"v_mul_f32 v195, s62, v195\n"
"v_mfma_f32_16x16x32_bf16 v[156:159], a[156:159], v[200:203], v[156:159]\n"
"v_exp_f32 v194, v194\n"
"v_mfma_f32_16x16x32_bf16 a[176:179], a[184:187], v[200:203], a[176:179]\n"
"v_exp_f32 v215, v215\n"
"v_mfma_f32_16x16x32_bf16 v[200:203], a[96:99], a[16:19], 0\n"
"v_cvt_pk_bf16_f32 v211, v214, v215\n"
"v_mul_f32 v196, s62, v196\n"
"v_mfma_f32_16x16x32_bf16 v[200:203], a[100:103], a[20:23], v[200:203]\n"
"v_exp_f32 v195, v195\n"
"v_mfma_f32_16x16x32_bf16 v[200:203], a[104:107], a[24:27], v[200:203]\n"
"v_cvt_pk_bf16_f32 v193, v194, v195\n"
"v_mul_f32 v197, s62, v197\n"
"v_mfma_f32_16x16x32_bf16 v[200:203], a[108:111], a[28:31], v[200:203]\n"
"v_exp_f32 v196, v196\n"
"v_mfma_f32_16x16x32_bf16 v[204:207], a[112:115], a[16:19], 0\n"
"v_exp_f32 v197, v197\n"
"v_mfma_f32_16x16x32_bf16 v[204:207], a[116:119], a[20:23], v[204:207]\n"
"v_mul_f32 v198, s62, v198\n"
"v_cvt_pk_bf16_f32 v194, v196, v197\n"
"v_mfma_f32_16x16x32_bf16 v[204:207], a[120:123], a[24:27], v[204:207]\n"
"v_exp_f32 v198, v198\n"
"v_mfma_f32_16x16x32_bf16 v[204:207], a[124:127], a[28:31], v[204:207]\n"
"v_mul_f32 v200, s62, v200\n"
"v_mul_f32 v201, s62, v201\n"
"v_mfma_f32_16x16x32_bf16 v[160:163], a[128:131], v[208:211], v[160:163]\n"
"ds_read_b64_tr_b16 a[128:129], v228 offset:0\n"
"v_mul_f32 v202, s62, v202\n"
"v_mfma_f32_16x16x32_bf16 v[164:167], a[132:135], v[208:211], v[164:167]\n"
"ds_read_b64_tr_b16 a[130:131], v228 offset:4096\n"
"v_mul_f32 v199, s62, v199\n"
"v_mfma_f32_16x16x32_bf16 v[168:171], a[136:139], v[208:211], v[168:171]\n"
"ds_read_b64_tr_b16 a[132:133], v229 offset:0\n"
"v_mul_f32 v203, s62, v203\n"
"v_mfma_f32_16x16x32_bf16 v[172:175], a[140:143], v[208:211], v[172:175]\n"
"ds_read_b64_tr_b16 a[134:135], v229 offset:4096\n"
"v_mul_f32 v204, s62, v204\n"
"v_mfma_f32_16x16x32_bf16 v[176:179], a[144:147], v[208:211], v[176:179]\n"
"ds_read_b64_tr_b16 a[136:137], v228 offset:512\n"
"v_exp_f32 v200, v200\n"
"v_mfma_f32_16x16x32_bf16 v[180:183], a[148:151], v[208:211], v[180:183]\n"
"ds_read_b64_tr_b16 a[138:139], v228 offset:4608\n"
"v_mul_f32 v205, s62, v205\n"
"v_mfma_f32_16x16x32_bf16 v[184:187], a[152:155], v[208:211], v[184:187]\n"
"ds_read_b64_tr_b16 a[140:141], v229 offset:512\n"
"v_mul_f32 v206, s62, v206\n"
"v_mfma_f32_16x16x32_bf16 v[188:191], a[156:159], v[208:211], v[188:191]\n"
"ds_read_b64_tr_b16 a[142:143], v229 offset:4608\n"
"v_exp_f32 v201, v201\n"
"v_mfma_f32_16x16x32_bf16 a[180:183], a[184:187], v[208:211], a[180:183]\n"
"ds_read_b64_tr_b16 a[144:145], v228 offset:1024\n"
"v_cvt_pk_bf16_f32 v200, v200, v201\n"
"v_mfma_f32_16x16x32_bf16 v[208:211], a[96:99], a[32:35], 0\n"
"ds_read_b64_tr_b16 a[146:147], v228 offset:5120\n"
"v_mul_f32 v207, s62, v207\n"
"v_mfma_f32_16x16x32_bf16 v[208:211], a[100:103], a[36:39], v[208:211]\n"
"ds_read_b64_tr_b16 a[148:149], v229 offset:1024\n"
"v_mfma_f32_16x16x32_bf16 v[208:211], a[104:107], a[40:43], v[208:211]\n"
"ds_read_b64_tr_b16 a[150:151], v229 offset:5120\n"
"v_exp_f32 v202, v202\n"
"v_mfma_f32_16x16x32_bf16 v[208:211], a[108:111], a[44:47], v[208:211]\n"
"ds_read_b64_tr_b16 a[152:153], v228 offset:1536\n"
"v_exp_f32 v199, v199\n"
"v_mfma_f32_16x16x32_bf16 v[212:215], a[112:115], a[32:35], 0\n"
"ds_read_b64_tr_b16 a[154:155], v228 offset:5632\n"
"v_cvt_pk_bf16_f32 v195, v198, v199\n"
"v_mfma_f32_16x16x32_bf16 v[212:215], a[116:119], a[36:39], v[212:215]\n"
"ds_read_b64_tr_b16 a[156:157], v229 offset:1536\n"
"v_exp_f32 v203, v203\n"
"v_mfma_f32_16x16x32_bf16 v[212:215], a[120:123], a[40:43], v[212:215]\n"
"ds_read_b64_tr_b16 a[158:159], v229 offset:5632\n"
"v_cvt_pk_bf16_f32 v201, v202, v203\n"
"v_mfma_f32_16x16x32_bf16 v[212:215], a[124:127], a[44:47], v[212:215]\n"
"v_mul_f32 v208, s62, v208\n"
"s_waitcnt lgkmcnt(5)\n"
"v_mfma_f32_16x16x32_bf16 v[0:3], a[128:131], v[192:195], v[0:3]\n"
"v_exp_f32 v204, v204\n"
"v_mfma_f32_16x16x32_bf16 v[4:7], a[132:135], v[192:195], v[4:7]\n"
"buffer_store_dwordx4 a[212:215], v227, s[48:51], s61 offen offset:2048 sc1 nt\n"
"v_mul_f32 v209, s62, v209\n"
"v_mfma_f32_16x16x32_bf16 v[8:11], a[136:139], v[192:195], v[8:11]\n"
"v_exp_f32 v208, v208\n"
"v_mfma_f32_16x16x32_bf16 v[12:15], a[140:143], v[192:195], v[12:15]\n"
"buffer_store_dwordx4 a[216:219], v227, s[48:51], s61 offen offset:3072 sc1 nt\n"
"v_mul_f32 v210, s62, v210\n"
"v_mfma_f32_16x16x32_bf16 v[16:19], a[144:147], v[192:195], v[16:19]\n"
"v_mul_f32 v211, s62, v211\n"
"s_waitcnt lgkmcnt(0)\n"
"v_mfma_f32_16x16x32_bf16 v[20:23], a[148:151], v[192:195], v[20:23]\n"
"v_exp_f32 v205, v205\n"
"v_mfma_f32_16x16x32_bf16 v[24:27], a[152:155], v[192:195], v[24:27]\n"
"v_cvt_pk_bf16_f32 v202, v204, v205\n"
"v_mul_f32 v212, s62, v212\n"
"v_mfma_f32_16x16x32_bf16 v[28:31], a[156:159], v[192:195], v[28:31]\n"
"v_exp_f32 v209, v209\n"
"v_mfma_f32_16x16x32_bf16 a[160:163], a[184:187], v[192:195], a[160:163]\n"
"v_exp_f32 v206, v206\n"
"v_mfma_f32_16x16x32_bf16 v[192:195], a[96:99], a[48:51], 0\n"
"v_cvt_pk_bf16_f32 v208, v208, v209\n"
"v_mul_f32 v213, s62, v213\n"
"v_mfma_f32_16x16x32_bf16 v[192:195], a[100:103], a[52:55], v[192:195]\n"
"v_exp_f32 v210, v210\n"
"v_mfma_f32_16x16x32_bf16 v[192:195], a[104:107], a[56:59], v[192:195]\n"
"v_exp_f32 v207, v207\n"
"v_mfma_f32_16x16x32_bf16 v[192:195], a[108:111], a[60:63], v[192:195]\n"
"v_cvt_pk_bf16_f32 v203, v206, v207\n"
"v_mul_f32 v214, s62, v214\n"
"v_mfma_f32_16x16x32_bf16 v[196:199], a[112:115], a[48:51], 0\n"
"v_exp_f32 v211, v211\n"
"v_mfma_f32_16x16x32_bf16 v[196:199], a[116:119], a[52:55], v[196:199]\n"
"v_cvt_pk_bf16_f32 v209, v210, v211\n"
"v_mul_f32 v215, s62, v215\n"
"v_mfma_f32_16x16x32_bf16 v[196:199], a[120:123], a[56:59], v[196:199]\n"
"v_exp_f32 v212, v212\n"
"v_mfma_f32_16x16x32_bf16 v[196:199], a[124:127], a[60:63], v[196:199]\n"
"v_mul_f32 v192, s62, v192\n"
"v_mul_f32 v193, s62, v193\n"
"v_mfma_f32_16x16x32_bf16 v[32:35], a[128:131], v[200:203], v[32:35]\n"
"v_exp_f32 v192, v192\n"
"v_mfma_f32_16x16x32_bf16 v[36:39], a[132:135], v[200:203], v[36:39]\n"
"v_exp_f32 v213, v213\n"
"v_mfma_f32_16x16x32_bf16 v[40:43], a[136:139], v[200:203], v[40:43]\n"
"v_cvt_pk_bf16_f32 v210, v212, v213\n"
"v_mul_f32 v194, s62, v194\n"
"v_mfma_f32_16x16x32_bf16 v[44:47], a[140:143], v[200:203], v[44:47]\n"
"v_exp_f32 v193, v193\n"
"v_mfma_f32_16x16x32_bf16 v[48:51], a[144:147], v[200:203], v[48:51]\n"
"s_waitcnt vmcnt(14)\n"
"s_barrier\n"
"v_mfma_f32_16x16x32_bf16 v[52:55], a[148:151], v[200:203], v[52:55]\n"
"v_exp_f32 v214, v214\n"
"v_mfma_f32_16x16x32_bf16 v[56:59], a[152:155], v[200:203], v[56:59]\n"
"s_add_u32 m0, s56, 0\n"
"s_add_u32 s72, s57, 163840\n"
"v_mfma_f32_16x16x32_bf16 v[60:63], a[156:159], v[200:203], v[60:63]\n"
"buffer_load_dwordx4 v226, s[40:43], s72 offen nt lds\n"
"v_cvt_pk_bf16_f32 v192, v192, v193\n"
"v_mfma_f32_16x16x32_bf16 a[164:167], a[184:187], v[200:203], a[164:167]\n"
"buffer_load_dwordx4 v226, s[40:43], s72 offen offset:2048 nt lds\n"
"v_mul_f32 v195, s62, v195\n"
"v_mfma_f32_16x16x32_bf16 v[200:203], a[96:99], a[64:67], 0\n"
"v_mul_f32 v196, s62, v196\n"
"s_add_u32 m0, s56, 32768\n"
"v_mfma_f32_16x16x32_bf16 v[200:203], a[100:103], a[68:71], v[200:203]\n"
"buffer_load_dwordx4 v226, s[44:47], s72 offen nt lds\n"
"v_mul_f32 v197, s62, v197\n"
"v_mfma_f32_16x16x32_bf16 v[200:203], a[104:107], a[72:75], v[200:203]\n"
"buffer_load_dwordx4 v226, s[44:47], s72 offen offset:2048 nt lds\n"
"v_exp_f32 v194, v194\n"
"v_mfma_f32_16x16x32_bf16 v[200:203], a[108:111], a[76:79], v[200:203]\n"
"v_exp_f32 v215, v215\n"
"v_mfma_f32_16x16x32_bf16 v[204:207], a[112:115], a[64:67], 0\n"
"v_cvt_pk_bf16_f32 v211, v214, v215\n"
"v_mul_f32 v198, s62, v198\n"
"v_mfma_f32_16x16x32_bf16 v[204:207], a[116:119], a[68:71], v[204:207]\n"
"v_exp_f32 v195, v195\n"
"v_mfma_f32_16x16x32_bf16 v[204:207], a[120:123], a[72:75], v[204:207]\n"
"v_cvt_pk_bf16_f32 v193, v194, v195\n"
"v_mul_f32 v200, s62, v200\n"
"v_mfma_f32_16x16x32_bf16 v[204:207], a[124:127], a[76:79], v[204:207]\n"
"v_exp_f32 v196, v196\n"
"v_mfma_f32_16x16x32_bf16 v[64:67], a[128:131], v[208:211], v[64:67]\n"
"v_exp_f32 v200, v200\n"
"v_mfma_f32_16x16x32_bf16 v[68:71], a[132:135], v[208:211], v[68:71]\n"
"v_exp_f32 v197, v197\n"
"v_mfma_f32_16x16x32_bf16 v[72:75], a[136:139], v[208:211], v[72:75]\n"
"v_mul_f32 v201, s62, v201\n"
"v_cvt_pk_bf16_f32 v194, v196, v197\n"
"v_mfma_f32_16x16x32_bf16 v[76:79], a[140:143], v[208:211], v[76:79]\n"
"v_exp_f32 v201, v201\n"
"v_mfma_f32_16x16x32_bf16 v[80:83], a[144:147], v[208:211], v[80:83]\n"
"v_exp_f32 v198, v198\n"
"v_mfma_f32_16x16x32_bf16 v[84:87], a[148:151], v[208:211], v[84:87]\n"
"v_mul_f32 v202, s62, v202\n"
"v_cvt_pk_bf16_f32 v200, v200, v201\n"
"v_mfma_f32_16x16x32_bf16 v[88:91], a[152:155], v[208:211], v[88:91]\n"
"v_mul_f32 v199, s62, v199\n"
"v_mul_f32 v203, s62, v203\n"
"v_mfma_f32_16x16x32_bf16 v[92:95], a[156:159], v[208:211], v[92:95]\n"
"v_exp_f32 v202, v202\n"
"v_mfma_f32_16x16x32_bf16 a[168:171], a[184:187], v[208:211], a[168:171]\n"
"v_exp_f32 v199, v199\n"
"v_mfma_f32_16x16x32_bf16 v[208:211], a[96:99], a[80:83], 0\n"
"ds_read_b128 a[96:99], v224 offset:8192\n"
"v_cvt_pk_bf16_f32 v195, v198, v199\n"
"v_mfma_f32_16x16x32_bf16 v[208:211], a[100:103], a[84:87], v[208:211]\n"
"v_exp_f32 v203, v203\n"
"v_mfma_f32_16x16x32_bf16 v[208:211], a[104:107], a[88:91], v[208:211]\n"
"ds_read_b128 a[100:103], v224 offset:8704\n"
"v_mul_f32 v204, s62, v204\n"
"v_mfma_f32_16x16x32_bf16 v[208:211], a[108:111], a[92:95], v[208:211]\n"
"v_cvt_pk_bf16_f32 v201, v202, v203\n"
"v_mul_f32 v205, s62, v205\n"
"v_mfma_f32_16x16x32_bf16 v[212:215], a[112:115], a[80:83], 0\n"
"ds_read_b128 a[104:107], v224 offset:9216\n"
"v_mul_f32 v206, s62, v206\n"
"v_mfma_f32_16x16x32_bf16 v[212:215], a[116:119], a[84:87], v[212:215]\n"
"v_exp_f32 v204, v204\n"
"v_mfma_f32_16x16x32_bf16 v[212:215], a[120:123], a[88:91], v[212:215]\n"
"ds_read_b128 a[108:111], v224 offset:9728\n"
"v_mul_f32 v208, s62, v208\n"
"v_mfma_f32_16x16x32_bf16 v[212:215], a[124:127], a[92:95], v[212:215]\n"
"v_exp_f32 v208, v208\n"
"v_mfma_f32_16x16x32_bf16 v[96:99], a[128:131], v[192:195], v[96:99]\n"
"ds_read_b128 a[112:115], v224 offset:12288\n"
"v_mul_f32 v209, s62, v209\n"
"v_mfma_f32_16x16x32_bf16 v[100:103], a[132:135], v[192:195], v[100:103]\n"
"v_exp_f32 v205, v205\n"
"v_mfma_f32_16x16x32_bf16 v[104:107], a[136:139], v[192:195], v[104:107]\n"
"ds_read_b128 a[116:119], v224 offset:12800\n"
"v_cvt_pk_bf16_f32 v202, v204, v205\n"
"v_mfma_f32_16x16x32_bf16 v[108:111], a[140:143], v[192:195], v[108:111]\n"
"v_exp_f32 v209, v209\n"
"v_mfma_f32_16x16x32_bf16 v[112:115], a[144:147], v[192:195], v[112:115]\n"
"ds_read_b128 a[120:123], v224 offset:13312\n"
"v_mul_f32 v210, s62, v210\n"
"v_mfma_f32_16x16x32_bf16 v[116:119], a[148:151], v[192:195], v[116:119]\n"
"v_exp_f32 v206, v206\n"
"v_mfma_f32_16x16x32_bf16 v[120:123], a[152:155], v[192:195], v[120:123]\n"
"ds_read_b128 a[124:127], v224 offset:13824\n"
"v_cvt_pk_bf16_f32 v208, v208, v209\n"
"v_mfma_f32_16x16x32_bf16 v[124:127], a[156:159], v[192:195], v[124:127]\n"
"v_mul_f32 v207, s62, v207\n"
"v_mul_f32 v211, s62, v211\n"
"v_mfma_f32_16x16x32_bf16 a[172:175], a[184:187], v[192:195], a[172:175]\n"
"v_mul_f32 v212, s62, v212\n"
"s_waitcnt lgkmcnt(2)\n"
"v_mfma_f32_16x16x32_bf16 v[192:195], a[96:99], a[0:3], 0\n"
"v_exp_f32 v210, v210\n"
"v_mfma_f32_16x16x32_bf16 v[192:195], a[100:103], a[4:7], v[192:195]\n"
"ds_read_b128 a[220:223], v230 offset:8192\n"
"v_mul_f32 v213, s62, v213\n"
"v_mfma_f32_16x16x32_bf16 v[192:195], a[104:107], a[8:11], v[192:195]\n"
"v_exp_f32 v207, v207\n"
"v_mfma_f32_16x16x32_bf16 v[192:195], a[108:111], a[12:15], v[192:195]\n"
"ds_read_b128 a[224:227], v231 offset:9216\n"
"v_cvt_pk_bf16_f32 v203, v206, v207\n"
"v_mfma_f32_16x16x32_bf16 v[196:199], a[112:115], a[0:3], 0\n"
"v_exp_f32 v211, v211\n"
"v_mfma_f32_16x16x32_bf16 v[196:199], a[116:119], a[4:7], v[196:199]\n"
"v_cvt_pk_bf16_f32 v209, v210, v211\n"
"s_waitcnt lgkmcnt(2)\n"
"v_mfma_f32_16x16x32_bf16 v[196:199], a[120:123], a[8:11], v[196:199]\n"
"v_exp_f32 v212, v212\n"
"v_mfma_f32_16x16x32_bf16 v[196:199], a[124:127], a[12:15], v[196:199]\n"
"v_mul_f32 v192, s62, v192\n"
"v_mul_f32 v193, s62, v193\n"
"v_mfma_f32_16x16x32_bf16 v[128:131], a[128:131], v[200:203], v[128:131]\n"
"v_exp_f32 v192, v192\n"
"v_mfma_f32_16x16x32_bf16 v[132:135], a[132:135], v[200:203], v[132:135]\n"
"v_exp_f32 v213, v213\n"
"v_mfma_f32_16x16x32_bf16 v[136:139], a[136:139], v[200:203], v[136:139]\n"
"v_mul_f32 v214, s62, v214\n"
"v_cvt_pk_bf16_f32 v210, v212, v213\n"
"v_mfma_f32_16x16x32_bf16 v[140:143], a[140:143], v[200:203], v[140:143]\n"
"v_exp_f32 v193, v193\n"
"v_mfma_f32_16x16x32_bf16 v[144:147], a[144:147], v[200:203], v[144:147]\n"
"v_exp_f32 v214, v214\n"
"v_mfma_f32_16x16x32_bf16 v[148:151], a[148:151], v[200:203], v[148:151]\n"
"v_mul_f32 v194, s62, v194\n"
"v_cvt_pk_bf16_f32 v192, v192, v193\n"
"v_mfma_f32_16x16x32_bf16 v[152:155], a[152:155], v[200:203], v[152:155]\n"
"v_mul_f32 v215, s62, v215\n"
"v_mul_f32 v195, s62, v195\n"
"v_mfma_f32_16x16x32_bf16 v[156:159], a[156:159], v[200:203], v[156:159]\n"
"v_exp_f32 v194, v194\n"
"v_mfma_f32_16x16x32_bf16 a[176:179], a[184:187], v[200:203], a[176:179]\n"
"v_exp_f32 v215, v215\n"
"v_mfma_f32_16x16x32_bf16 v[200:203], a[96:99], a[16:19], 0\n"
"v_cvt_pk_bf16_f32 v211, v214, v215\n"
"v_mul_f32 v196, s62, v196\n"
"v_mfma_f32_16x16x32_bf16 v[200:203], a[100:103], a[20:23], v[200:203]\n"
"v_exp_f32 v195, v195\n"
"v_mfma_f32_16x16x32_bf16 v[200:203], a[104:107], a[24:27], v[200:203]\n"
"v_cvt_pk_bf16_f32 v193, v194, v195\n"
"v_mul_f32 v197, s62, v197\n"
"v_mfma_f32_16x16x32_bf16 v[200:203], a[108:111], a[28:31], v[200:203]\n"
"v_exp_f32 v196, v196\n"
"v_mfma_f32_16x16x32_bf16 v[204:207], a[112:115], a[16:19], 0\n"
"v_exp_f32 v197, v197\n"
"v_mfma_f32_16x16x32_bf16 v[204:207], a[116:119], a[20:23], v[204:207]\n"
"v_mul_f32 v198, s62, v198\n"
"v_cvt_pk_bf16_f32 v194, v196, v197\n"
"v_mfma_f32_16x16x32_bf16 v[204:207], a[120:123], a[24:27], v[204:207]\n"
"v_exp_f32 v198, v198\n"
"v_mfma_f32_16x16x32_bf16 v[204:207], a[124:127], a[28:31], v[204:207]\n"
"v_mul_f32 v200, s62, v200\n"
"v_mul_f32 v201, s62, v201\n"
"v_mfma_f32_16x16x32_bf16 v[160:163], a[128:131], v[208:211], v[160:163]\n"
"ds_read_b64_tr_b16 a[128:129], v228 offset:8192\n"
"v_mul_f32 v202, s62, v202\n"
"v_mfma_f32_16x16x32_bf16 v[164:167], a[132:135], v[208:211], v[164:167]\n"
"ds_read_b64_tr_b16 a[130:131], v228 offset:12288\n"
"v_mul_f32 v199, s62, v199\n"
"v_mfma_f32_16x16x32_bf16 v[168:171], a[136:139], v[208:211], v[168:171]\n"
"ds_read_b64_tr_b16 a[132:133], v229 offset:8192\n"
"v_mul_f32 v203, s62, v203\n"
"v_mfma_f32_16x16x32_bf16 v[172:175], a[140:143], v[208:211], v[172:175]\n"
"ds_read_b64_tr_b16 a[134:135], v229 offset:12288\n"
"v_mul_f32 v204, s62, v204\n"
"v_mfma_f32_16x16x32_bf16 v[176:179], a[144:147], v[208:211], v[176:179]\n"
"ds_read_b64_tr_b16 a[136:137], v228 offset:8704\n"
"v_exp_f32 v200, v200\n"
"v_mfma_f32_16x16x32_bf16 v[180:183], a[148:151], v[208:211], v[180:183]\n"
"ds_read_b64_tr_b16 a[138:139], v228 offset:12800\n"
"v_mul_f32 v205, s62, v205\n"
"v_mfma_f32_16x16x32_bf16 v[184:187], a[152:155], v[208:211], v[184:187]\n"
"ds_read_b64_tr_b16 a[140:141], v229 offset:8704\n"
"v_mul_f32 v206, s62, v206\n"
"v_mfma_f32_16x16x32_bf16 v[188:191], a[156:159], v[208:211], v[188:191]\n"
"ds_read_b64_tr_b16 a[142:143], v229 offset:12800\n"
"v_exp_f32 v201, v201\n"
"v_mfma_f32_16x16x32_bf16 a[180:183], a[184:187], v[208:211], a[180:183]\n"
"ds_read_b64_tr_b16 a[144:145], v228 offset:9216\n"
"v_cvt_pk_bf16_f32 v200, v200, v201\n"
"v_mfma_f32_16x16x32_bf16 v[208:211], a[96:99], a[32:35], 0\n"
"ds_read_b64_tr_b16 a[146:147], v228 offset:13312\n"
"v_mul_f32 v207, s62, v207\n"
"v_mfma_f32_16x16x32_bf16 v[208:211], a[100:103], a[36:39], v[208:211]\n"
"ds_read_b64_tr_b16 a[148:149], v229 offset:9216\n"
"v_mfma_f32_16x16x32_bf16 v[208:211], a[104:107], a[40:43], v[208:211]\n"
"ds_read_b64_tr_b16 a[150:151], v229 offset:13312\n"
"v_exp_f32 v202, v202\n"
"v_mfma_f32_16x16x32_bf16 v[208:211], a[108:111], a[44:47], v[208:211]\n"
"ds_read_b64_tr_b16 a[152:153], v228 offset:9728\n"
"v_exp_f32 v199, v199\n"
"v_mfma_f32_16x16x32_bf16 v[212:215], a[112:115], a[32:35], 0\n"
"ds_read_b64_tr_b16 a[154:155], v228 offset:13824\n"
"v_cvt_pk_bf16_f32 v195, v198, v199\n"
"v_mfma_f32_16x16x32_bf16 v[212:215], a[116:119], a[36:39], v[212:215]\n"
"ds_read_b64_tr_b16 a[156:157], v229 offset:9728\n"
"v_exp_f32 v203, v203\n"
"v_mfma_f32_16x16x32_bf16 v[212:215], a[120:123], a[40:43], v[212:215]\n"
"ds_read_b64_tr_b16 a[158:159], v229 offset:13824\n"
"v_cvt_pk_bf16_f32 v201, v202, v203\n"
"v_mfma_f32_16x16x32_bf16 v[212:215], a[124:127], a[44:47], v[212:215]\n"
"v_mul_f32 v208, s62, v208\n"
"s_waitcnt lgkmcnt(5)\n"
"v_mfma_f32_16x16x32_bf16 v[0:3], a[128:131], v[192:195], v[0:3]\n"
"v_mul_f32 v209, s62, v209\n"
"s_add_u32 s61, s59, 8192\n"
"v_mfma_f32_16x16x32_bf16 v[4:7], a[132:135], v[192:195], v[4:7]\n"
"buffer_store_dwordx4 a[220:223], v227, s[48:51], s61 offen sc1 nt\n"
"v_exp_f32 v204, v204\n"
"v_mfma_f32_16x16x32_bf16 v[8:11], a[136:139], v[192:195], v[8:11]\n"
"v_exp_f32 v208, v208\n"
"v_mfma_f32_16x16x32_bf16 v[12:15], a[140:143], v[192:195], v[12:15]\n"
"buffer_store_dwordx4 a[224:227], v227, s[48:51], s61 offen offset:1024 sc1 nt\n"
"v_mul_f32 v210, s62, v210\n"
"v_mfma_f32_16x16x32_bf16 v[16:19], a[144:147], v[192:195], v[16:19]\n"
"v_mul_f32 v211, s62, v211\n"
"s_waitcnt lgkmcnt(0)\n"
"v_mfma_f32_16x16x32_bf16 v[20:23], a[148:151], v[192:195], v[20:23]\n"
"v_exp_f32 v205, v205\n"
"v_mfma_f32_16x16x32_bf16 v[24:27], a[152:155], v[192:195], v[24:27]\n"
"v_cvt_pk_bf16_f32 v202, v204, v205\n"
"v_mul_f32 v212, s62, v212\n"
"v_mfma_f32_16x16x32_bf16 v[28:31], a[156:159], v[192:195], v[28:31]\n"
"v_exp_f32 v209, v209\n"
"v_mfma_f32_16x16x32_bf16 a[160:163], a[184:187], v[192:195], a[160:163]\n"
"v_exp_f32 v206, v206\n"
"v_mfma_f32_16x16x32_bf16 v[192:195], a[96:99], a[48:51], 0\n"
"v_cvt_pk_bf16_f32 v208, v208, v209\n"
"v_mul_f32 v213, s62, v213\n"
"v_mfma_f32_16x16x32_bf16 v[192:195], a[100:103], a[52:55], v[192:195]\n"
"v_exp_f32 v210, v210\n"
"v_mfma_f32_16x16x32_bf16 v[192:195], a[104:107], a[56:59], v[192:195]\n"
"v_exp_f32 v207, v207\n"
"v_mfma_f32_16x16x32_bf16 v[192:195], a[108:111], a[60:63], v[192:195]\n"
"v_cvt_pk_bf16_f32 v203, v206, v207\n"
"v_mul_f32 v214, s62, v214\n"
"v_mfma_f32_16x16x32_bf16 v[196:199], a[112:115], a[48:51], 0\n"
"v_exp_f32 v211, v211\n"
"v_mfma_f32_16x16x32_bf16 v[196:199], a[116:119], a[52:55], v[196:199]\n"
"v_cvt_pk_bf16_f32 v209, v210, v211\n"
"v_mul_f32 v215, s62, v215\n"
"v_mfma_f32_16x16x32_bf16 v[196:199], a[120:123], a[56:59], v[196:199]\n"
"v_exp_f32 v212, v212\n"
"v_mfma_f32_16x16x32_bf16 v[196:199], a[124:127], a[60:63], v[196:199]\n"
"v_mul_f32 v192, s62, v192\n"
"v_mul_f32 v193, s62, v193\n"
"v_mfma_f32_16x16x32_bf16 v[32:35], a[128:131], v[200:203], v[32:35]\n"
"v_exp_f32 v192, v192\n"
"v_mfma_f32_16x16x32_bf16 v[36:39], a[132:135], v[200:203], v[36:39]\n"
"v_exp_f32 v213, v213\n"
"v_mfma_f32_16x16x32_bf16 v[40:43], a[136:139], v[200:203], v[40:43]\n"
"v_cvt_pk_bf16_f32 v210, v212, v213\n"
"v_mul_f32 v194, s62, v194\n"
"v_mfma_f32_16x16x32_bf16 v[44:47], a[140:143], v[200:203], v[44:47]\n"
"v_exp_f32 v193, v193\n"
"v_mfma_f32_16x16x32_bf16 v[48:51], a[144:147], v[200:203], v[48:51]\n"
"s_waitcnt vmcnt(14)\n"
"s_barrier\n"
"v_mfma_f32_16x16x32_bf16 v[52:55], a[148:151], v[200:203], v[52:55]\n"
"v_exp_f32 v214, v214\n"
"v_mfma_f32_16x16x32_bf16 v[56:59], a[152:155], v[200:203], v[56:59]\n"
"s_add_u32 m0, s56, 8192\n"
"s_add_u32 s72, s57, 172032\n"
"v_mfma_f32_16x16x32_bf16 v[60:63], a[156:159], v[200:203], v[60:63]\n"
"buffer_load_dwordx4 v226, s[40:43], s72 offen nt lds\n"
"v_cvt_pk_bf16_f32 v192, v192, v193\n"
"v_mfma_f32_16x16x32_bf16 a[164:167], a[184:187], v[200:203], a[164:167]\n"
"buffer_load_dwordx4 v226, s[40:43], s72 offen offset:2048 nt lds\n"
"v_mul_f32 v195, s62, v195\n"
"v_mfma_f32_16x16x32_bf16 v[200:203], a[96:99], a[64:67], 0\n"
"v_mul_f32 v196, s62, v196\n"
"s_add_u32 m0, s56, 40960\n"
"v_mfma_f32_16x16x32_bf16 v[200:203], a[100:103], a[68:71], v[200:203]\n"
"buffer_load_dwordx4 v226, s[44:47], s72 offen nt lds\n"
"v_mul_f32 v197, s62, v197\n"
"v_mfma_f32_16x16x32_bf16 v[200:203], a[104:107], a[72:75], v[200:203]\n"
"buffer_load_dwordx4 v226, s[44:47], s72 offen offset:2048 nt lds\n"
"v_exp_f32 v194, v194\n"
"v_mfma_f32_16x16x32_bf16 v[200:203], a[108:111], a[76:79], v[200:203]\n"
"v_exp_f32 v215, v215\n"
"v_mfma_f32_16x16x32_bf16 v[204:207], a[112:115], a[64:67], 0\n"
"v_cvt_pk_bf16_f32 v211, v214, v215\n"
"v_mul_f32 v198, s62, v198\n"
"v_mfma_f32_16x16x32_bf16 v[204:207], a[116:119], a[68:71], v[204:207]\n"
"v_exp_f32 v195, v195\n"
"v_mfma_f32_16x16x32_bf16 v[204:207], a[120:123], a[72:75], v[204:207]\n"
"v_cvt_pk_bf16_f32 v193, v194, v195\n"
"v_mul_f32 v200, s62, v200\n"
"v_mfma_f32_16x16x32_bf16 v[204:207], a[124:127], a[76:79], v[204:207]\n"
"v_exp_f32 v196, v196\n"
"v_mfma_f32_16x16x32_bf16 v[64:67], a[128:131], v[208:211], v[64:67]\n"
"v_exp_f32 v200, v200\n"
"v_mfma_f32_16x16x32_bf16 v[68:71], a[132:135], v[208:211], v[68:71]\n"
"v_exp_f32 v197, v197\n"
"v_mfma_f32_16x16x32_bf16 v[72:75], a[136:139], v[208:211], v[72:75]\n"
"v_mul_f32 v201, s62, v201\n"
"v_cvt_pk_bf16_f32 v194, v196, v197\n"
"v_mfma_f32_16x16x32_bf16 v[76:79], a[140:143], v[208:211], v[76:79]\n"
"v_exp_f32 v201, v201\n"
"v_mfma_f32_16x16x32_bf16 v[80:83], a[144:147], v[208:211], v[80:83]\n"
"v_exp_f32 v198, v198\n"
"v_mfma_f32_16x16x32_bf16 v[84:87], a[148:151], v[208:211], v[84:87]\n"
"v_mul_f32 v202, s62, v202\n"
"v_cvt_pk_bf16_f32 v200, v200, v201\n"
"v_mfma_f32_16x16x32_bf16 v[88:91], a[152:155], v[208:211], v[88:91]\n"
"v_mul_f32 v199, s62, v199\n"
"v_mul_f32 v203, s62, v203\n"
"v_mfma_f32_16x16x32_bf16 v[92:95], a[156:159], v[208:211], v[92:95]\n"
"v_exp_f32 v202, v202\n"
"v_mfma_f32_16x16x32_bf16 a[168:171], a[184:187], v[208:211], a[168:171]\n"
"v_exp_f32 v199, v199\n"
"v_mfma_f32_16x16x32_bf16 v[208:211], a[96:99], a[80:83], 0\n"
"ds_read_b128 a[96:99], v224 offset:16384\n"
"v_cvt_pk_bf16_f32 v195, v198, v199\n"
"v_mfma_f32_16x16x32_bf16 v[208:211], a[100:103], a[84:87], v[208:211]\n"
"v_exp_f32 v203, v203\n"
"v_mfma_f32_16x16x32_bf16 v[208:211], a[104:107], a[88:91], v[208:211]\n"
"ds_read_b128 a[100:103], v224 offset:16896\n"
"v_mul_f32 v204, s62, v204\n"
"v_mfma_f32_16x16x32_bf16 v[208:211], a[108:111], a[92:95], v[208:211]\n"
"v_cvt_pk_bf16_f32 v201, v202, v203\n"
"v_mul_f32 v205, s62, v205\n"
"v_mfma_f32_16x16x32_bf16 v[212:215], a[112:115], a[80:83], 0\n"
"ds_read_b128 a[104:107], v224 offset:17408\n"
"v_mul_f32 v206, s62, v206\n"
"v_mfma_f32_16x16x32_bf16 v[212:215], a[116:119], a[84:87], v[212:215]\n"
"v_exp_f32 v204, v204\n"
"v_mfma_f32_16x16x32_bf16 v[212:215], a[120:123], a[88:91], v[212:215]\n"
"ds_read_b128 a[108:111], v224 offset:17920\n"
"v_mul_f32 v208, s62, v208\n"
"v_mfma_f32_16x16x32_bf16 v[212:215], a[124:127], a[92:95], v[212:215]\n"
"v_exp_f32 v208, v208\n"
"v_mfma_f32_16x16x32_bf16 v[96:99], a[128:131], v[192:195], v[96:99]\n"
"ds_read_b128 a[112:115], v224 offset:20480\n"
"v_mul_f32 v209, s62, v209\n"
"v_mfma_f32_16x16x32_bf16 v[100:103], a[132:135], v[192:195], v[100:103]\n"
"v_exp_f32 v205, v205\n"
"v_mfma_f32_16x16x32_bf16 v[104:107], a[136:139], v[192:195], v[104:107]\n"
"ds_read_b128 a[116:119], v224 offset:20992\n"
"v_cvt_pk_bf16_f32 v202, v204, v205\n"
"v_mfma_f32_16x16x32_bf16 v[108:111], a[140:143], v[192:195], v[108:111]\n"
"v_exp_f32 v209, v209\n"
"v_mfma_f32_16x16x32_bf16 v[112:115], a[144:147], v[192:195], v[112:115]\n"
"ds_read_b128 a[120:123], v224 offset:21504\n"
"v_mul_f32 v210, s62, v210\n"
"v_mfma_f32_16x16x32_bf16 v[116:119], a[148:151], v[192:195], v[116:119]\n"
"v_exp_f32 v206, v206\n"
"v_mfma_f32_16x16x32_bf16 v[120:123], a[152:155], v[192:195], v[120:123]\n"
"ds_read_b128 a[124:127], v224 offset:22016\n"
"v_cvt_pk_bf16_f32 v208, v208, v209\n"
"v_mfma_f32_16x16x32_bf16 v[124:127], a[156:159], v[192:195], v[124:127]\n"
"v_mul_f32 v207, s62, v207\n"
"v_mul_f32 v211, s62, v211\n"
"v_mfma_f32_16x16x32_bf16 a[172:175], a[184:187], v[192:195], a[172:175]\n"
"v_mul_f32 v212, s62, v212\n"
"s_waitcnt lgkmcnt(2)\n"
"v_mfma_f32_16x16x32_bf16 v[192:195], a[96:99], a[0:3], 0\n"
"v_exp_f32 v210, v210\n"
"v_mfma_f32_16x16x32_bf16 v[192:195], a[100:103], a[4:7], v[192:195]\n"
"ds_read_b128 a[228:231], v230 offset:10240\n"
"v_mul_f32 v213, s62, v213\n"
"v_mfma_f32_16x16x32_bf16 v[192:195], a[104:107], a[8:11], v[192:195]\n"
"v_exp_f32 v207, v207\n"
"v_mfma_f32_16x16x32_bf16 v[192:195], a[108:111], a[12:15], v[192:195]\n"
"ds_read_b128 a[232:235], v231 offset:11264\n"
"v_cvt_pk_bf16_f32 v203, v206, v207\n"
"v_mfma_f32_16x16x32_bf16 v[196:199], a[112:115], a[0:3], 0\n"
"v_exp_f32 v211, v211\n"
"v_mfma_f32_16x16x32_bf16 v[196:199], a[116:119], a[4:7], v[196:199]\n"
"v_cvt_pk_bf16_f32 v209, v210, v211\n"
"s_waitcnt lgkmcnt(2)\n"
"v_mfma_f32_16x16x32_bf16 v[196:199], a[120:123], a[8:11], v[196:199]\n"
"v_exp_f32 v212, v212\n"
"v_mfma_f32_16x16x32_bf16 v[196:199], a[124:127], a[12:15], v[196:199]\n"
"v_mul_f32 v192, s62, v192\n"
"v_mul_f32 v193, s62, v193\n"
"v_mfma_f32_16x16x32_bf16 v[128:131], a[128:131], v[200:203], v[128:131]\n"
"v_exp_f32 v192, v192\n"
"v_mfma_f32_16x16x32_bf16 v[132:135], a[132:135], v[200:203], v[132:135]\n"
"v_exp_f32 v213, v213\n"
"v_mfma_f32_16x16x32_bf16 v[136:139], a[136:139], v[200:203], v[136:139]\n"
"v_mul_f32 v214, s62, v214\n"
"v_cvt_pk_bf16_f32 v210, v212, v213\n"
"v_mfma_f32_16x16x32_bf16 v[140:143], a[140:143], v[200:203], v[140:143]\n"
"v_exp_f32 v193, v193\n"
"v_mfma_f32_16x16x32_bf16 v[144:147], a[144:147], v[200:203], v[144:147]\n"
"v_exp_f32 v214, v214\n"
"v_mfma_f32_16x16x32_bf16 v[148:151], a[148:151], v[200:203], v[148:151]\n"
"v_mul_f32 v194, s62, v194\n"
"v_cvt_pk_bf16_f32 v192, v192, v193\n"
"v_mfma_f32_16x16x32_bf16 v[152:155], a[152:155], v[200:203], v[152:155]\n"
"v_mul_f32 v215, s62, v215\n"
"v_mul_f32 v195, s62, v195\n"
"v_mfma_f32_16x16x32_bf16 v[156:159], a[156:159], v[200:203], v[156:159]\n"
"v_exp_f32 v194, v194\n"
"v_mfma_f32_16x16x32_bf16 a[176:179], a[184:187], v[200:203], a[176:179]\n"
"v_exp_f32 v215, v215\n"
"v_mfma_f32_16x16x32_bf16 v[200:203], a[96:99], a[16:19], 0\n"
"v_cvt_pk_bf16_f32 v211, v214, v215\n"
"v_mul_f32 v196, s62, v196\n"
"v_mfma_f32_16x16x32_bf16 v[200:203], a[100:103], a[20:23], v[200:203]\n"
"v_exp_f32 v195, v195\n"
"v_mfma_f32_16x16x32_bf16 v[200:203], a[104:107], a[24:27], v[200:203]\n"
"v_cvt_pk_bf16_f32 v193, v194, v195\n"
"v_mul_f32 v197, s62, v197\n"
"v_mfma_f32_16x16x32_bf16 v[200:203], a[108:111], a[28:31], v[200:203]\n"
"v_exp_f32 v196, v196\n"
"v_mfma_f32_16x16x32_bf16 v[204:207], a[112:115], a[16:19], 0\n"
"v_exp_f32 v197, v197\n"
"v_mfma_f32_16x16x32_bf16 v[204:207], a[116:119], a[20:23], v[204:207]\n"
"v_mul_f32 v198, s62, v198\n"
"v_cvt_pk_bf16_f32 v194, v196, v197\n"
"v_mfma_f32_16x16x32_bf16 v[204:207], a[120:123], a[24:27], v[204:207]\n"
"v_exp_f32 v198, v198\n"
"v_mfma_f32_16x16x32_bf16 v[204:207], a[124:127], a[28:31], v[204:207]\n"
"v_mul_f32 v200, s62, v200\n"
"v_mul_f32 v201, s62, v201\n"
"v_mfma_f32_16x16x32_bf16 v[160:163], a[128:131], v[208:211], v[160:163]\n"
"ds_read_b64_tr_b16 a[128:129], v228 offset:16384\n"
"v_mul_f32 v202, s62, v202\n"
"v_mfma_f32_16x16x32_bf16 v[164:167], a[132:135], v[208:211], v[164:167]\n"
"ds_read_b64_tr_b16 a[130:131], v228 offset:20480\n"
"v_mul_f32 v199, s62, v199\n"
"v_mfma_f32_16x16x32_bf16 v[168:171], a[136:139], v[208:211], v[168:171]\n"
"ds_read_b64_tr_b16 a[132:133], v229 offset:16384\n"
"v_mul_f32 v203, s62, v203\n"
"v_mfma_f32_16x16x32_bf16 v[172:175], a[140:143], v[208:211], v[172:175]\n"
"ds_read_b64_tr_b16 a[134:135], v229 offset:20480\n"
"v_mul_f32 v204, s62, v204\n"
"v_mfma_f32_16x16x32_bf16 v[176:179], a[144:147], v[208:211], v[176:179]\n"
"ds_read_b64_tr_b16 a[136:137], v228 offset:16896\n"
"v_exp_f32 v200, v200\n"
"v_mfma_f32_16x16x32_bf16 v[180:183], a[148:151], v[208:211], v[180:183]\n"
"ds_read_b64_tr_b16 a[138:139], v228 offset:20992\n"
"v_mul_f32 v205, s62, v205\n"
"v_mfma_f32_16x16x32_bf16 v[184:187], a[152:155], v[208:211], v[184:187]\n"
"ds_read_b64_tr_b16 a[140:141], v229 offset:16896\n"
"v_mul_f32 v206, s62, v206\n"
"v_mfma_f32_16x16x32_bf16 v[188:191], a[156:159], v[208:211], v[188:191]\n"
"ds_read_b64_tr_b16 a[142:143], v229 offset:20992\n"
"v_exp_f32 v201, v201\n"
"v_mfma_f32_16x16x32_bf16 a[180:183], a[184:187], v[208:211], a[180:183]\n"
"ds_read_b64_tr_b16 a[144:145], v228 offset:17408\n"
"v_cvt_pk_bf16_f32 v200, v200, v201\n"
"v_mfma_f32_16x16x32_bf16 v[208:211], a[96:99], a[32:35], 0\n"
"ds_read_b64_tr_b16 a[146:147], v228 offset:21504\n"
"v_mul_f32 v207, s62, v207\n"
"v_mfma_f32_16x16x32_bf16 v[208:211], a[100:103], a[36:39], v[208:211]\n"
"ds_read_b64_tr_b16 a[148:149], v229 offset:17408\n"
"v_mfma_f32_16x16x32_bf16 v[208:211], a[104:107], a[40:43], v[208:211]\n"
"ds_read_b64_tr_b16 a[150:151], v229 offset:21504\n"
"v_exp_f32 v202, v202\n"
"v_mfma_f32_16x16x32_bf16 v[208:211], a[108:111], a[44:47], v[208:211]\n"
"ds_read_b64_tr_b16 a[152:153], v228 offset:17920\n"
"v_exp_f32 v199, v199\n"
"v_mfma_f32_16x16x32_bf16 v[212:215], a[112:115], a[32:35], 0\n"
"ds_read_b64_tr_b16 a[154:155], v228 offset:22016\n"
"v_cvt_pk_bf16_f32 v195, v198, v199\n"
"v_mfma_f32_16x16x32_bf16 v[212:215], a[116:119], a[36:39], v[212:215]\n"
"ds_read_b64_tr_b16 a[156:157], v229 offset:17920\n"
"v_exp_f32 v203, v203\n"
"v_mfma_f32_16x16x32_bf16 v[212:215], a[120:123], a[40:43], v[212:215]\n"
"ds_read_b64_tr_b16 a[158:159], v229 offset:22016\n"
"v_cvt_pk_bf16_f32 v201, v202, v203\n"
"v_mfma_f32_16x16x32_bf16 v[212:215], a[124:127], a[44:47], v[212:215]\n"
"v_mul_f32 v208, s62, v208\n"
"s_waitcnt lgkmcnt(5)\n"
"v_mfma_f32_16x16x32_bf16 v[0:3], a[128:131], v[192:195], v[0:3]\n"
"v_exp_f32 v204, v204\n"
"v_mfma_f32_16x16x32_bf16 v[4:7], a[132:135], v[192:195], v[4:7]\n"
"buffer_store_dwordx4 a[228:231], v227, s[48:51], s61 offen offset:2048 sc1 nt\n"
"v_mul_f32 v209, s62, v209\n"
"v_mfma_f32_16x16x32_bf16 v[8:11], a[136:139], v[192:195], v[8:11]\n"
"v_exp_f32 v208, v208\n"
"v_mfma_f32_16x16x32_bf16 v[12:15], a[140:143], v[192:195], v[12:15]\n"
"buffer_store_dwordx4 a[232:235], v227, s[48:51], s61 offen offset:3072 sc1 nt\n"
"v_mul_f32 v210, s62, v210\n"
"v_mfma_f32_16x16x32_bf16 v[16:19], a[144:147], v[192:195], v[16:19]\n"
"v_mul_f32 v211, s62, v211\n"
"s_waitcnt lgkmcnt(0)\n"
"v_mfma_f32_16x16x32_bf16 v[20:23], a[148:151], v[192:195], v[20:23]\n"
"v_exp_f32 v205, v205\n"
"v_mfma_f32_16x16x32_bf16 v[24:27], a[152:155], v[192:195], v[24:27]\n"
"v_cvt_pk_bf16_f32 v202, v204, v205\n"
"v_mul_f32 v212, s62, v212\n"
"v_mfma_f32_16x16x32_bf16 v[28:31], a[156:159], v[192:195], v[28:31]\n"
"v_exp_f32 v209, v209\n"
"v_mfma_f32_16x16x32_bf16 a[160:163], a[184:187], v[192:195], a[160:163]\n"
"v_exp_f32 v206, v206\n"
"v_mfma_f32_16x16x32_bf16 v[192:195], a[96:99], a[48:51], 0\n"
"v_cvt_pk_bf16_f32 v208, v208, v209\n"
"v_mul_f32 v213, s62, v213\n"
"v_mfma_f32_16x16x32_bf16 v[192:195], a[100:103], a[52:55], v[192:195]\n"
"v_exp_f32 v210, v210\n"
"v_mfma_f32_16x16x32_bf16 v[192:195], a[104:107], a[56:59], v[192:195]\n"
"v_exp_f32 v207, v207\n"
"v_mfma_f32_16x16x32_bf16 v[192:195], a[108:111], a[60:63], v[192:195]\n"
"v_cvt_pk_bf16_f32 v203, v206, v207\n"
"v_mul_f32 v214, s62, v214\n"
"v_mfma_f32_16x16x32_bf16 v[196:199], a[112:115], a[48:51], 0\n"
"v_exp_f32 v211, v211\n"
"v_mfma_f32_16x16x32_bf16 v[196:199], a[116:119], a[52:55], v[196:199]\n"
"v_cvt_pk_bf16_f32 v209, v210, v211\n"
"v_mul_f32 v215, s62, v215\n"
"v_mfma_f32_16x16x32_bf16 v[196:199], a[120:123], a[56:59], v[196:199]\n"
"v_exp_f32 v212, v212\n"
"v_mfma_f32_16x16x32_bf16 v[196:199], a[124:127], a[60:63], v[196:199]\n"
"v_mul_f32 v192, s62, v192\n"
"v_mul_f32 v193, s62, v193\n"
"v_mfma_f32_16x16x32_bf16 v[32:35], a[128:131], v[200:203], v[32:35]\n"
"v_exp_f32 v192, v192\n"
"v_mfma_f32_16x16x32_bf16 v[36:39], a[132:135], v[200:203], v[36:39]\n"
"v_exp_f32 v213, v213\n"
"v_mfma_f32_16x16x32_bf16 v[40:43], a[136:139], v[200:203], v[40:43]\n"
"v_cvt_pk_bf16_f32 v210, v212, v213\n"
"v_mul_f32 v194, s62, v194\n"
"v_mfma_f32_16x16x32_bf16 v[44:47], a[140:143], v[200:203], v[44:47]\n"
"v_exp_f32 v193, v193\n"
"v_mfma_f32_16x16x32_bf16 v[48:51], a[144:147], v[200:203], v[48:51]\n"
"s_waitcnt vmcnt(14)\n"
"s_barrier\n"
"v_mfma_f32_16x16x32_bf16 v[52:55], a[148:151], v[200:203], v[52:55]\n"
"v_exp_f32 v214, v214\n"
"v_mfma_f32_16x16x32_bf16 v[56:59], a[152:155], v[200:203], v[56:59]\n"
"s_add_u32 m0, s56, 16384\n"
"s_add_u32 s72, s57, 180224\n"
"v_mfma_f32_16x16x32_bf16 v[60:63], a[156:159], v[200:203], v[60:63]\n"
"buffer_load_dwordx4 v226, s[40:43], s72 offen nt lds\n"
"v_cvt_pk_bf16_f32 v192, v192, v193\n"
"v_mfma_f32_16x16x32_bf16 a[164:167], a[184:187], v[200:203], a[164:167]\n"
"buffer_load_dwordx4 v226, s[40:43], s72 offen offset:2048 nt lds\n"
"v_mul_f32 v195, s62, v195\n"
"v_mfma_f32_16x16x32_bf16 v[200:203], a[96:99], a[64:67], 0\n"
"v_mul_f32 v196, s62, v196\n"
"s_add_u32 m0, s56, 49152\n"
"v_mfma_f32_16x16x32_bf16 v[200:203], a[100:103], a[68:71], v[200:203]\n"
"buffer_load_dwordx4 v226, s[44:47], s72 offen nt lds\n"
"v_mul_f32 v197, s62, v197\n"
"v_mfma_f32_16x16x32_bf16 v[200:203], a[104:107], a[72:75], v[200:203]\n"
"buffer_load_dwordx4 v226, s[44:47], s72 offen offset:2048 nt lds\n"
"v_exp_f32 v194, v194\n"
"v_mfma_f32_16x16x32_bf16 v[200:203], a[108:111], a[76:79], v[200:203]\n"
"v_exp_f32 v215, v215\n"
"v_mfma_f32_16x16x32_bf16 v[204:207], a[112:115], a[64:67], 0\n"
"v_cvt_pk_bf16_f32 v211, v214, v215\n"
"v_mul_f32 v198, s62, v198\n"
"v_mfma_f32_16x16x32_bf16 v[204:207], a[116:119], a[68:71], v[204:207]\n"
"v_exp_f32 v195, v195\n"
"v_mfma_f32_16x16x32_bf16 v[204:207], a[120:123], a[72:75], v[204:207]\n"
"v_cvt_pk_bf16_f32 v193, v194, v195\n"
"v_mul_f32 v200, s62, v200\n"
"v_mfma_f32_16x16x32_bf16 v[204:207], a[124:127], a[76:79], v[204:207]\n"
"v_exp_f32 v196, v196\n"
"v_mfma_f32_16x16x32_bf16 v[64:67], a[128:131], v[208:211], v[64:67]\n"
"v_exp_f32 v200, v200\n"
"v_mfma_f32_16x16x32_bf16 v[68:71], a[132:135], v[208:211], v[68:71]\n"
"v_exp_f32 v197, v197\n"
"v_mfma_f32_16x16x32_bf16 v[72:75], a[136:139], v[208:211], v[72:75]\n"
"v_mul_f32 v201, s62, v201\n"
"v_cvt_pk_bf16_f32 v194, v196, v197\n"
"v_mfma_f32_16x16x32_bf16 v[76:79], a[140:143], v[208:211], v[76:79]\n"
"v_exp_f32 v201, v201\n"
"v_mfma_f32_16x16x32_bf16 v[80:83], a[144:147], v[208:211], v[80:83]\n"
"v_exp_f32 v198, v198\n"
"v_mfma_f32_16x16x32_bf16 v[84:87], a[148:151], v[208:211], v[84:87]\n"
"v_mul_f32 v202, s62, v202\n"
"v_cvt_pk_bf16_f32 v200, v200, v201\n"
"v_mfma_f32_16x16x32_bf16 v[88:91], a[152:155], v[208:211], v[88:91]\n"
"v_mul_f32 v199, s62, v199\n"
"v_mul_f32 v203, s62, v203\n"
"v_mfma_f32_16x16x32_bf16 v[92:95], a[156:159], v[208:211], v[92:95]\n"
"v_exp_f32 v202, v202\n"
"v_mfma_f32_16x16x32_bf16 a[168:171], a[184:187], v[208:211], a[168:171]\n"
"v_exp_f32 v199, v199\n"
"v_mfma_f32_16x16x32_bf16 v[208:211], a[96:99], a[80:83], 0\n"
"ds_read_b128 a[96:99], v224 offset:24576\n"
"v_cvt_pk_bf16_f32 v195, v198, v199\n"
"v_mfma_f32_16x16x32_bf16 v[208:211], a[100:103], a[84:87], v[208:211]\n"
"v_exp_f32 v203, v203\n"
"v_mfma_f32_16x16x32_bf16 v[208:211], a[104:107], a[88:91], v[208:211]\n"
"ds_read_b128 a[100:103], v224 offset:25088\n"
"v_mul_f32 v204, s62, v204\n"
"v_mfma_f32_16x16x32_bf16 v[208:211], a[108:111], a[92:95], v[208:211]\n"
"v_cvt_pk_bf16_f32 v201, v202, v203\n"
"v_mul_f32 v205, s62, v205\n"
"v_mfma_f32_16x16x32_bf16 v[212:215], a[112:115], a[80:83], 0\n"
"ds_read_b128 a[104:107], v224 offset:25600\n"
"v_mul_f32 v206, s62, v206\n"
"v_mfma_f32_16x16x32_bf16 v[212:215], a[116:119], a[84:87], v[212:215]\n"
"v_exp_f32 v204, v204\n"
"v_mfma_f32_16x16x32_bf16 v[212:215], a[120:123], a[88:91], v[212:215]\n"
"ds_read_b128 a[108:111], v224 offset:26112\n"
"v_mul_f32 v208, s62, v208\n"
"v_mfma_f32_16x16x32_bf16 v[212:215], a[124:127], a[92:95], v[212:215]\n"
"v_exp_f32 v208, v208\n"
"v_mfma_f32_16x16x32_bf16 v[96:99], a[128:131], v[192:195], v[96:99]\n"
"ds_read_b128 a[112:115], v224 offset:28672\n"
"v_mul_f32 v209, s62, v209\n"
"v_mfma_f32_16x16x32_bf16 v[100:103], a[132:135], v[192:195], v[100:103]\n"
"v_exp_f32 v205, v205\n"
"v_mfma_f32_16x16x32_bf16 v[104:107], a[136:139], v[192:195], v[104:107]\n"
"ds_read_b128 a[116:119], v224 offset:29184\n"
"v_cvt_pk_bf16_f32 v202, v204, v205\n"
"v_mfma_f32_16x16x32_bf16 v[108:111], a[140:143], v[192:195], v[108:111]\n"
"v_exp_f32 v209, v209\n"
"v_mfma_f32_16x16x32_bf16 v[112:115], a[144:147], v[192:195], v[112:115]\n"
"ds_read_b128 a[120:123], v224 offset:29696\n"
"v_mul_f32 v210, s62, v210\n"
"v_mfma_f32_16x16x32_bf16 v[116:119], a[148:151], v[192:195], v[116:119]\n"
"v_exp_f32 v206, v206\n"
"v_mfma_f32_16x16x32_bf16 v[120:123], a[152:155], v[192:195], v[120:123]\n"
"ds_read_b128 a[124:127], v224 offset:30208\n"
"v_cvt_pk_bf16_f32 v208, v208, v209\n"
"v_mfma_f32_16x16x32_bf16 v[124:127], a[156:159], v[192:195], v[124:127]\n"
"v_mul_f32 v207, s62, v207\n"
"v_mul_f32 v211, s62, v211\n"
"v_mfma_f32_16x16x32_bf16 a[172:175], a[184:187], v[192:195], a[172:175]\n"
"v_mul_f32 v212, s62, v212\n"
"s_waitcnt lgkmcnt(2)\n"
"v_mfma_f32_16x16x32_bf16 v[192:195], a[96:99], a[0:3], 0\n"
"v_exp_f32 v210, v210\n"
"v_mfma_f32_16x16x32_bf16 v[192:195], a[100:103], a[4:7], v[192:195]\n"
"ds_read_b128 a[236:239], v230 offset:12288\n"
"v_mul_f32 v213, s62, v213\n"
"v_mfma_f32_16x16x32_bf16 v[192:195], a[104:107], a[8:11], v[192:195]\n"
"v_exp_f32 v207, v207\n"
"v_mfma_f32_16x16x32_bf16 v[192:195], a[108:111], a[12:15], v[192:195]\n"
"ds_read_b128 a[240:243], v231 offset:13312\n"
"v_cvt_pk_bf16_f32 v203, v206, v207\n"
"v_mfma_f32_16x16x32_bf16 v[196:199], a[112:115], a[0:3], 0\n"
"v_exp_f32 v211, v211\n"
"v_mfma_f32_16x16x32_bf16 v[196:199], a[116:119], a[4:7], v[196:199]\n"
"v_cvt_pk_bf16_f32 v209, v210, v211\n"
"s_waitcnt lgkmcnt(2)\n"
"v_mfma_f32_16x16x32_bf16 v[196:199], a[120:123], a[8:11], v[196:199]\n"
"v_exp_f32 v212, v212\n"
"v_mfma_f32_16x16x32_bf16 v[196:199], a[124:127], a[12:15], v[196:199]\n"
"v_mul_f32 v192, s62, v192\n"
"v_mul_f32 v193, s62, v193\n"
"v_mfma_f32_16x16x32_bf16 v[128:131], a[128:131], v[200:203], v[128:131]\n"
"v_exp_f32 v192, v192\n"
"v_mfma_f32_16x16x32_bf16 v[132:135], a[132:135], v[200:203], v[132:135]\n"
"v_exp_f32 v213, v213\n"
"v_mfma_f32_16x16x32_bf16 v[136:139], a[136:139], v[200:203], v[136:139]\n"
"v_mul_f32 v214, s62, v214\n"
"v_cvt_pk_bf16_f32 v210, v212, v213\n"
"v_mfma_f32_16x16x32_bf16 v[140:143], a[140:143], v[200:203], v[140:143]\n"
"v_exp_f32 v193, v193\n"
"v_mfma_f32_16x16x32_bf16 v[144:147], a[144:147], v[200:203], v[144:147]\n"
"v_exp_f32 v214, v214\n"
"v_mfma_f32_16x16x32_bf16 v[148:151], a[148:151], v[200:203], v[148:151]\n"
"v_mul_f32 v194, s62, v194\n"
"v_cvt_pk_bf16_f32 v192, v192, v193\n"
"v_mfma_f32_16x16x32_bf16 v[152:155], a[152:155], v[200:203], v[152:155]\n"
"v_mul_f32 v215, s62, v215\n"
"v_mul_f32 v195, s62, v195\n"
"v_mfma_f32_16x16x32_bf16 v[156:159], a[156:159], v[200:203], v[156:159]\n"
"v_exp_f32 v194, v194\n"
"v_mfma_f32_16x16x32_bf16 a[176:179], a[184:187], v[200:203], a[176:179]\n"
"v_exp_f32 v215, v215\n"
"v_mfma_f32_16x16x32_bf16 v[200:203], a[96:99], a[16:19], 0\n"
"v_cvt_pk_bf16_f32 v211, v214, v215\n"
"v_mul_f32 v196, s62, v196\n"
"v_mfma_f32_16x16x32_bf16 v[200:203], a[100:103], a[20:23], v[200:203]\n"
"v_exp_f32 v195, v195\n"
"v_mfma_f32_16x16x32_bf16 v[200:203], a[104:107], a[24:27], v[200:203]\n"
"v_cvt_pk_bf16_f32 v193, v194, v195\n"
"v_mul_f32 v197, s62, v197\n"
"v_mfma_f32_16x16x32_bf16 v[200:203], a[108:111], a[28:31], v[200:203]\n"
"v_exp_f32 v196, v196\n"
"v_mfma_f32_16x16x32_bf16 v[204:207], a[112:115], a[16:19], 0\n"
"v_exp_f32 v197, v197\n"
"v_mfma_f32_16x16x32_bf16 v[204:207], a[116:119], a[20:23], v[204:207]\n"
"v_mul_f32 v198, s62, v198\n"
"v_cvt_pk_bf16_f32 v194, v196, v197\n"
"v_mfma_f32_16x16x32_bf16 v[204:207], a[120:123], a[24:27], v[204:207]\n"
"v_exp_f32 v198, v198\n"
"v_mfma_f32_16x16x32_bf16 v[204:207], a[124:127], a[28:31], v[204:207]\n"
"v_mul_f32 v200, s62, v200\n"
"v_mul_f32 v201, s62, v201\n"
"v_mfma_f32_16x16x32_bf16 v[160:163], a[128:131], v[208:211], v[160:163]\n"
"ds_read_b64_tr_b16 a[128:129], v228 offset:24576\n"
"v_mul_f32 v202, s62, v202\n"
"v_mfma_f32_16x16x32_bf16 v[164:167], a[132:135], v[208:211], v[164:167]\n"
"ds_read_b64_tr_b16 a[130:131], v228 offset:28672\n"
"v_mul_f32 v199, s62, v199\n"
"v_mfma_f32_16x16x32_bf16 v[168:171], a[136:139], v[208:211], v[168:171]\n"
"ds_read_b64_tr_b16 a[132:133], v229 offset:24576\n"
"v_mul_f32 v203, s62, v203\n"
"v_mfma_f32_16x16x32_bf16 v[172:175], a[140:143], v[208:211], v[172:175]\n"
"ds_read_b64_tr_b16 a[134:135], v229 offset:28672\n"
"v_mul_f32 v204, s62, v204\n"
"v_mfma_f32_16x16x32_bf16 v[176:179], a[144:147], v[208:211], v[176:179]\n"
"ds_read_b64_tr_b16 a[136:137], v228 offset:25088\n"
"v_exp_f32 v200, v200\n"
"v_mfma_f32_16x16x32_bf16 v[180:183], a[148:151], v[208:211], v[180:183]\n"
"ds_read_b64_tr_b16 a[138:139], v228 offset:29184\n"
"v_mul_f32 v205, s62, v205\n"
"v_mfma_f32_16x16x32_bf16 v[184:187], a[152:155], v[208:211], v[184:187]\n"
"ds_read_b64_tr_b16 a[140:141], v229 offset:25088\n"
"v_mul_f32 v206, s62, v206\n"
"v_mfma_f32_16x16x32_bf16 v[188:191], a[156:159], v[208:211], v[188:191]\n"
"ds_read_b64_tr_b16 a[142:143], v229 offset:29184\n"
"v_exp_f32 v201, v201\n"
"v_mfma_f32_16x16x32_bf16 a[180:183], a[184:187], v[208:211], a[180:183]\n"
"ds_read_b64_tr_b16 a[144:145], v228 offset:25600\n"
"v_cvt_pk_bf16_f32 v200, v200, v201\n"
"v_mfma_f32_16x16x32_bf16 v[208:211], a[96:99], a[32:35], 0\n"
"ds_read_b64_tr_b16 a[146:147], v228 offset:29696\n"
"v_mul_f32 v207, s62, v207\n"
"v_mfma_f32_16x16x32_bf16 v[208:211], a[100:103], a[36:39], v[208:211]\n"
"ds_read_b64_tr_b16 a[148:149], v229 offset:25600\n"
"v_mfma_f32_16x16x32_bf16 v[208:211], a[104:107], a[40:43], v[208:211]\n"
"ds_read_b64_tr_b16 a[150:151], v229 offset:29696\n"
"v_exp_f32 v202, v202\n"
"v_mfma_f32_16x16x32_bf16 v[208:211], a[108:111], a[44:47], v[208:211]\n"
"ds_read_b64_tr_b16 a[152:153], v228 offset:26112\n"
"v_exp_f32 v199, v199\n"
"v_mfma_f32_16x16x32_bf16 v[212:215], a[112:115], a[32:35], 0\n"
"ds_read_b64_tr_b16 a[154:155], v228 offset:30208\n"
"v_cvt_pk_bf16_f32 v195, v198, v199\n"
"v_mfma_f32_16x16x32_bf16 v[212:215], a[116:119], a[36:39], v[212:215]\n"
"ds_read_b64_tr_b16 a[156:157], v229 offset:26112\n"
"v_exp_f32 v203, v203\n"
"v_mfma_f32_16x16x32_bf16 v[212:215], a[120:123], a[40:43], v[212:215]\n"
"ds_read_b64_tr_b16 a[158:159], v229 offset:30208\n"
"v_cvt_pk_bf16_f32 v201, v202, v203\n"
"v_mfma_f32_16x16x32_bf16 v[212:215], a[124:127], a[44:47], v[212:215]\n"
"v_mul_f32 v208, s62, v208\n"
"s_waitcnt lgkmcnt(5)\n"
"v_mfma_f32_16x16x32_bf16 v[0:3], a[128:131], v[192:195], v[0:3]\n"
"v_mul_f32 v209, s62, v209\n"
"s_add_u32 s61, s59, 12288\n"
"v_mfma_f32_16x16x32_bf16 v[4:7], a[132:135], v[192:195], v[4:7]\n"
"buffer_store_dwordx4 a[236:239], v227, s[48:51], s61 offen sc1 nt\n"
"v_exp_f32 v204, v204\n"
"v_mfma_f32_16x16x32_bf16 v[8:11], a[136:139], v[192:195], v[8:11]\n"
"v_exp_f32 v208, v208\n"
"v_mfma_f32_16x16x32_bf16 v[12:15], a[140:143], v[192:195], v[12:15]\n"
"buffer_store_dwordx4 a[240:243], v227, s[48:51], s61 offen offset:1024 sc1 nt\n"
"v_mul_f32 v210, s62, v210\n"
"v_mfma_f32_16x16x32_bf16 v[16:19], a[144:147], v[192:195], v[16:19]\n"
"v_mul_f32 v211, s62, v211\n"
"s_waitcnt lgkmcnt(0)\n"
"v_mfma_f32_16x16x32_bf16 v[20:23], a[148:151], v[192:195], v[20:23]\n"
"v_exp_f32 v205, v205\n"
"v_mfma_f32_16x16x32_bf16 v[24:27], a[152:155], v[192:195], v[24:27]\n"
"v_cvt_pk_bf16_f32 v202, v204, v205\n"
"v_mul_f32 v212, s62, v212\n"
"v_mfma_f32_16x16x32_bf16 v[28:31], a[156:159], v[192:195], v[28:31]\n"
"v_exp_f32 v209, v209\n"
"v_mfma_f32_16x16x32_bf16 a[160:163], a[184:187], v[192:195], a[160:163]\n"
"v_exp_f32 v206, v206\n"
"v_mfma_f32_16x16x32_bf16 v[192:195], a[96:99], a[48:51], 0\n"
"v_cvt_pk_bf16_f32 v208, v208, v209\n"
"v_mul_f32 v213, s62, v213\n"
"v_mfma_f32_16x16x32_bf16 v[192:195], a[100:103], a[52:55], v[192:195]\n"
"v_exp_f32 v210, v210\n"
"v_mfma_f32_16x16x32_bf16 v[192:195], a[104:107], a[56:59], v[192:195]\n"
"v_exp_f32 v207, v207\n"
"v_mfma_f32_16x16x32_bf16 v[192:195], a[108:111], a[60:63], v[192:195]\n"
"v_cvt_pk_bf16_f32 v203, v206, v207\n"
"v_mul_f32 v214, s62, v214\n"
"v_mfma_f32_16x16x32_bf16 v[196:199], a[112:115], a[48:51], 0\n"
"v_exp_f32 v211, v211\n"
"v_mfma_f32_16x16x32_bf16 v[196:199], a[116:119], a[52:55], v[196:199]\n"
"v_cvt_pk_bf16_f32 v209, v210, v211\n"
"v_mul_f32 v215, s62, v215\n"
"v_mfma_f32_16x16x32_bf16 v[196:199], a[120:123], a[56:59], v[196:199]\n"
"v_exp_f32 v212, v212\n"
"v_mfma_f32_16x16x32_bf16 v[196:199], a[124:127], a[60:63], v[196:199]\n"
"v_mul_f32 v192, s62, v192\n"
"v_mul_f32 v193, s62, v193\n"
"v_mfma_f32_16x16x32_bf16 v[32:35], a[128:131], v[200:203], v[32:35]\n"
"v_exp_f32 v192, v192\n"
"v_mfma_f32_16x16x32_bf16 v[36:39], a[132:135], v[200:203], v[36:39]\n"
"v_exp_f32 v213, v213\n"
"v_mfma_f32_16x16x32_bf16 v[40:43], a[136:139], v[200:203], v[40:43]\n"
"v_cvt_pk_bf16_f32 v210, v212, v213\n"
"v_mul_f32 v194, s62, v194\n"
"v_mfma_f32_16x16x32_bf16 v[44:47], a[140:143], v[200:203], v[44:47]\n"
"v_exp_f32 v193, v193\n"
"v_mfma_f32_16x16x32_bf16 v[48:51], a[144:147], v[200:203], v[48:51]\n"
"s_waitcnt vmcnt(14)\n"
"s_barrier\n"
"v_mfma_f32_16x16x32_bf16 v[52:55], a[148:151], v[200:203], v[52:55]\n"
"v_exp_f32 v214, v214\n"
"v_mfma_f32_16x16x32_bf16 v[56:59], a[152:155], v[200:203], v[56:59]\n"
"s_add_u32 m0, s56, 24576\n"
"s_add_u32 s72, s57, 188416\n"
"v_mfma_f32_16x16x32_bf16 v[60:63], a[156:159], v[200:203], v[60:63]\n"
"buffer_load_dwordx4 v226, s[40:43], s72 offen nt lds\n"
"v_cvt_pk_bf16_f32 v192, v192, v193\n"
"v_mfma_f32_16x16x32_bf16 a[164:167], a[184:187], v[200:203], a[164:167]\n"
"buffer_load_dwordx4 v226, s[40:43], s72 offen offset:2048 nt lds\n"
"v_mul_f32 v195, s62, v195\n"
"v_mfma_f32_16x16x32_bf16 v[200:203], a[96:99], a[64:67], 0\n"
"v_mul_f32 v196, s62, v196\n"
"s_add_u32 m0, s56, 57344\n"
"v_mfma_f32_16x16x32_bf16 v[200:203], a[100:103], a[68:71], v[200:203]\n"
"buffer_load_dwordx4 v226, s[44:47], s72 offen nt lds\n"
"v_mul_f32 v197, s62, v197\n"
"v_mfma_f32_16x16x32_bf16 v[200:203], a[104:107], a[72:75], v[200:203]\n"
"buffer_load_dwordx4 v226, s[44:47], s72 offen offset:2048 nt lds\n"
"v_exp_f32 v194, v194\n"
"v_mfma_f32_16x16x32_bf16 v[200:203], a[108:111], a[76:79], v[200:203]\n"
"v_exp_f32 v215, v215\n"
"v_mfma_f32_16x16x32_bf16 v[204:207], a[112:115], a[64:67], 0\n"
"v_cvt_pk_bf16_f32 v211, v214, v215\n"
"v_mul_f32 v198, s62, v198\n"
"v_mfma_f32_16x16x32_bf16 v[204:207], a[116:119], a[68:71], v[204:207]\n"
"v_exp_f32 v195, v195\n"
"v_mfma_f32_16x16x32_bf16 v[204:207], a[120:123], a[72:75], v[204:207]\n"
"v_cvt_pk_bf16_f32 v193, v194, v195\n"
"v_mul_f32 v200, s62, v200\n"
"v_mfma_f32_16x16x32_bf16 v[204:207], a[124:127], a[76:79], v[204:207]\n"
"v_exp_f32 v196, v196\n"
"v_mfma_f32_16x16x32_bf16 v[64:67], a[128:131], v[208:211], v[64:67]\n"
"v_exp_f32 v200, v200\n"
"v_mfma_f32_16x16x32_bf16 v[68:71], a[132:135], v[208:211], v[68:71]\n"
"v_exp_f32 v197, v197\n"
"v_mfma_f32_16x16x32_bf16 v[72:75], a[136:139], v[208:211], v[72:75]\n"
"v_mul_f32 v201, s62, v201\n"
"v_cvt_pk_bf16_f32 v194, v196, v197\n"
"v_mfma_f32_16x16x32_bf16 v[76:79], a[140:143], v[208:211], v[76:79]\n"
"v_exp_f32 v201, v201\n"
"v_mfma_f32_16x16x32_bf16 v[80:83], a[144:147], v[208:211], v[80:83]\n"
"v_exp_f32 v198, v198\n"
"v_mfma_f32_16x16x32_bf16 v[84:87], a[148:151], v[208:211], v[84:87]\n"
"v_mul_f32 v202, s62, v202\n"
"v_cvt_pk_bf16_f32 v200, v200, v201\n"
"v_mfma_f32_16x16x32_bf16 v[88:91], a[152:155], v[208:211], v[88:91]\n"
"v_mul_f32 v199, s62, v199\n"
"v_mul_f32 v203, s62, v203\n"
"v_mfma_f32_16x16x32_bf16 v[92:95], a[156:159], v[208:211], v[92:95]\n"
"v_exp_f32 v202, v202\n"
"v_mfma_f32_16x16x32_bf16 a[168:171], a[184:187], v[208:211], a[168:171]\n"
"v_exp_f32 v199, v199\n"
"v_mfma_f32_16x16x32_bf16 v[208:211], a[96:99], a[80:83], 0\n"
"ds_read_b128 a[96:99], v224 offset:0\n"
"v_cvt_pk_bf16_f32 v195, v198, v199\n"
"v_mfma_f32_16x16x32_bf16 v[208:211], a[100:103], a[84:87], v[208:211]\n"
"v_exp_f32 v203, v203\n"
"v_mfma_f32_16x16x32_bf16 v[208:211], a[104:107], a[88:91], v[208:211]\n"
"ds_read_b128 a[100:103], v224 offset:512\n"
"v_mul_f32 v204, s62, v204\n"
"v_mfma_f32_16x16x32_bf16 v[208:211], a[108:111], a[92:95], v[208:211]\n"
"v_cvt_pk_bf16_f32 v201, v202, v203\n"
"v_mul_f32 v205, s62, v205\n"
"v_mfma_f32_16x16x32_bf16 v[212:215], a[112:115], a[80:83], 0\n"
"ds_read_b128 a[104:107], v224 offset:1024\n"
"v_mul_f32 v206, s62, v206\n"
"v_mfma_f32_16x16x32_bf16 v[212:215], a[116:119], a[84:87], v[212:215]\n"
"v_exp_f32 v204, v204\n"
"v_mfma_f32_16x16x32_bf16 v[212:215], a[120:123], a[88:91], v[212:215]\n"
"ds_read_b128 a[108:111], v224 offset:1536\n"
"v_mul_f32 v208, s62, v208\n"
"v_mfma_f32_16x16x32_bf16 v[212:215], a[124:127], a[92:95], v[212:215]\n"
"v_exp_f32 v208, v208\n"
"v_mfma_f32_16x16x32_bf16 v[96:99], a[128:131], v[192:195], v[96:99]\n"
"ds_read_b128 a[112:115], v224 offset:4096\n"
"v_mul_f32 v209, s62, v209\n"
"v_mfma_f32_16x16x32_bf16 v[100:103], a[132:135], v[192:195], v[100:103]\n"
"v_exp_f32 v205, v205\n"
"v_mfma_f32_16x16x32_bf16 v[104:107], a[136:139], v[192:195], v[104:107]\n"
"ds_read_b128 a[116:119], v224 offset:4608\n"
"v_cvt_pk_bf16_f32 v202, v204, v205\n"
"v_mfma_f32_16x16x32_bf16 v[108:111], a[140:143], v[192:195], v[108:111]\n"
"v_exp_f32 v209, v209\n"
"v_mfma_f32_16x16x32_bf16 v[112:115], a[144:147], v[192:195], v[112:115]\n"
"ds_read_b128 a[120:123], v224 offset:5120\n"
"v_mul_f32 v210, s62, v210\n"
"v_mfma_f32_16x16x32_bf16 v[116:119], a[148:151], v[192:195], v[116:119]\n"
"v_exp_f32 v206, v206\n"
"v_mfma_f32_16x16x32_bf16 v[120:123], a[152:155], v[192:195], v[120:123]\n"
"ds_read_b128 a[124:127], v224 offset:5632\n"
"v_cvt_pk_bf16_f32 v208, v208, v209\n"
"v_mfma_f32_16x16x32_bf16 v[124:127], a[156:159], v[192:195], v[124:127]\n"
"v_mul_f32 v207, s62, v207\n"
"v_mul_f32 v211, s62, v211\n"
"v_mfma_f32_16x16x32_bf16 a[172:175], a[184:187], v[192:195], a[172:175]\n"
"v_mul_f32 v212, s62, v212\n"
"s_waitcnt lgkmcnt(2)\n"
"v_mfma_f32_16x16x32_bf16 v[192:195], a[96:99], a[0:3], 0\n"
"v_exp_f32 v210, v210\n"
"v_mfma_f32_16x16x32_bf16 v[192:195], a[100:103], a[4:7], v[192:195]\n"
"ds_read_b128 a[244:247], v230 offset:14336\n"
"v_mul_f32 v213, s62, v213\n"
"v_mfma_f32_16x16x32_bf16 v[192:195], a[104:107], a[8:11], v[192:195]\n"
"v_exp_f32 v207, v207\n"
"v_mfma_f32_16x16x32_bf16 v[192:195], a[108:111], a[12:15], v[192:195]\n"
"ds_read_b128 a[248:251], v231 offset:15360\n"
"v_cvt_pk_bf16_f32 v203, v206, v207\n"
"v_mfma_f32_16x16x32_bf16 v[196:199], a[112:115], a[0:3], 0\n"
"v_exp_f32 v211, v211\n"
"v_mfma_f32_16x16x32_bf16 v[196:199], a[116:119], a[4:7], v[196:199]\n"
"v_cvt_pk_bf16_f32 v209, v210, v211\n"
"s_waitcnt lgkmcnt(2)\n"
"v_mfma_f32_16x16x32_bf16 v[196:199], a[120:123], a[8:11], v[196:199]\n"
"v_exp_f32 v212, v212\n"
"v_mfma_f32_16x16x32_bf16 v[196:199], a[124:127], a[12:15], v[196:199]\n"
"v_mul_f32 v192, s62, v192\n"
"v_mul_f32 v193, s62, v193\n"
"v_mfma_f32_16x16x32_bf16 v[128:131], a[128:131], v[200:203], v[128:131]\n"
"v_exp_f32 v192, v192\n"
"v_mfma_f32_16x16x32_bf16 v[132:135], a[132:135], v[200:203], v[132:135]\n"
"v_exp_f32 v213, v213\n"
"v_mfma_f32_16x16x32_bf16 v[136:139], a[136:139], v[200:203], v[136:139]\n"
"v_mul_f32 v214, s62, v214\n"
"v_cvt_pk_bf16_f32 v210, v212, v213\n"
"v_mfma_f32_16x16x32_bf16 v[140:143], a[140:143], v[200:203], v[140:143]\n"
"v_exp_f32 v193, v193\n"
"v_mfma_f32_16x16x32_bf16 v[144:147], a[144:147], v[200:203], v[144:147]\n"
"v_exp_f32 v214, v214\n"
"v_mfma_f32_16x16x32_bf16 v[148:151], a[148:151], v[200:203], v[148:151]\n"
"v_mul_f32 v194, s62, v194\n"
"v_cvt_pk_bf16_f32 v192, v192, v193\n"
"v_mfma_f32_16x16x32_bf16 v[152:155], a[152:155], v[200:203], v[152:155]\n"
"v_mul_f32 v215, s62, v215\n"
"v_mul_f32 v195, s62, v195\n"
"v_mfma_f32_16x16x32_bf16 v[156:159], a[156:159], v[200:203], v[156:159]\n"
"v_exp_f32 v194, v194\n"
"v_mfma_f32_16x16x32_bf16 a[176:179], a[184:187], v[200:203], a[176:179]\n"
"v_exp_f32 v215, v215\n"
"v_mfma_f32_16x16x32_bf16 v[200:203], a[96:99], a[16:19], 0\n"
"v_cvt_pk_bf16_f32 v211, v214, v215\n"
"v_mul_f32 v196, s62, v196\n"
"v_mfma_f32_16x16x32_bf16 v[200:203], a[100:103], a[20:23], v[200:203]\n"
"v_exp_f32 v195, v195\n"
"v_mfma_f32_16x16x32_bf16 v[200:203], a[104:107], a[24:27], v[200:203]\n"
"v_cvt_pk_bf16_f32 v193, v194, v195\n"
"v_mul_f32 v197, s62, v197\n"
"v_mfma_f32_16x16x32_bf16 v[200:203], a[108:111], a[28:31], v[200:203]\n"
"v_exp_f32 v196, v196\n"
"v_mfma_f32_16x16x32_bf16 v[204:207], a[112:115], a[16:19], 0\n"
"v_exp_f32 v197, v197\n"
"v_mfma_f32_16x16x32_bf16 v[204:207], a[116:119], a[20:23], v[204:207]\n"
"v_mul_f32 v198, s62, v198\n"
"v_cvt_pk_bf16_f32 v194, v196, v197\n"
"v_mfma_f32_16x16x32_bf16 v[204:207], a[120:123], a[24:27], v[204:207]\n"
"v_exp_f32 v198, v198\n"
"v_mfma_f32_16x16x32_bf16 v[204:207], a[124:127], a[28:31], v[204:207]\n"
"v_mul_f32 v200, s62, v200\n"
"v_mul_f32 v201, s62, v201\n"
"v_mfma_f32_16x16x32_bf16 v[160:163], a[128:131], v[208:211], v[160:163]\n"
"ds_read_b64_tr_b16 a[128:129], v228 offset:0\n"
"v_mul_f32 v202, s62, v202\n"
"v_mfma_f32_16x16x32_bf16 v[164:167], a[132:135], v[208:211], v[164:167]\n"
"ds_read_b64_tr_b16 a[130:131], v228 offset:4096\n"
"v_mul_f32 v199, s62, v199\n"
"v_mfma_f32_16x16x32_bf16 v[168:171], a[136:139], v[208:211], v[168:171]\n"
"ds_read_b64_tr_b16 a[132:133], v229 offset:0\n"
"v_mul_f32 v203, s62, v203\n"
"v_mfma_f32_16x16x32_bf16 v[172:175], a[140:143], v[208:211], v[172:175]\n"
"ds_read_b64_tr_b16 a[134:135], v229 offset:4096\n"
"v_mul_f32 v204, s62, v204\n"
"v_mfma_f32_16x16x32_bf16 v[176:179], a[144:147], v[208:211], v[176:179]\n"
"ds_read_b64_tr_b16 a[136:137], v228 offset:512\n"
"v_exp_f32 v200, v200\n"
"v_mfma_f32_16x16x32_bf16 v[180:183], a[148:151], v[208:211], v[180:183]\n"
"ds_read_b64_tr_b16 a[138:139], v228 offset:4608\n"
"v_mul_f32 v205, s62, v205\n"
"v_mfma_f32_16x16x32_bf16 v[184:187], a[152:155], v[208:211], v[184:187]\n"
"ds_read_b64_tr_b16 a[140:141], v229 offset:512\n"
"v_mul_f32 v206, s62, v206\n"
"v_mfma_f32_16x16x32_bf16 v[188:191], a[156:159], v[208:211], v[188:191]\n"
"ds_read_b64_tr_b16 a[142:143], v229 offset:4608\n"
"v_exp_f32 v201, v201\n"
"v_mfma_f32_16x16x32_bf16 a[180:183], a[184:187], v[208:211], a[180:183]\n"
"ds_read_b64_tr_b16 a[144:145], v228 offset:1024\n"
"v_cvt_pk_bf16_f32 v200, v200, v201\n"
"v_mfma_f32_16x16x32_bf16 v[208:211], a[96:99], a[32:35], 0\n"
"ds_read_b64_tr_b16 a[146:147], v228 offset:5120\n"
"v_mul_f32 v207, s62, v207\n"
"v_mfma_f32_16x16x32_bf16 v[208:211], a[100:103], a[36:39], v[208:211]\n"
"ds_read_b64_tr_b16 a[148:149], v229 offset:1024\n"
"v_mfma_f32_16x16x32_bf16 v[208:211], a[104:107], a[40:43], v[208:211]\n"
"ds_read_b64_tr_b16 a[150:151], v229 offset:5120\n"
"v_exp_f32 v202, v202\n"
"v_mfma_f32_16x16x32_bf16 v[208:211], a[108:111], a[44:47], v[208:211]\n"
"ds_read_b64_tr_b16 a[152:153], v228 offset:1536\n"
"v_exp_f32 v199, v199\n"
"v_mfma_f32_16x16x32_bf16 v[212:215], a[112:115], a[32:35], 0\n"
"ds_read_b64_tr_b16 a[154:155], v228 offset:5632\n"
"v_cvt_pk_bf16_f32 v195, v198, v199\n"
"v_mfma_f32_16x16x32_bf16 v[212:215], a[116:119], a[36:39], v[212:215]\n"
"ds_read_b64_tr_b16 a[156:157], v229 offset:1536\n"
"v_exp_f32 v203, v203\n"
"v_mfma_f32_16x16x32_bf16 v[212:215], a[120:123], a[40:43], v[212:215]\n"
"ds_read_b64_tr_b16 a[158:159], v229 offset:5632\n"
"v_cvt_pk_bf16_f32 v201, v202, v203\n"
"v_mfma_f32_16x16x32_bf16 v[212:215], a[124:127], a[44:47], v[212:215]\n"
"v_mul_f32 v208, s62, v208\n"
"s_waitcnt lgkmcnt(5)\n"
"v_mfma_f32_16x16x32_bf16 v[0:3], a[128:131], v[192:195], v[0:3]\n"
"v_exp_f32 v204, v204\n"
"v_mfma_f32_16x16x32_bf16 v[4:7], a[132:135], v[192:195], v[4:7]\n"
"buffer_store_dwordx4 a[244:247], v227, s[48:51], s61 offen offset:2048 sc1 nt\n"
"v_mul_f32 v209, s62, v209\n"
"v_mfma_f32_16x16x32_bf16 v[8:11], a[136:139], v[192:195], v[8:11]\n"
"v_exp_f32 v208, v208\n"
"v_mfma_f32_16x16x32_bf16 v[12:15], a[140:143], v[192:195], v[12:15]\n"
"buffer_store_dwordx4 a[248:251], v227, s[48:51], s61 offen offset:3072 sc1 nt\n"
"v_mul_f32 v210, s62, v210\n"
"v_mfma_f32_16x16x32_bf16 v[16:19], a[144:147], v[192:195], v[16:19]\n"
"v_mul_f32 v211, s62, v211\n"
"s_waitcnt lgkmcnt(0)\n"
"v_mfma_f32_16x16x32_bf16 v[20:23], a[148:151], v[192:195], v[20:23]\n"
"v_exp_f32 v205, v205\n"
"v_mfma_f32_16x16x32_bf16 v[24:27], a[152:155], v[192:195], v[24:27]\n"
"v_cvt_pk_bf16_f32 v202, v204, v205\n"
"v_mul_f32 v212, s62, v212\n"
"v_mfma_f32_16x16x32_bf16 v[28:31], a[156:159], v[192:195], v[28:31]\n"
"v_exp_f32 v209, v209\n"
"v_mfma_f32_16x16x32_bf16 a[160:163], a[184:187], v[192:195], a[160:163]\n"
"v_exp_f32 v206, v206\n"
"v_mfma_f32_16x16x32_bf16 v[192:195], a[96:99], a[48:51], 0\n"
"v_cvt_pk_bf16_f32 v208, v208, v209\n"
"v_mul_f32 v213, s62, v213\n"
"v_mfma_f32_16x16x32_bf16 v[192:195], a[100:103], a[52:55], v[192:195]\n"
"v_exp_f32 v210, v210\n"
"v_mfma_f32_16x16x32_bf16 v[192:195], a[104:107], a[56:59], v[192:195]\n"
"v_exp_f32 v207, v207\n"
"v_mfma_f32_16x16x32_bf16 v[192:195], a[108:111], a[60:63], v[192:195]\n"
"v_cvt_pk_bf16_f32 v203, v206, v207\n"
"v_mul_f32 v214, s62, v214\n"
"v_mfma_f32_16x16x32_bf16 v[196:199], a[112:115], a[48:51], 0\n"
"v_exp_f32 v211, v211\n"
"v_mfma_f32_16x16x32_bf16 v[196:199], a[116:119], a[52:55], v[196:199]\n"
"v_cvt_pk_bf16_f32 v209, v210, v211\n"
"v_mul_f32 v215, s62, v215\n"
"v_mfma_f32_16x16x32_bf16 v[196:199], a[120:123], a[56:59], v[196:199]\n"
"v_exp_f32 v212, v212\n"
"v_mfma_f32_16x16x32_bf16 v[196:199], a[124:127], a[60:63], v[196:199]\n"
"v_mul_f32 v192, s62, v192\n"
"v_mul_f32 v193, s62, v193\n"
"v_mfma_f32_16x16x32_bf16 v[32:35], a[128:131], v[200:203], v[32:35]\n"
"v_exp_f32 v192, v192\n"
"v_mfma_f32_16x16x32_bf16 v[36:39], a[132:135], v[200:203], v[36:39]\n"
"v_exp_f32 v213, v213\n"
"v_mfma_f32_16x16x32_bf16 v[40:43], a[136:139], v[200:203], v[40:43]\n"
"v_cvt_pk_bf16_f32 v210, v212, v213\n"
"v_mul_f32 v194, s62, v194\n"
"v_mfma_f32_16x16x32_bf16 v[44:47], a[140:143], v[200:203], v[44:47]\n"
"v_exp_f32 v193, v193\n"
"v_mfma_f32_16x16x32_bf16 v[48:51], a[144:147], v[200:203], v[48:51]\n"
"s_waitcnt vmcnt(14)\n"
"s_barrier\n"
"v_mfma_f32_16x16x32_bf16 v[52:55], a[148:151], v[200:203], v[52:55]\n"
"v_exp_f32 v214, v214\n"
"v_mfma_f32_16x16x32_bf16 v[56:59], a[152:155], v[200:203], v[56:59]\n"
"v_cvt_pk_bf16_f32 v192, v192, v193\n"
"v_mul_f32 v195, s62, v195\n"
"v_mfma_f32_16x16x32_bf16 v[60:63], a[156:159], v[200:203], v[60:63]\n"
"v_exp_f32 v194, v194\n"
"v_mfma_f32_16x16x32_bf16 a[164:167], a[184:187], v[200:203], a[164:167]\n"
"v_exp_f32 v215, v215\n"
"v_mfma_f32_16x16x32_bf16 v[200:203], a[96:99], a[64:67], 0\n"
"v_cvt_pk_bf16_f32 v211, v214, v215\n"
"v_mul_f32 v196, s62, v196\n"
"v_mfma_f32_16x16x32_bf16 v[200:203], a[100:103], a[68:71], v[200:203]\n"
"v_exp_f32 v195, v195\n"
"v_mfma_f32_16x16x32_bf16 v[200:203], a[104:107], a[72:75], v[200:203]\n"
"v_cvt_pk_bf16_f32 v193, v194, v195\n"
"v_mul_f32 v197, s62, v197\n"
"v_mfma_f32_16x16x32_bf16 v[200:203], a[108:111], a[76:79], v[200:203]\n"
"v_exp_f32 v196, v196\n"
"v_mfma_f32_16x16x32_bf16 v[204:207], a[112:115], a[64:67], 0\n"
"v_exp_f32 v197, v197\n"
"v_mfma_f32_16x16x32_bf16 v[204:207], a[116:119], a[68:71], v[204:207]\n"
"v_mul_f32 v198, s62, v198\n"
"v_cvt_pk_bf16_f32 v194, v196, v197\n"
"v_mfma_f32_16x16x32_bf16 v[204:207], a[120:123], a[72:75], v[204:207]\n"
"v_exp_f32 v198, v198\n"
"v_mfma_f32_16x16x32_bf16 v[204:207], a[124:127], a[76:79], v[204:207]\n"
"v_mul_f32 v200, s62, v200\n"
"v_mul_f32 v201, s62, v201\n"
"v_mfma_f32_16x16x32_bf16 v[64:67], a[128:131], v[208:211], v[64:67]\n"
"v_exp_f32 v200, v200\n"
"v_mfma_f32_16x16x32_bf16 v[68:71], a[132:135], v[208:211], v[68:71]\n"
"v_exp_f32 v201, v201\n"
"v_mfma_f32_16x16x32_bf16 v[72:75], a[136:139], v[208:211], v[72:75]\n"
"v_mul_f32 v202, s62, v202\n"
"v_cvt_pk_bf16_f32 v200, v200, v201\n"
"v_mfma_f32_16x16x32_bf16 v[76:79], a[140:143], v[208:211], v[76:79]\n"
"v_mul_f32 v199, s62, v199\n"
"v_mul_f32 v203, s62, v203\n"
"v_mfma_f32_16x16x32_bf16 v[80:83], a[144:147], v[208:211], v[80:83]\n"
"v_exp_f32 v202, v202\n"
"v_mfma_f32_16x16x32_bf16 v[84:87], a[148:151], v[208:211], v[84:87]\n"
"v_exp_f32 v199, v199\n"
"v_mfma_f32_16x16x32_bf16 v[88:91], a[152:155], v[208:211], v[88:91]\n"
"v_cvt_pk_bf16_f32 v195, v198, v199\n"
"v_mul_f32 v204, s62, v204\n"
"v_mfma_f32_16x16x32_bf16 v[92:95], a[156:159], v[208:211], v[92:95]\n"
"v_exp_f32 v203, v203\n"
"v_mfma_f32_16x16x32_bf16 a[168:171], a[184:187], v[208:211], a[168:171]\n"
"v_cvt_pk_bf16_f32 v201, v202, v203\n"
"v_mul_f32 v205, s62, v205\n"
"v_mfma_f32_16x16x32_bf16 v[208:211], a[96:99], a[80:83], 0\n"
"ds_read_b128 a[96:99], v224 offset:8192\n"
"v_mul_f32 v206, s62, v206\n"
"v_mfma_f32_16x16x32_bf16 v[208:211], a[100:103], a[84:87], v[208:211]\n"
"v_exp_f32 v204, v204\n"
"v_mfma_f32_16x16x32_bf16 v[208:211], a[104:107], a[88:91], v[208:211]\n"
"ds_read_b128 a[100:103], v224 offset:8704\n"
"v_mul_f32 v207, s62, v207\n"
"v_mfma_f32_16x16x32_bf16 v[208:211], a[108:111], a[92:95], v[208:211]\n"
"v_exp_f32 v205, v205\n"
"v_mfma_f32_16x16x32_bf16 v[212:215], a[112:115], a[80:83], 0\n"
"ds_read_b128 a[104:107], v224 offset:9216\n"
"v_cvt_pk_bf16_f32 v202, v204, v205\n"
"v_mfma_f32_16x16x32_bf16 v[212:215], a[116:119], a[84:87], v[212:215]\n"
"v_exp_f32 v206, v206\n"
"v_mfma_f32_16x16x32_bf16 v[212:215], a[120:123], a[88:91], v[212:215]\n"
"ds_read_b128 a[108:111], v224 offset:9728\n"
"v_mul_f32 v208, s62, v208\n"
"v_mfma_f32_16x16x32_bf16 v[212:215], a[124:127], a[92:95], v[212:215]\n"
"v_exp_f32 v208, v208\n"
"v_mfma_f32_16x16x32_bf16 v[96:99], a[128:131], v[192:195], v[96:99]\n"
"ds_read_b128 a[112:115], v224 offset:12288\n"
"v_mul_f32 v209, s62, v209\n"
"v_mfma_f32_16x16x32_bf16 v[100:103], a[132:135], v[192:195], v[100:103]\n"
"v_exp_f32 v209, v209\n"
"v_mfma_f32_16x16x32_bf16 v[104:107], a[136:139], v[192:195], v[104:107]\n"
"ds_read_b128 a[116:119], v224 offset:12800\n"
"v_mul_f32 v210, s62, v210\n"
"v_mfma_f32_16x16x32_bf16 v[108:111], a[140:143], v[192:195], v[108:111]\n"
"v_cvt_pk_bf16_f32 v208, v208, v209\n"
"v_mul_f32 v211, s62, v211\n"
"v_mfma_f32_16x16x32_bf16 v[112:115], a[144:147], v[192:195], v[112:115]\n"
"ds_read_b128 a[120:123], v224 offset:13312\n"
"v_mul_f32 v212, s62, v212\n"
"v_mfma_f32_16x16x32_bf16 v[116:119], a[148:151], v[192:195], v[116:119]\n"
"v_exp_f32 v210, v210\n"
"v_mfma_f32_16x16x32_bf16 v[120:123], a[152:155], v[192:195], v[120:123]\n"
"ds_read_b128 a[124:127], v224 offset:13824\n"
"v_mul_f32 v213, s62, v213\n"
"v_mfma_f32_16x16x32_bf16 v[124:127], a[156:159], v[192:195], v[124:127]\n"
"v_exp_f32 v207, v207\n"
"v_mfma_f32_16x16x32_bf16 a[172:175], a[184:187], v[192:195], a[172:175]\n"
"v_cvt_pk_bf16_f32 v203, v206, v207\n"
"s_waitcnt lgkmcnt(2)\n"
"v_mfma_f32_16x16x32_bf16 v[192:195], a[96:99], a[0:3], 0\n"
"v_exp_f32 v211, v211\n"
"v_mfma_f32_16x16x32_bf16 v[192:195], a[100:103], a[4:7], v[192:195]\n"
"ds_read_b128 a[252:255], v230 offset:16384\n"
"v_cvt_pk_bf16_f32 v209, v210, v211\n"
"v_mfma_f32_16x16x32_bf16 v[192:195], a[104:107], a[8:11], v[192:195]\n"
"v_exp_f32 v212, v212\n"
"v_mfma_f32_16x16x32_bf16 v[192:195], a[108:111], a[12:15], v[192:195]\n"
"ds_read_b128 a[188:191], v231 offset:17408\n"
"v_mul_f32 v214, s62, v214\n"
"v_mfma_f32_16x16x32_bf16 v[196:199], a[112:115], a[0:3], 0\n"
"v_exp_f32 v213, v213\n"
"v_mfma_f32_16x16x32_bf16 v[196:199], a[116:119], a[4:7], v[196:199]\n"
"v_cvt_pk_bf16_f32 v210, v212, v213\n"
"s_waitcnt lgkmcnt(2)\n"
"v_mfma_f32_16x16x32_bf16 v[196:199], a[120:123], a[8:11], v[196:199]\n"
"v_mul_f32 v192, s62, v192\n"
"v_mul_f32 v193, s62, v193\n"
"v_mfma_f32_16x16x32_bf16 v[196:199], a[124:127], a[12:15], v[196:199]\n"
"v_exp_f32 v192, v192\n"
"v_mfma_f32_16x16x32_bf16 v[128:131], a[128:131], v[200:203], v[128:131]\n"
"v_exp_f32 v193, v193\n"
"v_mfma_f32_16x16x32_bf16 v[132:135], a[132:135], v[200:203], v[132:135]\n"
"v_exp_f32 v214, v214\n"
"v_mfma_f32_16x16x32_bf16 v[136:139], a[136:139], v[200:203], v[136:139]\n"
"v_mul_f32 v194, s62, v194\n"
"v_cvt_pk_bf16_f32 v192, v192, v193\n"
"v_mfma_f32_16x16x32_bf16 v[140:143], a[140:143], v[200:203], v[140:143]\n"
"v_mul_f32 v215, s62, v215\n"
"v_mul_f32 v195, s62, v195\n"
"v_mfma_f32_16x16x32_bf16 v[144:147], a[144:147], v[200:203], v[144:147]\n"
"v_exp_f32 v194, v194\n"
"v_mfma_f32_16x16x32_bf16 v[148:151], a[148:151], v[200:203], v[148:151]\n"
"v_exp_f32 v215, v215\n"
"v_mfma_f32_16x16x32_bf16 v[152:155], a[152:155], v[200:203], v[152:155]\n"
"v_cvt_pk_bf16_f32 v211, v214, v215\n"
"v_mul_f32 v196, s62, v196\n"
"v_mfma_f32_16x16x32_bf16 v[156:159], a[156:159], v[200:203], v[156:159]\n"
"v_exp_f32 v195, v195\n"
"v_mfma_f32_16x16x32_bf16 a[176:179], a[184:187], v[200:203], a[176:179]\n"
"v_cvt_pk_bf16_f32 v193, v194, v195\n"
"v_mul_f32 v197, s62, v197\n"
"v_mfma_f32_16x16x32_bf16 v[200:203], a[96:99], a[16:19], 0\n"
"v_exp_f32 v196, v196\n"
"v_mfma_f32_16x16x32_bf16 v[200:203], a[100:103], a[20:23], v[200:203]\n"
"v_exp_f32 v197, v197\n"
"v_mfma_f32_16x16x32_bf16 v[200:203], a[104:107], a[24:27], v[200:203]\n"
"v_mul_f32 v198, s62, v198\n"
"v_cvt_pk_bf16_f32 v194, v196, v197\n"
"v_mfma_f32_16x16x32_bf16 v[200:203], a[108:111], a[28:31], v[200:203]\n"
"v_exp_f32 v198, v198\n"
"v_mfma_f32_16x16x32_bf16 v[204:207], a[112:115], a[16:19], 0\n"
"v_mul_f32 v199, s62, v199\n"
"v_mfma_f32_16x16x32_bf16 v[204:207], a[116:119], a[20:23], v[204:207]\n"
"v_exp_f32 v199, v199\n"
"v_mfma_f32_16x16x32_bf16 v[204:207], a[120:123], a[24:27], v[204:207]\n"
"v_cvt_pk_bf16_f32 v195, v198, v199\n"
"v_mfma_f32_16x16x32_bf16 v[204:207], a[124:127], a[28:31], v[204:207]\n"
"v_mul_f32 v200, s62, v200\n"
"v_mul_f32 v201, s62, v201\n"
"v_mfma_f32_16x16x32_bf16 v[160:163], a[128:131], v[208:211], v[160:163]\n"
"ds_read_b64_tr_b16 a[128:129], v228 offset:8192\n"
"v_mul_f32 v202, s62, v202\n"
"v_mfma_f32_16x16x32_bf16 v[164:167], a[132:135], v[208:211], v[164:167]\n"
"ds_read_b64_tr_b16 a[130:131], v228 offset:12288\n"
"v_mul_f32 v203, s62, v203\n"
"v_mfma_f32_16x16x32_bf16 v[168:171], a[136:139], v[208:211], v[168:171]\n"
"ds_read_b64_tr_b16 a[132:133], v229 offset:8192\n"
"v_mul_f32 v204, s62, v204\n"
"v_mfma_f32_16x16x32_bf16 v[172:175], a[140:143], v[208:211], v[172:175]\n"
"ds_read_b64_tr_b16 a[134:135], v229 offset:12288\n"
"v_mul_f32 v205, s62, v205\n"
"v_mfma_f32_16x16x32_bf16 v[176:179], a[144:147], v[208:211], v[176:179]\n"
"ds_read_b64_tr_b16 a[136:137], v228 offset:8704\n"
"v_exp_f32 v200, v200\n"
"v_mfma_f32_16x16x32_bf16 v[180:183], a[148:151], v[208:211], v[180:183]\n"
"ds_read_b64_tr_b16 a[138:139], v228 offset:12800\n"
"v_mul_f32 v206, s62, v206\n"
"v_mfma_f32_16x16x32_bf16 v[184:187], a[152:155], v[208:211], v[184:187]\n"
"ds_read_b64_tr_b16 a[140:141], v229 offset:8704\n"
"v_mul_f32 v207, s62, v207\n"
"v_mfma_f32_16x16x32_bf16 v[188:191], a[156:159], v[208:211], v[188:191]\n"
"ds_read_b64_tr_b16 a[142:143], v229 offset:12800\n"
"v_exp_f32 v201, v201\n"
"v_mfma_f32_16x16x32_bf16 a[180:183], a[184:187], v[208:211], a[180:183]\n"
"ds_read_b64_tr_b16 a[144:145], v228 offset:9216\n"
"v_cvt_pk_bf16_f32 v200, v200, v201\n"
"v_mfma_f32_16x16x32_bf16 v[208:211], a[96:99], a[32:35], 0\n"
"ds_read_b64_tr_b16 a[146:147], v228 offset:13312\n"
"v_mfma_f32_16x16x32_bf16 v[208:211], a[100:103], a[36:39], v[208:211]\n"
"ds_read_b64_tr_b16 a[148:149], v229 offset:9216\n"
"v_mfma_f32_16x16x32_bf16 v[208:211], a[104:107], a[40:43], v[208:211]\n"
"ds_read_b64_tr_b16 a[150:151], v229 offset:13312\n"
"v_exp_f32 v202, v202\n"
"v_mfma_f32_16x16x32_bf16 v[208:211], a[108:111], a[44:47], v[208:211]\n"
"ds_read_b64_tr_b16 a[152:153], v228 offset:9728\n"
"v_mfma_f32_16x16x32_bf16 v[212:215], a[112:115], a[32:35], 0\n"
"ds_read_b64_tr_b16 a[154:155], v228 offset:13824\n"
"v_mfma_f32_16x16x32_bf16 v[212:215], a[116:119], a[36:39], v[212:215]\n"
"ds_read_b64_tr_b16 a[156:157], v229 offset:9728\n"
"v_exp_f32 v203, v203\n"
"v_mfma_f32_16x16x32_bf16 v[212:215], a[120:123], a[40:43], v[212:215]\n"
"ds_read_b64_tr_b16 a[158:159], v229 offset:13824\n"
"v_cvt_pk_bf16_f32 v201, v202, v203\n"
"v_mfma_f32_16x16x32_bf16 v[212:215], a[124:127], a[44:47], v[212:215]\n"
"v_mul_f32 v208, s62, v208\n"
"s_waitcnt lgkmcnt(5)\n"
"v_mfma_f32_16x16x32_bf16 v[0:3], a[128:131], v[192:195], v[0:3]\n"
"v_mul_f32 v209, s62, v209\n"
"s_add_u32 s61, s59, 16384\n"
"v_mfma_f32_16x16x32_bf16 v[4:7], a[132:135], v[192:195], v[4:7]\n"
"buffer_store_dwordx4 a[252:255], v227, s[48:51], s61 offen sc1 nt\n"
"v_exp_f32 v204, v204\n"
"v_mfma_f32_16x16x32_bf16 v[8:11], a[136:139], v[192:195], v[8:11]\n"
"v_exp_f32 v208, v208\n"
"v_mfma_f32_16x16x32_bf16 v[12:15], a[140:143], v[192:195], v[12:15]\n"
"buffer_store_dwordx4 a[188:191], v227, s[48:51], s61 offen offset:1024 sc1 nt\n"
"v_mul_f32 v210, s62, v210\n"
"v_mfma_f32_16x16x32_bf16 v[16:19], a[144:147], v[192:195], v[16:19]\n"
"v_mul_f32 v211, s62, v211\n"
"s_waitcnt lgkmcnt(0)\n"
"v_mfma_f32_16x16x32_bf16 v[20:23], a[148:151], v[192:195], v[20:23]\n"
"v_exp_f32 v205, v205\n"
"v_mfma_f32_16x16x32_bf16 v[24:27], a[152:155], v[192:195], v[24:27]\n"
"v_cvt_pk_bf16_f32 v202, v204, v205\n"
"v_mul_f32 v212, s62, v212\n"
"v_mfma_f32_16x16x32_bf16 v[28:31], a[156:159], v[192:195], v[28:31]\n"
"v_exp_f32 v209, v209\n"
"v_mfma_f32_16x16x32_bf16 a[160:163], a[184:187], v[192:195], a[160:163]\n"
"v_exp_f32 v206, v206\n"
"v_mfma_f32_16x16x32_bf16 v[192:195], a[96:99], a[48:51], 0\n"
"v_cvt_pk_bf16_f32 v208, v208, v209\n"
"v_mul_f32 v213, s62, v213\n"
"v_mfma_f32_16x16x32_bf16 v[192:195], a[100:103], a[52:55], v[192:195]\n"
"v_exp_f32 v210, v210\n"
"v_mfma_f32_16x16x32_bf16 v[192:195], a[104:107], a[56:59], v[192:195]\n"
"v_exp_f32 v207, v207\n"
"v_mfma_f32_16x16x32_bf16 v[192:195], a[108:111], a[60:63], v[192:195]\n"
"v_cvt_pk_bf16_f32 v203, v206, v207\n"
"v_mul_f32 v214, s62, v214\n"
"v_mfma_f32_16x16x32_bf16 v[196:199], a[112:115], a[48:51], 0\n"
"v_exp_f32 v211, v211\n"
"v_mfma_f32_16x16x32_bf16 v[196:199], a[116:119], a[52:55], v[196:199]\n"
"v_cvt_pk_bf16_f32 v209, v210, v211\n"
"v_mul_f32 v215, s62, v215\n"
"v_mfma_f32_16x16x32_bf16 v[196:199], a[120:123], a[56:59], v[196:199]\n"
"v_exp_f32 v212, v212\n"
"v_mfma_f32_16x16x32_bf16 v[196:199], a[124:127], a[60:63], v[196:199]\n"
"v_mul_f32 v192, s62, v192\n"
"v_mul_f32 v193, s62, v193\n"
"v_mfma_f32_16x16x32_bf16 v[32:35], a[128:131], v[200:203], v[32:35]\n"
"v_exp_f32 v192, v192\n"
"v_mfma_f32_16x16x32_bf16 v[36:39], a[132:135], v[200:203], v[36:39]\n"
"v_exp_f32 v213, v213\n"
"v_mfma_f32_16x16x32_bf16 v[40:43], a[136:139], v[200:203], v[40:43]\n"
"v_cvt_pk_bf16_f32 v210, v212, v213\n"
"v_mul_f32 v194, s62, v194\n"
"v_mfma_f32_16x16x32_bf16 v[44:47], a[140:143], v[200:203], v[44:47]\n"
"v_exp_f32 v193, v193\n"
"v_mfma_f32_16x16x32_bf16 v[48:51], a[144:147], v[200:203], v[48:51]\n"
"s_waitcnt vmcnt(10)\n"
"s_barrier\n"
"v_mfma_f32_16x16x32_bf16 v[52:55], a[148:151], v[200:203], v[52:55]\n"
"v_exp_f32 v214, v214\n"
"v_mfma_f32_16x16x32_bf16 v[56:59], a[152:155], v[200:203], v[56:59]\n"
"v_cvt_pk_bf16_f32 v192, v192, v193\n"
"v_mul_f32 v195, s62, v195\n"
"v_mfma_f32_16x16x32_bf16 v[60:63], a[156:159], v[200:203], v[60:63]\n"
"v_exp_f32 v194, v194\n"
"v_mfma_f32_16x16x32_bf16 a[164:167], a[184:187], v[200:203], a[164:167]\n"
"v_exp_f32 v215, v215\n"
"v_mfma_f32_16x16x32_bf16 v[200:203], a[96:99], a[64:67], 0\n"
"v_cvt_pk_bf16_f32 v211, v214, v215\n"
"v_mul_f32 v196, s62, v196\n"
"v_mfma_f32_16x16x32_bf16 v[200:203], a[100:103], a[68:71], v[200:203]\n"
"v_exp_f32 v195, v195\n"
"v_mfma_f32_16x16x32_bf16 v[200:203], a[104:107], a[72:75], v[200:203]\n"
"v_cvt_pk_bf16_f32 v193, v194, v195\n"
"v_mul_f32 v197, s62, v197\n"
"v_mfma_f32_16x16x32_bf16 v[200:203], a[108:111], a[76:79], v[200:203]\n"
"v_exp_f32 v196, v196\n"
"v_mfma_f32_16x16x32_bf16 v[204:207], a[112:115], a[64:67], 0\n"
"v_exp_f32 v197, v197\n"
"v_mfma_f32_16x16x32_bf16 v[204:207], a[116:119], a[68:71], v[204:207]\n"
"v_mul_f32 v198, s62, v198\n"
"v_cvt_pk_bf16_f32 v194, v196, v197\n"
"v_mfma_f32_16x16x32_bf16 v[204:207], a[120:123], a[72:75], v[204:207]\n"
"v_exp_f32 v198, v198\n"
"v_mfma_f32_16x16x32_bf16 v[204:207], a[124:127], a[76:79], v[204:207]\n"
"v_mul_f32 v200, s62, v200\n"
"v_mul_f32 v201, s62, v201\n"
"v_mfma_f32_16x16x32_bf16 v[64:67], a[128:131], v[208:211], v[64:67]\n"
"v_exp_f32 v200, v200\n"
"v_mfma_f32_16x16x32_bf16 v[68:71], a[132:135], v[208:211], v[68:71]\n"
"v_exp_f32 v201, v201\n"
"v_mfma_f32_16x16x32_bf16 v[72:75], a[136:139], v[208:211], v[72:75]\n"
"v_mul_f32 v202, s62, v202\n"
"v_cvt_pk_bf16_f32 v200, v200, v201\n"
"v_mfma_f32_16x16x32_bf16 v[76:79], a[140:143], v[208:211], v[76:79]\n"
"v_mul_f32 v199, s62, v199\n"
"v_mul_f32 v203, s62, v203\n"
"v_mfma_f32_16x16x32_bf16 v[80:83], a[144:147], v[208:211], v[80:83]\n"
"v_exp_f32 v202, v202\n"
"v_mfma_f32_16x16x32_bf16 v[84:87], a[148:151], v[208:211], v[84:87]\n"
"v_exp_f32 v199, v199\n"
"v_mfma_f32_16x16x32_bf16 v[88:91], a[152:155], v[208:211], v[88:91]\n"
"v_cvt_pk_bf16_f32 v195, v198, v199\n"
"v_mul_f32 v204, s62, v204\n"
"v_mfma_f32_16x16x32_bf16 v[92:95], a[156:159], v[208:211], v[92:95]\n"
"v_exp_f32 v203, v203\n"
"v_mfma_f32_16x16x32_bf16 a[168:171], a[184:187], v[208:211], a[168:171]\n"
"v_cvt_pk_bf16_f32 v201, v202, v203\n"
"v_mul_f32 v205, s62, v205\n"
"v_mfma_f32_16x16x32_bf16 v[208:211], a[96:99], a[80:83], 0\n"
"ds_read_b128 a[96:99], v224 offset:16384\n"
"v_mul_f32 v206, s62, v206\n"
"v_mfma_f32_16x16x32_bf16 v[208:211], a[100:103], a[84:87], v[208:211]\n"
"v_exp_f32 v204, v204\n"
"v_mfma_f32_16x16x32_bf16 v[208:211], a[104:107], a[88:91], v[208:211]\n"
"ds_read_b128 a[100:103], v224 offset:16896\n"
"v_mul_f32 v207, s62, v207\n"
"v_mfma_f32_16x16x32_bf16 v[208:211], a[108:111], a[92:95], v[208:211]\n"
"v_exp_f32 v205, v205\n"
"v_mfma_f32_16x16x32_bf16 v[212:215], a[112:115], a[80:83], 0\n"
"ds_read_b128 a[104:107], v224 offset:17408\n"
"v_cvt_pk_bf16_f32 v202, v204, v205\n"
"v_mfma_f32_16x16x32_bf16 v[212:215], a[116:119], a[84:87], v[212:215]\n"
"v_exp_f32 v206, v206\n"
"v_mfma_f32_16x16x32_bf16 v[212:215], a[120:123], a[88:91], v[212:215]\n"
"ds_read_b128 a[108:111], v224 offset:17920\n"
"v_mul_f32 v208, s62, v208\n"
"v_mfma_f32_16x16x32_bf16 v[212:215], a[124:127], a[92:95], v[212:215]\n"
"v_exp_f32 v208, v208\n"
"v_mfma_f32_16x16x32_bf16 v[96:99], a[128:131], v[192:195], v[96:99]\n"
"ds_read_b128 a[112:115], v224 offset:20480\n"
"v_mul_f32 v209, s62, v209\n"
"v_mfma_f32_16x16x32_bf16 v[100:103], a[132:135], v[192:195], v[100:103]\n"
"v_exp_f32 v209, v209\n"
"v_mfma_f32_16x16x32_bf16 v[104:107], a[136:139], v[192:195], v[104:107]\n"
"ds_read_b128 a[116:119], v224 offset:20992\n"
"v_mul_f32 v210, s62, v210\n"
"v_mfma_f32_16x16x32_bf16 v[108:111], a[140:143], v[192:195], v[108:111]\n"
"v_cvt_pk_bf16_f32 v208, v208, v209\n"
"v_mul_f32 v211, s62, v211\n"
"v_mfma_f32_16x16x32_bf16 v[112:115], a[144:147], v[192:195], v[112:115]\n"
"ds_read_b128 a[120:123], v224 offset:21504\n"
"v_mul_f32 v212, s62, v212\n"
"v_mfma_f32_16x16x32_bf16 v[116:119], a[148:151], v[192:195], v[116:119]\n"
"v_exp_f32 v210, v210\n"
"v_mfma_f32_16x16x32_bf16 v[120:123], a[152:155], v[192:195], v[120:123]\n"
"ds_read_b128 a[124:127], v224 offset:22016\n"
"v_mul_f32 v213, s62, v213\n"
"v_mfma_f32_16x16x32_bf16 v[124:127], a[156:159], v[192:195], v[124:127]\n"
"v_exp_f32 v207, v207\n"
"v_mfma_f32_16x16x32_bf16 a[172:175], a[184:187], v[192:195], a[172:175]\n"
"v_cvt_pk_bf16_f32 v203, v206, v207\n"
"s_waitcnt lgkmcnt(2)\n"
"v_mfma_f32_16x16x32_bf16 v[192:195], a[96:99], a[0:3], 0\n"
"v_exp_f32 v211, v211\n"
"v_mfma_f32_16x16x32_bf16 v[192:195], a[100:103], a[4:7], v[192:195]\n"
"ds_read_b128 a[192:195], v230 offset:18432\n"
"v_cvt_pk_bf16_f32 v209, v210, v211\n"
"v_mfma_f32_16x16x32_bf16 v[192:195], a[104:107], a[8:11], v[192:195]\n"
"v_exp_f32 v212, v212\n"
"v_mfma_f32_16x16x32_bf16 v[192:195], a[108:111], a[12:15], v[192:195]\n"
"ds_read_b128 a[196:199], v231 offset:19456\n"
"v_mul_f32 v214, s62, v214\n"
"v_mfma_f32_16x16x32_bf16 v[196:199], a[112:115], a[0:3], 0\n"
"v_exp_f32 v213, v213\n"
"v_mfma_f32_16x16x32_bf16 v[196:199], a[116:119], a[4:7], v[196:199]\n"
"v_cvt_pk_bf16_f32 v210, v212, v213\n"
"s_waitcnt lgkmcnt(2)\n"
"v_mfma_f32_16x16x32_bf16 v[196:199], a[120:123], a[8:11], v[196:199]\n"
"v_mul_f32 v192, s62, v192\n"
"v_mul_f32 v193, s62, v193\n"
"v_mfma_f32_16x16x32_bf16 v[196:199], a[124:127], a[12:15], v[196:199]\n"
"v_exp_f32 v192, v192\n"
"v_mfma_f32_16x16x32_bf16 v[128:131], a[128:131], v[200:203], v[128:131]\n"
"v_exp_f32 v193, v193\n"
"v_mfma_f32_16x16x32_bf16 v[132:135], a[132:135], v[200:203], v[132:135]\n"
"v_exp_f32 v214, v214\n"
"v_mfma_f32_16x16x32_bf16 v[136:139], a[136:139], v[200:203], v[136:139]\n"
"v_mul_f32 v194, s62, v194\n"
"v_cvt_pk_bf16_f32 v192, v192, v193\n"
"v_mfma_f32_16x16x32_bf16 v[140:143], a[140:143], v[200:203], v[140:143]\n"
"v_mul_f32 v215, s62, v215\n"
"v_mul_f32 v195, s62, v195\n"
"v_mfma_f32_16x16x32_bf16 v[144:147], a[144:147], v[200:203], v[144:147]\n"
"v_exp_f32 v194, v194\n"
"v_mfma_f32_16x16x32_bf16 v[148:151], a[148:151], v[200:203], v[148:151]\n"
"v_exp_f32 v215, v215\n"
"v_mfma_f32_16x16x32_bf16 v[152:155], a[152:155], v[200:203], v[152:155]\n"
"v_cvt_pk_bf16_f32 v211, v214, v215\n"
"v_mul_f32 v196, s62, v196\n"
"v_mfma_f32_16x16x32_bf16 v[156:159], a[156:159], v[200:203], v[156:159]\n"
"v_exp_f32 v195, v195\n"
"v_mfma_f32_16x16x32_bf16 a[176:179], a[184:187], v[200:203], a[176:179]\n"
"v_cvt_pk_bf16_f32 v193, v194, v195\n"
"v_mul_f32 v197, s62, v197\n"
"v_mfma_f32_16x16x32_bf16 v[200:203], a[96:99], a[16:19], 0\n"
"v_exp_f32 v196, v196\n"
"v_mfma_f32_16x16x32_bf16 v[200:203], a[100:103], a[20:23], v[200:203]\n"
"v_exp_f32 v197, v197\n"
"v_mfma_f32_16x16x32_bf16 v[200:203], a[104:107], a[24:27], v[200:203]\n"
"v_mul_f32 v198, s62, v198\n"
"v_cvt_pk_bf16_f32 v194, v196, v197\n"
"v_mfma_f32_16x16x32_bf16 v[200:203], a[108:111], a[28:31], v[200:203]\n"
"v_exp_f32 v198, v198\n"
"v_mfma_f32_16x16x32_bf16 v[204:207], a[112:115], a[16:19], 0\n"
"v_mul_f32 v199, s62, v199\n"
"v_mfma_f32_16x16x32_bf16 v[204:207], a[116:119], a[20:23], v[204:207]\n"
"v_exp_f32 v199, v199\n"
"v_mfma_f32_16x16x32_bf16 v[204:207], a[120:123], a[24:27], v[204:207]\n"
"v_cvt_pk_bf16_f32 v195, v198, v199\n"
"v_mfma_f32_16x16x32_bf16 v[204:207], a[124:127], a[28:31], v[204:207]\n"
"v_mul_f32 v200, s62, v200\n"
"v_mul_f32 v201, s62, v201\n"
"v_mfma_f32_16x16x32_bf16 v[160:163], a[128:131], v[208:211], v[160:163]\n"
"ds_read_b64_tr_b16 a[128:129], v228 offset:16384\n"
"v_mul_f32 v202, s62, v202\n"
"v_mfma_f32_16x16x32_bf16 v[164:167], a[132:135], v[208:211], v[164:167]\n"
"ds_read_b64_tr_b16 a[130:131], v228 offset:20480\n"
"v_mul_f32 v203, s62, v203\n"
"v_mfma_f32_16x16x32_bf16 v[168:171], a[136:139], v[208:211], v[168:171]\n"
"ds_read_b64_tr_b16 a[132:133], v229 offset:16384\n"
"v_mul_f32 v204, s62, v204\n"
"v_mfma_f32_16x16x32_bf16 v[172:175], a[140:143], v[208:211], v[172:175]\n"
"ds_read_b64_tr_b16 a[134:135], v229 offset:20480\n"
"v_mul_f32 v205, s62, v205\n"
"v_mfma_f32_16x16x32_bf16 v[176:179], a[144:147], v[208:211], v[176:179]\n"
"ds_read_b64_tr_b16 a[136:137], v228 offset:16896\n"
"v_exp_f32 v200, v200\n"
"v_mfma_f32_16x16x32_bf16 v[180:183], a[148:151], v[208:211], v[180:183]\n"
"ds_read_b64_tr_b16 a[138:139], v228 offset:20992\n"
"v_mul_f32 v206, s62, v206\n"
"v_mfma_f32_16x16x32_bf16 v[184:187], a[152:155], v[208:211], v[184:187]\n"
"ds_read_b64_tr_b16 a[140:141], v229 offset:16896\n"
"v_mul_f32 v207, s62, v207\n"
"v_mfma_f32_16x16x32_bf16 v[188:191], a[156:159], v[208:211], v[188:191]\n"
"ds_read_b64_tr_b16 a[142:143], v229 offset:20992\n"
"v_exp_f32 v201, v201\n"
"v_mfma_f32_16x16x32_bf16 a[180:183], a[184:187], v[208:211], a[180:183]\n"
"ds_read_b64_tr_b16 a[144:145], v228 offset:17408\n"
"v_cvt_pk_bf16_f32 v200, v200, v201\n"
"v_mfma_f32_16x16x32_bf16 v[208:211], a[96:99], a[32:35], 0\n"
"ds_read_b64_tr_b16 a[146:147], v228 offset:21504\n"
"v_mfma_f32_16x16x32_bf16 v[208:211], a[100:103], a[36:39], v[208:211]\n"
"ds_read_b64_tr_b16 a[148:149], v229 offset:17408\n"
"v_mfma_f32_16x16x32_bf16 v[208:211], a[104:107], a[40:43], v[208:211]\n"
"ds_read_b64_tr_b16 a[150:151], v229 offset:21504\n"
"v_exp_f32 v202, v202\n"
"v_mfma_f32_16x16x32_bf16 v[208:211], a[108:111], a[44:47], v[208:211]\n"
"ds_read_b64_tr_b16 a[152:153], v228 offset:17920\n"
"v_mfma_f32_16x16x32_bf16 v[212:215], a[112:115], a[32:35], 0\n"
"ds_read_b64_tr_b16 a[154:155], v228 offset:22016\n"
"v_mfma_f32_16x16x32_bf16 v[212:215], a[116:119], a[36:39], v[212:215]\n"
"ds_read_b64_tr_b16 a[156:157], v229 offset:17920\n"
"v_exp_f32 v203, v203\n"
"v_mfma_f32_16x16x32_bf16 v[212:215], a[120:123], a[40:43], v[212:215]\n"
"ds_read_b64_tr_b16 a[158:159], v229 offset:22016\n"
"v_cvt_pk_bf16_f32 v201, v202, v203\n"
"v_mfma_f32_16x16x32_bf16 v[212:215], a[124:127], a[44:47], v[212:215]\n"
"v_mul_f32 v208, s62, v208\n"
"s_waitcnt lgkmcnt(5)\n"
"v_mfma_f32_16x16x32_bf16 v[0:3], a[128:131], v[192:195], v[0:3]\n"
"v_exp_f32 v204, v204\n"
"v_mfma_f32_16x16x32_bf16 v[4:7], a[132:135], v[192:195], v[4:7]\n"
"buffer_store_dwordx4 a[192:195], v227, s[48:51], s61 offen offset:2048 sc1 nt\n"
"v_mul_f32 v209, s62, v209\n"
"v_mfma_f32_16x16x32_bf16 v[8:11], a[136:139], v[192:195], v[8:11]\n"
"v_exp_f32 v208, v208\n"
"v_mfma_f32_16x16x32_bf16 v[12:15], a[140:143], v[192:195], v[12:15]\n"
"buffer_store_dwordx4 a[196:199], v227, s[48:51], s61 offen offset:3072 sc1 nt\n"
"v_mul_f32 v210, s62, v210\n"
"v_mfma_f32_16x16x32_bf16 v[16:19], a[144:147], v[192:195], v[16:19]\n"
"v_mul_f32 v211, s62, v211\n"
"s_waitcnt lgkmcnt(0)\n"
"v_mfma_f32_16x16x32_bf16 v[20:23], a[148:151], v[192:195], v[20:23]\n"
"v_exp_f32 v205, v205\n"
"v_mfma_f32_16x16x32_bf16 v[24:27], a[152:155], v[192:195], v[24:27]\n"
"v_cvt_pk_bf16_f32 v202, v204, v205\n"
"v_mul_f32 v212, s62, v212\n"
"v_mfma_f32_16x16x32_bf16 v[28:31], a[156:159], v[192:195], v[28:31]\n"
"v_exp_f32 v209, v209\n"
"v_mfma_f32_16x16x32_bf16 a[160:163], a[184:187], v[192:195], a[160:163]\n"
"v_exp_f32 v206, v206\n"
"v_mfma_f32_16x16x32_bf16 v[192:195], a[96:99], a[48:51], 0\n"
"v_cvt_pk_bf16_f32 v208, v208, v209\n"
"v_mul_f32 v213, s62, v213\n"
"v_mfma_f32_16x16x32_bf16 v[192:195], a[100:103], a[52:55], v[192:195]\n"
"v_exp_f32 v210, v210\n"
"v_mfma_f32_16x16x32_bf16 v[192:195], a[104:107], a[56:59], v[192:195]\n"
"v_exp_f32 v207, v207\n"
"v_mfma_f32_16x16x32_bf16 v[192:195], a[108:111], a[60:63], v[192:195]\n"
"v_cvt_pk_bf16_f32 v203, v206, v207\n"
"v_mul_f32 v214, s62, v214\n"
"v_mfma_f32_16x16x32_bf16 v[196:199], a[112:115], a[48:51], 0\n"
"v_exp_f32 v211, v211\n"
"v_mfma_f32_16x16x32_bf16 v[196:199], a[116:119], a[52:55], v[196:199]\n"
"v_cvt_pk_bf16_f32 v209, v210, v211\n"
"v_mul_f32 v215, s62, v215\n"
"v_mfma_f32_16x16x32_bf16 v[196:199], a[120:123], a[56:59], v[196:199]\n"
"v_exp_f32 v212, v212\n"
"v_mfma_f32_16x16x32_bf16 v[196:199], a[124:127], a[60:63], v[196:199]\n"
"v_mul_f32 v192, s62, v192\n"
"v_mul_f32 v193, s62, v193\n"
"v_mfma_f32_16x16x32_bf16 v[32:35], a[128:131], v[200:203], v[32:35]\n"
"v_exp_f32 v192, v192\n"
"v_mfma_f32_16x16x32_bf16 v[36:39], a[132:135], v[200:203], v[36:39]\n"
"v_exp_f32 v213, v213\n"
"v_mfma_f32_16x16x32_bf16 v[40:43], a[136:139], v[200:203], v[40:43]\n"
"v_cvt_pk_bf16_f32 v210, v212, v213\n"
"v_mul_f32 v194, s62, v194\n"
"v_mfma_f32_16x16x32_bf16 v[44:47], a[140:143], v[200:203], v[44:47]\n"
"v_exp_f32 v193, v193\n"
"v_mfma_f32_16x16x32_bf16 v[48:51], a[144:147], v[200:203], v[48:51]\n"
"s_waitcnt vmcnt(6)\n"
"s_barrier\n"
"v_mfma_f32_16x16x32_bf16 v[52:55], a[148:151], v[200:203], v[52:55]\n"
"v_exp_f32 v214, v214\n"
"v_mfma_f32_16x16x32_bf16 v[56:59], a[152:155], v[200:203], v[56:59]\n"
"v_cvt_pk_bf16_f32 v192, v192, v193\n"
"v_mul_f32 v195, s62, v195\n"
"v_mfma_f32_16x16x32_bf16 v[60:63], a[156:159], v[200:203], v[60:63]\n"
"v_exp_f32 v194, v194\n"
"v_mfma_f32_16x16x32_bf16 a[164:167], a[184:187], v[200:203], a[164:167]\n"
"v_exp_f32 v215, v215\n"
"v_mfma_f32_16x16x32_bf16 v[200:203], a[96:99], a[64:67], 0\n"
"v_cvt_pk_bf16_f32 v211, v214, v215\n"
"v_mul_f32 v196, s62, v196\n"
"v_mfma_f32_16x16x32_bf16 v[200:203], a[100:103], a[68:71], v[200:203]\n"
"v_exp_f32 v195, v195\n"
"v_mfma_f32_16x16x32_bf16 v[200:203], a[104:107], a[72:75], v[200:203]\n"
"v_cvt_pk_bf16_f32 v193, v194, v195\n"
"v_mul_f32 v197, s62, v197\n"
"v_mfma_f32_16x16x32_bf16 v[200:203], a[108:111], a[76:79], v[200:203]\n"
"v_exp_f32 v196, v196\n"
"v_mfma_f32_16x16x32_bf16 v[204:207], a[112:115], a[64:67], 0\n"
"v_exp_f32 v197, v197\n"
"v_mfma_f32_16x16x32_bf16 v[204:207], a[116:119], a[68:71], v[204:207]\n"
"v_mul_f32 v198, s62, v198\n"
"v_cvt_pk_bf16_f32 v194, v196, v197\n"
"v_mfma_f32_16x16x32_bf16 v[204:207], a[120:123], a[72:75], v[204:207]\n"
"v_exp_f32 v198, v198\n"
"v_mfma_f32_16x16x32_bf16 v[204:207], a[124:127], a[76:79], v[204:207]\n"
"v_mul_f32 v200, s62, v200\n"
"v_mul_f32 v201, s62, v201\n"
"v_mfma_f32_16x16x32_bf16 v[64:67], a[128:131], v[208:211], v[64:67]\n"
"v_exp_f32 v200, v200\n"
"v_mfma_f32_16x16x32_bf16 v[68:71], a[132:135], v[208:211], v[68:71]\n"
"v_exp_f32 v201, v201\n"
"v_mfma_f32_16x16x32_bf16 v[72:75], a[136:139], v[208:211], v[72:75]\n"
"v_mul_f32 v202, s62, v202\n"
"v_cvt_pk_bf16_f32 v200, v200, v201\n"
"v_mfma_f32_16x16x32_bf16 v[76:79], a[140:143], v[208:211], v[76:79]\n"
"v_mul_f32 v199, s62, v199\n"
"v_mul_f32 v203, s62, v203\n"
"v_mfma_f32_16x16x32_bf16 v[80:83], a[144:147], v[208:211], v[80:83]\n"
"v_exp_f32 v202, v202\n"
"v_mfma_f32_16x16x32_bf16 v[84:87], a[148:151], v[208:211], v[84:87]\n"
"v_exp_f32 v199, v199\n"
"v_mfma_f32_16x16x32_bf16 v[88:91], a[152:155], v[208:211], v[88:91]\n"
"v_cvt_pk_bf16_f32 v195, v198, v199\n"
"v_mul_f32 v204, s62, v204\n"
"v_mfma_f32_16x16x32_bf16 v[92:95], a[156:159], v[208:211], v[92:95]\n"
"v_exp_f32 v203, v203\n"
"v_mfma_f32_16x16x32_bf16 a[168:171], a[184:187], v[208:211], a[168:171]\n"
"v_cvt_pk_bf16_f32 v201, v202, v203\n"
"v_mul_f32 v205, s62, v205\n"
"v_mfma_f32_16x16x32_bf16 v[208:211], a[96:99], a[80:83], 0\n"
"ds_read_b128 a[96:99], v224 offset:24576\n"
"v_mul_f32 v206, s62, v206\n"
"v_mfma_f32_16x16x32_bf16 v[208:211], a[100:103], a[84:87], v[208:211]\n"
"v_exp_f32 v204, v204\n"
"v_mfma_f32_16x16x32_bf16 v[208:211], a[104:107], a[88:91], v[208:211]\n"
"ds_read_b128 a[100:103], v224 offset:25088\n"
"v_mul_f32 v207, s62, v207\n"
"v_mfma_f32_16x16x32_bf16 v[208:211], a[108:111], a[92:95], v[208:211]\n"
"v_exp_f32 v205, v205\n"
"v_mfma_f32_16x16x32_bf16 v[212:215], a[112:115], a[80:83], 0\n"
"ds_read_b128 a[104:107], v224 offset:25600\n"
"v_cvt_pk_bf16_f32 v202, v204, v205\n"
"v_mfma_f32_16x16x32_bf16 v[212:215], a[116:119], a[84:87], v[212:215]\n"
"v_exp_f32 v206, v206\n"
"v_mfma_f32_16x16x32_bf16 v[212:215], a[120:123], a[88:91], v[212:215]\n"
"ds_read_b128 a[108:111], v224 offset:26112\n"
"v_mul_f32 v208, s62, v208\n"
"v_mfma_f32_16x16x32_bf16 v[212:215], a[124:127], a[92:95], v[212:215]\n"
"v_exp_f32 v208, v208\n"
"v_mfma_f32_16x16x32_bf16 v[96:99], a[128:131], v[192:195], v[96:99]\n"
"ds_read_b128 a[112:115], v224 offset:28672\n"
"v_mul_f32 v209, s62, v209\n"
"v_mfma_f32_16x16x32_bf16 v[100:103], a[132:135], v[192:195], v[100:103]\n"
"v_exp_f32 v209, v209\n"
"v_mfma_f32_16x16x32_bf16 v[104:107], a[136:139], v[192:195], v[104:107]\n"
"ds_read_b128 a[116:119], v224 offset:29184\n"
"v_mul_f32 v210, s62, v210\n"
"v_mfma_f32_16x16x32_bf16 v[108:111], a[140:143], v[192:195], v[108:111]\n"
"v_cvt_pk_bf16_f32 v208, v208, v209\n"
"v_mul_f32 v211, s62, v211\n"
"v_mfma_f32_16x16x32_bf16 v[112:115], a[144:147], v[192:195], v[112:115]\n"
"ds_read_b128 a[120:123], v224 offset:29696\n"
"v_mul_f32 v212, s62, v212\n"
"v_mfma_f32_16x16x32_bf16 v[116:119], a[148:151], v[192:195], v[116:119]\n"
"v_exp_f32 v210, v210\n"
"v_mfma_f32_16x16x32_bf16 v[120:123], a[152:155], v[192:195], v[120:123]\n"
"ds_read_b128 a[124:127], v224 offset:30208\n"
"v_mul_f32 v213, s62, v213\n"
"v_mfma_f32_16x16x32_bf16 v[124:127], a[156:159], v[192:195], v[124:127]\n"
"v_exp_f32 v207, v207\n"
"v_mfma_f32_16x16x32_bf16 a[172:175], a[184:187], v[192:195], a[172:175]\n"
"v_cvt_pk_bf16_f32 v203, v206, v207\n"
"s_waitcnt lgkmcnt(2)\n"
"v_mfma_f32_16x16x32_bf16 v[192:195], a[96:99], a[0:3], 0\n"
"v_exp_f32 v211, v211\n"
"v_mfma_f32_16x16x32_bf16 v[192:195], a[100:103], a[4:7], v[192:195]\n"
"ds_read_b128 a[200:203], v230 offset:20480\n"
"v_cvt_pk_bf16_f32 v209, v210, v211\n"
"v_mfma_f32_16x16x32_bf16 v[192:195], a[104:107], a[8:11], v[192:195]\n"
"v_exp_f32 v212, v212\n"
"v_mfma_f32_16x16x32_bf16 v[192:195], a[108:111], a[12:15], v[192:195]\n"
"ds_read_b128 a[204:207], v231 offset:21504\n"
"v_mul_f32 v214, s62, v214\n"
"v_mfma_f32_16x16x32_bf16 v[196:199], a[112:115], a[0:3], 0\n"
"v_exp_f32 v213, v213\n"
"v_mfma_f32_16x16x32_bf16 v[196:199], a[116:119], a[4:7], v[196:199]\n"
"ds_read_b128 a[208:211], v230 offset:22528\n"
"s_waitcnt lgkmcnt(3)\n"
"v_mfma_f32_16x16x32_bf16 v[196:199], a[120:123], a[8:11], v[196:199]\n"
"v_mul_f32 v192, s62, v192\n"
"v_mul_f32 v193, s62, v193\n"
"v_mfma_f32_16x16x32_bf16 v[196:199], a[124:127], a[12:15], v[196:199]\n"
"ds_read_b128 a[212:215], v231 offset:23552\n"
"v_cvt_pk_bf16_f32 v210, v212, v213\n"
"v_mfma_f32_16x16x32_bf16 v[128:131], a[128:131], v[200:203], v[128:131]\n"
"v_exp_f32 v192, v192\n"
"v_mfma_f32_16x16x32_bf16 v[132:135], a[132:135], v[200:203], v[132:135]\n"
"v_exp_f32 v193, v193\n"
"v_mfma_f32_16x16x32_bf16 v[136:139], a[136:139], v[200:203], v[136:139]\n"
"v_exp_f32 v214, v214\n"
"v_mfma_f32_16x16x32_bf16 v[140:143], a[140:143], v[200:203], v[140:143]\n"
"v_mul_f32 v194, s62, v194\n"
"v_cvt_pk_bf16_f32 v192, v192, v193\n"
"v_mfma_f32_16x16x32_bf16 v[144:147], a[144:147], v[200:203], v[144:147]\n"
"v_mul_f32 v215, s62, v215\n"
"v_mul_f32 v195, s62, v195\n"
"v_mfma_f32_16x16x32_bf16 v[148:151], a[148:151], v[200:203], v[148:151]\n"
"v_exp_f32 v194, v194\n"
"v_mfma_f32_16x16x32_bf16 v[152:155], a[152:155], v[200:203], v[152:155]\n"
"v_exp_f32 v215, v215\n"
"v_mfma_f32_16x16x32_bf16 v[156:159], a[156:159], v[200:203], v[156:159]\n"
"v_cvt_pk_bf16_f32 v211, v214, v215\n"
"v_mul_f32 v196, s62, v196\n"
"v_mfma_f32_16x16x32_bf16 a[176:179], a[184:187], v[200:203], a[176:179]\n"
"v_exp_f32 v195, v195\n"
"v_mfma_f32_16x16x32_bf16 v[200:203], a[96:99], a[16:19], 0\n"
"v_cvt_pk_bf16_f32 v193, v194, v195\n"
"v_mul_f32 v197, s62, v197\n"
"v_mfma_f32_16x16x32_bf16 v[200:203], a[100:103], a[20:23], v[200:203]\n"
"v_exp_f32 v196, v196\n"
"v_mfma_f32_16x16x32_bf16 v[200:203], a[104:107], a[24:27], v[200:203]\n"
"v_exp_f32 v197, v197\n"
"v_mfma_f32_16x16x32_bf16 v[200:203], a[108:111], a[28:31], v[200:203]\n"
"v_mul_f32 v198, s62, v198\n"
"v_cvt_pk_bf16_f32 v194, v196, v197\n"
"v_mfma_f32_16x16x32_bf16 v[204:207], a[112:115], a[16:19], 0\n"
"v_exp_f32 v198, v198\n"
"v_mfma_f32_16x16x32_bf16 v[204:207], a[116:119], a[20:23], v[204:207]\n"
"v_mul_f32 v199, s62, v199\n"
"v_add_u32 v233, 0, v232\n"
"v_mfma_f32_16x16x32_bf16 v[204:207], a[120:123], a[24:27], v[204:207]\n"
"v_mul_f32 v200, s62, v200\n"
"v_mul_f32 v201, s62, v201\n"
"v_mfma_f32_16x16x32_bf16 v[204:207], a[124:127], a[28:31], v[204:207]\n"
"v_exp_f32 v200, v200\n"
"v_mfma_f32_16x16x32_bf16 v[160:163], a[128:131], v[208:211], v[160:163]\n"
"ds_read_b64_tr_b16 a[128:129], v228 offset:24576\n"
"v_mul_f32 v202, s62, v202\n"
"v_mfma_f32_16x16x32_bf16 v[164:167], a[132:135], v[208:211], v[164:167]\n"
"ds_read_b64_tr_b16 a[130:131], v228 offset:28672\n"
"v_mul_f32 v203, s62, v203\n"
"v_mfma_f32_16x16x32_bf16 v[168:171], a[136:139], v[208:211], v[168:171]\n"
"ds_read_b64_tr_b16 a[132:133], v229 offset:24576\n"
"v_mul_f32 v204, s62, v204\n"
"v_mfma_f32_16x16x32_bf16 v[172:175], a[140:143], v[208:211], v[172:175]\n"
"ds_read_b64_tr_b16 a[134:135], v229 offset:28672\n"
"v_mul_f32 v205, s62, v205\n"
"v_mfma_f32_16x16x32_bf16 v[176:179], a[144:147], v[208:211], v[176:179]\n"
"ds_read_b64_tr_b16 a[136:137], v228 offset:25088\n"
"v_mul_f32 v206, s62, v206\n"
"v_mfma_f32_16x16x32_bf16 v[180:183], a[148:151], v[208:211], v[180:183]\n"
"ds_read_b64_tr_b16 a[138:139], v228 offset:29184\n"
"v_mul_f32 v207, s62, v207\n"
"v_mfma_f32_16x16x32_bf16 v[184:187], a[152:155], v[208:211], v[184:187]\n"
"ds_read_b64_tr_b16 a[140:141], v229 offset:25088\n"
"v_xor_b32 v239, 32, v233\n"
"v_mfma_f32_16x16x32_bf16 v[188:191], a[156:159], v[208:211], v[188:191]\n"
"ds_read_b64_tr_b16 a[142:143], v229 offset:29184\n"
"v_exp_f32 v201, v201\n"
"v_mfma_f32_16x16x32_bf16 a[180:183], a[184:187], v[208:211], a[180:183]\n"
"ds_read_b64_tr_b16 a[144:145], v228 offset:25600\n"
"v_cvt_pk_bf16_f32 v200, v200, v201\n"
"v_mfma_f32_16x16x32_bf16 v[208:211], a[96:99], a[32:35], 0\n"
"ds_read_b64_tr_b16 a[146:147], v228 offset:29696\n"
"s_add_u32 s61, s59, 20480\n"
"v_mfma_f32_16x16x32_bf16 v[208:211], a[100:103], a[36:39], v[208:211]\n"
"ds_read_b64_tr_b16 a[148:149], v229 offset:25600\n"
"s_waitcnt lgkmcnt(7)\n"
"buffer_store_dwordx4 a[200:203], v227, s[48:51], s61 offen sc1 nt\n"
"v_mfma_f32_16x16x32_bf16 v[208:211], a[104:107], a[40:43], v[208:211]\n"
"ds_read_b64_tr_b16 a[150:151], v229 offset:29696\n"
"v_exp_f32 v202, v202\n"
"v_mfma_f32_16x16x32_bf16 v[208:211], a[108:111], a[44:47], v[208:211]\n"
"ds_read_b64_tr_b16 a[152:153], v228 offset:26112\n"
"v_exp_f32 v199, v199\n"
"v_mfma_f32_16x16x32_bf16 v[212:215], a[112:115], a[32:35], 0\n"
"ds_read_b64_tr_b16 a[154:155], v228 offset:30208\n"
"buffer_store_dwordx4 a[204:207], v227, s[48:51], s61 offen offset:1024 sc1 nt\n"
"v_mfma_f32_16x16x32_bf16 v[212:215], a[116:119], a[36:39], v[212:215]\n"
"ds_read_b64_tr_b16 a[156:157], v229 offset:26112\n"
"v_cvt_pk_bf16_f32 v195, v198, v199\n"
"v_exp_f32 v203, v203\n"
"v_mfma_f32_16x16x32_bf16 v[212:215], a[120:123], a[40:43], v[212:215]\n"
"ds_read_b64_tr_b16 a[158:159], v229 offset:30208\n"
"v_cvt_pk_bf16_f32 v201, v202, v203\n"
"v_mfma_f32_16x16x32_bf16 v[212:215], a[124:127], a[44:47], v[212:215]\n"
"buffer_store_dwordx4 a[208:211], v227, s[48:51], s61 offen offset:2048 sc1 nt\n"
"v_mul_f32 v208, s62, v208\n"
"v_mfma_f32_16x16x32_bf16 v[0:3], a[128:131], v[192:195], v[0:3]\n"
"v_exp_f32 v204, v204\n"
"v_mfma_f32_16x16x32_bf16 v[4:7], a[132:135], v[192:195], v[4:7]\n"
"v_mul_f32 v209, s62, v209\n"
"s_waitcnt lgkmcnt(3)\n"
"v_mfma_f32_16x16x32_bf16 v[8:11], a[136:139], v[192:195], v[8:11]\n"
"buffer_store_dwordx4 a[212:215], v227, s[48:51], s61 offen offset:3072 sc1 nt\n"
"v_mul_f32 v210, s62, v210\n"
"v_mfma_f32_16x16x32_bf16 v[12:15], a[140:143], v[192:195], v[12:15]\n"
"v_exp_f32 v208, v208\n"
"v_mfma_f32_16x16x32_bf16 v[16:19], a[144:147], v[192:195], v[16:19]\n"
"v_exp_f32 v205, v205\n"
"v_mfma_f32_16x16x32_bf16 v[20:23], a[148:151], v[192:195], v[20:23]\n"
"v_cvt_pk_bf16_f32 v202, v204, v205\n"
"s_waitcnt lgkmcnt(0)\n"
"v_mfma_f32_16x16x32_bf16 v[24:27], a[152:155], v[192:195], v[24:27]\n"
"v_exp_f32 v209, v209\n"
"v_mfma_f32_16x16x32_bf16 v[28:31], a[156:159], v[192:195], v[28:31]\n"
"v_exp_f32 v206, v206\n"
"v_mfma_f32_16x16x32_bf16 a[160:163], a[184:187], v[192:195], a[160:163]\n"
"v_cvt_pk_bf16_f32 v208, v208, v209\n"
"v_mul_f32 v211, s62, v211\n"
"v_mfma_f32_16x16x32_bf16 v[192:195], a[96:99], a[48:51], 0\n"
"v_exp_f32 v210, v210\n"
"v_mfma_f32_16x16x32_bf16 v[192:195], a[100:103], a[52:55], v[192:195]\n"
"s_nop 2\n"
"v_accvgpr_read_b32 v234, a160\n"
"v_rcp_f32 v236, v234\n"
"v_mfma_f32_16x16x32_bf16 v[192:195], a[104:107], a[56:59], v[192:195]\n"
"v_cmp_nle_f32 vcc, s63, v234\n"
"s_or_b64 s[68:69], s[68:69], vcc\n"
"v_mfma_f32_16x16x32_bf16 v[192:195], a[108:111], a[60:63], v[192:195]\n"
"v_exp_f32 v207, v207\n"
"v_cmp_nge_f32 vcc, s64, v234\n"
"s_or_b64 s[68:69], s[68:69], vcc\n"
"v_mfma_f32_16x16x32_bf16 v[196:199], a[112:115], a[48:51], 0\n"
"v_mul_f32 v0, v236, v0\n"
"v_mul_f32 v1, v236, v1\n"
"v_mfma_f32_16x16x32_bf16 v[196:199], a[116:119], a[52:55], v[196:199]\n"
"v_cvt_pk_bf16_f32 v203, v206, v207\n"
"v_exp_f32 v211, v211\n"
"v_mul_f32 v2, v236, v2\n"
"v_mfma_f32_16x16x32_bf16 v[196:199], a[120:123], a[56:59], v[196:199]\n"
"v_mul_f32 v3, v236, v3\n"
"v_cvt_pk_bf16_f32 v0, v0, v1\n"
"v_mul_f32 v16, v236, v16\n"
"v_mfma_f32_16x16x32_bf16 v[196:199], a[124:127], a[60:63], v[196:199]\n"
"v_mul_f32 v212, s62, v212\n"
"v_cvt_pk_bf16_f32 v209, v210, v211\n"
"v_cvt_pk_bf16_f32 v1, v2, v3\n"
"v_mul_f32 v17, v236, v17\n"
"v_mfma_f32_16x16x32_bf16 v[32:35], a[128:131], v[200:203], v[32:35]\n"
"v_mul_f32 v18, v236, v18\n"
"v_mul_f32 v19, v236, v19\n"
"v_cvt_pk_bf16_f32 v16, v16, v17\n"
"v_mfma_f32_16x16x32_bf16 v[36:39], a[132:135], v[200:203], v[36:39]\n"
"v_exp_f32 v212, v212\n"
"v_mul_f32 v4, v236, v4\n"
"v_cvt_pk_bf16_f32 v17, v18, v19\n"
"v_mfma_f32_16x16x32_bf16 v[40:43], a[136:139], v[200:203], v[40:43]\n"
"ds_write2_b64 v233, v[0:1], v[16:17] offset0:0 offset1:16\n"
"v_mul_f32 v192, s62, v192\n"
"v_mul_f32 v5, v236, v5\n"
"v_mul_f32 v6, v236, v6\n"
"v_mfma_f32_16x16x32_bf16 v[44:47], a[140:143], v[200:203], v[44:47]\n"
"v_mul_f32 v213, s62, v213\n"
"v_mul_f32 v7, v236, v7\n"
"v_cvt_pk_bf16_f32 v4, v4, v5\n"
"v_mfma_f32_16x16x32_bf16 v[48:51], a[144:147], v[200:203], v[48:51]\n"
"v_exp_f32 v192, v192\n"
"v_cvt_pk_bf16_f32 v5, v6, v7\n"
"v_mul_f32 v20, v236, v20\n"
"v_mul_f32 v21, v236, v21\n"
"v_mfma_f32_16x16x32_bf16 v[52:55], a[148:151], v[200:203], v[52:55]\n"
"v_exp_f32 v213, v213\n"
"v_mul_f32 v193, s62, v193\n"
"v_mul_f32 v22, v236, v22\n"
"v_mfma_f32_16x16x32_bf16 v[56:59], a[152:155], v[200:203], v[56:59]\n"
"v_mul_f32 v214, s62, v214\n"
"v_mul_f32 v8, v236, v8\n"
"v_mul_f32 v23, v236, v23\n"
"v_cvt_pk_bf16_f32 v20, v20, v21\n"
"v_mfma_f32_16x16x32_bf16 v[60:63], a[156:159], v[200:203], v[60:63]\n"
"v_cvt_pk_bf16_f32 v210, v212, v213\n"
"v_exp_f32 v193, v193\n"
"v_mul_f32 v9, v236, v9\n"
"v_cvt_pk_bf16_f32 v21, v22, v23\n"
"v_mfma_f32_16x16x32_bf16 a[164:167], a[184:187], v[200:203], a[164:167]\n"
"ds_write2_b64 v239, v[4:5], v[20:21] offset0:0 offset1:16\n"
"v_exp_f32 v214, v214\n"
"v_mul_f32 v10, v236, v10\n"
"v_mul_f32 v11, v236, v11\n"
"v_cvt_pk_bf16_f32 v8, v8, v9\n"
"v_mfma_f32_16x16x32_bf16 v[200:203], a[96:99], a[64:67], 0\n"
"v_mul_f32 v194, s62, v194\n"
"v_cvt_pk_bf16_f32 v192, v192, v193\n"
"v_cvt_pk_bf16_f32 v9, v10, v11\n"
"v_mul_f32 v24, v236, v24\n"
"v_mfma_f32_16x16x32_bf16 v[200:203], a[100:103], a[68:71], v[200:203]\n"
"v_mul_f32 v215, s62, v215\n"
"v_mul_f32 v25, v236, v25\n"
"v_mul_f32 v26, v236, v26\n"
"v_accvgpr_read_b32 v235, a164\n"
"v_rcp_f32 v237, v235\n"
"v_mfma_f32_16x16x32_bf16 v[200:203], a[104:107], a[72:75], v[200:203]\n"
"v_exp_f32 v194, v194\n"
"v_mul_f32 v27, v236, v27\n"
"v_cvt_pk_bf16_f32 v24, v24, v25\n"
"v_xor_b32 v240, 64, v233\n"
"v_cmp_nle_f32 vcc, s63, v235\n"
"s_or_b64 s[68:69], s[68:69], vcc\n"
"v_mfma_f32_16x16x32_bf16 v[200:203], a[108:111], a[76:79], v[200:203]\n"
"v_exp_f32 v215, v215\n"
"v_mul_f32 v195, s62, v195\n"
"v_mul_f32 v12, v236, v12\n"
"v_mul_f32 v13, v236, v13\n"
"v_cvt_pk_bf16_f32 v25, v26, v27\n"
"v_cmp_nge_f32 vcc, s64, v235\n"
"s_or_b64 s[68:69], s[68:69], vcc\n"
"v_mfma_f32_16x16x32_bf16 v[204:207], a[112:115], a[64:67], 0\n"
"ds_write2_b64 v240, v[8:9], v[24:25] offset0:0 offset1:16\n"
"v_mul_f32 v14, v236, v14\n"
"v_mul_f32 v32, v237, v32\n"
"v_mul_f32 v33, v237, v33\n"
"v_mfma_f32_16x16x32_bf16 v[204:207], a[116:119], a[68:71], v[204:207]\n"
"v_cvt_pk_bf16_f32 v211, v214, v215\n"
"v_exp_f32 v195, v195\n"
"v_mul_f32 v15, v236, v15\n"
"v_cvt_pk_bf16_f32 v12, v12, v13\n"
"v_mul_f32 v28, v236, v28\n"
"v_mul_f32 v34, v237, v34\n"
"v_mfma_f32_16x16x32_bf16 v[204:207], a[120:123], a[72:75], v[204:207]\n"
"v_cvt_pk_bf16_f32 v13, v14, v15\n"
"v_mul_f32 v29, v236, v29\n"
"v_mul_f32 v35, v237, v35\n"
"v_cvt_pk_bf16_f32 v32, v32, v33\n"
"v_mul_f32 v48, v237, v48\n"
"v_mfma_f32_16x16x32_bf16 v[204:207], a[124:127], a[76:79], v[204:207]\n"
"v_mul_f32 v196, s62, v196\n"
"v_cvt_pk_bf16_f32 v193, v194, v195\n"
"v_mul_f32 v30, v236, v30\n"
"v_mul_f32 v31, v236, v31\n"
"v_cvt_pk_bf16_f32 v28, v28, v29\n"
"v_xor_b32 v241, 96, v233\n"
"v_add_u32 v233, 4096, v232\n"
"v_cvt_pk_bf16_f32 v33, v34, v35\n"
"v_mul_f32 v49, v237, v49\n"
"v_mfma_f32_16x16x32_bf16 v[64:67], a[128:131], v[208:211], v[64:67]\n"
"v_cvt_pk_bf16_f32 v29, v30, v31\n"
"v_mul_f32 v50, v237, v50\n"
"v_mul_f32 v51, v237, v51\n"
"v_cvt_pk_bf16_f32 v48, v48, v49\n"
"v_mfma_f32_16x16x32_bf16 v[68:71], a[132:135], v[208:211], v[68:71]\n"
"ds_write2_b64 v241, v[12:13], v[28:29] offset0:0 offset1:16\n"
"v_exp_f32 v196, v196\n"
"v_mul_f32 v36, v237, v36\n"
"v_cvt_pk_bf16_f32 v49, v50, v51\n"
"v_mfma_f32_16x16x32_bf16 v[72:75], a[136:139], v[208:211], v[72:75]\n"
"ds_write2_b64 v233, v[32:33], v[48:49] offset0:0 offset1:16\n"
"v_mul_f32 v200, s62, v200\n"
"v_mul_f32 v37, v237, v37\n"
"v_mul_f32 v38, v237, v38\n"
"v_mfma_f32_16x16x32_bf16 v[76:79], a[140:143], v[208:211], v[76:79]\n"
"ds_read_b128 a[216:219], v230 offset:0\n"
"v_mul_f32 v197, s62, v197\n"
"v_mul_f32 v39, v237, v39\n"
"v_cvt_pk_bf16_f32 v36, v36, v37\n"
"v_mfma_f32_16x16x32_bf16 v[80:83], a[144:147], v[208:211], v[80:83]\n"
"ds_read_b128 a[220:223], v231 offset:1024\n"
"v_exp_f32 v200, v200\n"
"v_cvt_pk_bf16_f32 v37, v38, v39\n"
"v_mul_f32 v52, v237, v52\n"
"v_mul_f32 v53, v237, v53\n"
"v_mfma_f32_16x16x32_bf16 v[84:87], a[148:151], v[208:211], v[84:87]\n"
"ds_read_b128 a[224:227], v230 offset:2048\n"
"v_exp_f32 v197, v197\n"
"v_mul_f32 v201, s62, v201\n"
"v_mul_f32 v54, v237, v54\n"
"v_mfma_f32_16x16x32_bf16 v[88:91], a[152:155], v[208:211], v[88:91]\n"
"ds_read_b128 a[228:231], v231 offset:3072\n"
"v_mul_f32 v198, s62, v198\n"
"v_mul_f32 v40, v237, v40\n"
"v_mul_f32 v55, v237, v55\n"
"v_cvt_pk_bf16_f32 v52, v52, v53\n"
"v_xor_b32 v239, 32, v233\n"
"v_mfma_f32_16x16x32_bf16 v[92:95], a[156:159], v[208:211], v[92:95]\n"
"v_cvt_pk_bf16_f32 v194, v196, v197\n"
"v_exp_f32 v201, v201\n"
"v_mul_f32 v41, v237, v41\n"
"v_cvt_pk_bf16_f32 v53, v54, v55\n"
"v_mfma_f32_16x16x32_bf16 a[168:171], a[184:187], v[208:211], a[168:171]\n"
"ds_write2_b64 v239, v[36:37], v[52:53] offset0:0 offset1:16\n"
"v_exp_f32 v198, v198\n"
"v_mul_f32 v42, v237, v42\n"
"v_mul_f32 v43, v237, v43\n"
"v_cvt_pk_bf16_f32 v40, v40, v41\n"
"v_mfma_f32_16x16x32_bf16 v[208:211], a[96:99], a[80:83], 0\n"
"v_mul_f32 v202, s62, v202\n"
"v_cvt_pk_bf16_f32 v200, v200, v201\n"
"v_cvt_pk_bf16_f32 v41, v42, v43\n"
"v_mul_f32 v56, v237, v56\n"
"v_mfma_f32_16x16x32_bf16 v[208:211], a[100:103], a[84:87], v[208:211]\n"
"v_mul_f32 v199, s62, v199\n"
"v_mul_f32 v57, v237, v57\n"
"v_mul_f32 v58, v237, v58\n"
"v_accvgpr_read_b32 v234, a168\n"
"v_rcp_f32 v236, v234\n"
"v_mfma_f32_16x16x32_bf16 v[208:211], a[104:107], a[88:91], v[208:211]\n"
"v_exp_f32 v202, v202\n"
"v_mul_f32 v59, v237, v59\n"
"v_cvt_pk_bf16_f32 v56, v56, v57\n"
"v_xor_b32 v240, 64, v233\n"
"v_cmp_nle_f32 vcc, s63, v234\n"
"s_or_b64 s[68:69], s[68:69], vcc\n"
"v_mfma_f32_16x16x32_bf16 v[208:211], a[108:111], a[92:95], v[208:211]\n"
"v_exp_f32 v199, v199\n"
"v_mul_f32 v203, s62, v203\n"
"v_mul_f32 v44, v237, v44\n"
"v_mul_f32 v45, v237, v45\n"
"v_cvt_pk_bf16_f32 v57, v58, v59\n"
"v_cmp_nge_f32 vcc, s64, v234\n"
"s_or_b64 s[68:69], s[68:69], vcc\n"
"v_mfma_f32_16x16x32_bf16 v[212:215], a[112:115], a[80:83], 0\n"
"ds_write2_b64 v240, v[40:41], v[56:57] offset0:0 offset1:16\n"
"v_mul_f32 v46, v237, v46\n"
"v_mul_f32 v64, v236, v64\n"
"v_mul_f32 v65, v236, v65\n"
"v_mfma_f32_16x16x32_bf16 v[212:215], a[116:119], a[84:87], v[212:215]\n"
"v_cvt_pk_bf16_f32 v195, v198, v199\n"
"v_exp_f32 v203, v203\n"
"v_mul_f32 v47, v237, v47\n"
"v_cvt_pk_bf16_f32 v44, v44, v45\n"
"v_mul_f32 v60, v237, v60\n"
"v_mul_f32 v66, v236, v66\n"
"s_add_u32 s94, s59, 98304\n"
"v_mfma_f32_16x16x32_bf16 v[212:215], a[120:123], a[88:91], v[212:215]\n"
"s_waitcnt lgkmcnt(1)\n"
"buffer_store_dwordx4 a[216:219], v227, s[48:51], s94 offen sc1 nt\n"
"v_cvt_pk_bf16_f32 v45, v46, v47\n"
"v_mul_f32 v61, v237, v61\n"
"v_mul_f32 v67, v236, v67\n"
"v_cvt_pk_bf16_f32 v64, v64, v65\n"
"v_mul_f32 v80, v236, v80\n"
"v_mfma_f32_16x16x32_bf16 v[212:215], a[124:127], a[92:95], v[212:215]\n"
"buffer_store_dwordx4 a[220:223], v227, s[48:51], s94 offen offset:1024 sc1 nt\n"
"v_mul_f32 v204, s62, v204\n"
"v_cvt_pk_bf16_f32 v201, v202, v203\n"
"v_mul_f32 v62, v237, v62\n"
"v_mul_f32 v63, v237, v63\n"
"v_cvt_pk_bf16_f32 v60, v60, v61\n"
"v_xor_b32 v241, 96, v233\n"
"v_add_u32 v233, 8192, v232\n"
"v_cvt_pk_bf16_f32 v65, v66, v67\n"
"v_mul_f32 v81, v236, v81\n"
"v_mfma_f32_16x16x32_bf16 v[96:99], a[128:131], v[192:195], v[96:99]\n"
"buffer_store_dwordx4 a[224:227], v227, s[48:51], s94 offen offset:2048 sc1 nt\n"
"v_cvt_pk_bf16_f32 v61, v62, v63\n"
"v_mul_f32 v82, v236, v82\n"
"v_mul_f32 v83, v236, v83\n"
"v_cvt_pk_bf16_f32 v80, v80, v81\n"
"v_mfma_f32_16x16x32_bf16 v[100:103], a[132:135], v[192:195], v[100:103]\n"
"ds_write2_b64 v241, v[44:45], v[60:61] offset0:0 offset1:16\n"
"v_exp_f32 v204, v204\n"
"v_mul_f32 v68, v236, v68\n"
"v_cvt_pk_bf16_f32 v81, v82, v83\n"
"v_mfma_f32_16x16x32_bf16 v[104:107], a[136:139], v[192:195], v[104:107]\n"
"ds_write2_b64 v233, v[64:65], v[80:81] offset0:0 offset1:16\n"
"v_mul_f32 v208, s62, v208\n"
"v_mul_f32 v69, v236, v69\n"
"v_mul_f32 v70, v236, v70\n"
"v_mfma_f32_16x16x32_bf16 v[108:111], a[140:143], v[192:195], v[108:111]\n"
"buffer_store_dwordx4 a[228:231], v227, s[48:51], s94 offen offset:3072 sc1 nt\n"
"v_mul_f32 v205, s62, v205\n"
"v_mul_f32 v71, v236, v71\n"
"v_cvt_pk_bf16_f32 v68, v68, v69\n"
"v_mfma_f32_16x16x32_bf16 v[112:115], a[144:147], v[192:195], v[112:115]\n"
"ds_read_b128 a[232:235], v230 offset:4096\n"
"v_exp_f32 v208, v208\n"
"v_cvt_pk_bf16_f32 v69, v70, v71\n"
"v_mul_f32 v84, v236, v84\n"
"v_mul_f32 v85, v236, v85\n"
"v_mfma_f32_16x16x32_bf16 v[116:119], a[148:151], v[192:195], v[116:119]\n"
"ds_read_b128 a[236:239], v231 offset:5120\n"
"v_exp_f32 v205, v205\n"
"v_mul_f32 v209, s62, v209\n"
"v_mul_f32 v86, v236, v86\n"
"v_mfma_f32_16x16x32_bf16 v[120:123], a[152:155], v[192:195], v[120:123]\n"
"ds_read_b128 a[240:243], v230 offset:6144\n"
"v_mul_f32 v206, s62, v206\n"
"v_mul_f32 v72, v236, v72\n"
"v_mul_f32 v87, v236, v87\n"
"v_cvt_pk_bf16_f32 v84, v84, v85\n"
"v_xor_b32 v239, 32, v233\n"
"v_mfma_f32_16x16x32_bf16 v[124:127], a[156:159], v[192:195], v[124:127]\n"
"ds_read_b128 a[244:247], v231 offset:7168\n"
"v_cvt_pk_bf16_f32 v202, v204, v205\n"
"v_exp_f32 v209, v209\n"
"v_mul_f32 v73, v236, v73\n"
"v_cvt_pk_bf16_f32 v85, v86, v87\n"
"v_mfma_f32_16x16x32_bf16 a[172:175], a[184:187], v[192:195], a[172:175]\n"
"ds_write2_b64 v239, v[68:69], v[84:85] offset0:0 offset1:16\n"
"v_exp_f32 v206, v206\n"
"v_mul_f32 v74, v236, v74\n"
"v_mul_f32 v75, v236, v75\n"
"v_cvt_pk_bf16_f32 v72, v72, v73\n"
"v_mul_f32 v210, s62, v210\n"
"v_cvt_pk_bf16_f32 v208, v208, v209\n"
"v_cvt_pk_bf16_f32 v73, v74, v75\n"
"v_mul_f32 v88, v236, v88\n"
"v_mul_f32 v207, s62, v207\n"
"v_mul_f32 v89, v236, v89\n"
"v_mul_f32 v90, v236, v90\n"
"v_accvgpr_read_b32 v235, a172\n"
"v_rcp_f32 v237, v235\n"
"v_exp_f32 v210, v210\n"
"v_mul_f32 v91, v236, v91\n"
"v_cvt_pk_bf16_f32 v88, v88, v89\n"
"v_xor_b32 v240, 64, v233\n"
"v_cmp_nle_f32 vcc, s63, v235\n"
"s_or_b64 s[68:69], s[68:69], vcc\n"
"v_mul_f32 v211, s62, v211\n"
"v_exp_f32 v207, v207\n"
"v_mul_f32 v76, v236, v76\n"
"v_mul_f32 v77, v236, v77\n"
"v_cvt_pk_bf16_f32 v89, v90, v91\n"
"v_cmp_nge_f32 vcc, s64, v235\n"
"s_or_b64 s[68:69], s[68:69], vcc\n"
"ds_write2_b64 v240, v[72:73], v[88:89] offset0:0 offset1:16\n"
"v_mul_f32 v78, v236, v78\n"
"v_mul_f32 v96, v237, v96\n"
"v_mul_f32 v97, v237, v97\n"
"v_cvt_pk_bf16_f32 v203, v206, v207\n"
"v_exp_f32 v211, v211\n"
"v_mul_f32 v79, v236, v79\n"
"v_cvt_pk_bf16_f32 v76, v76, v77\n"
"v_mul_f32 v92, v236, v92\n"
"v_mul_f32 v98, v237, v98\n"
"s_add_u32 s94, s59, 102400\n"
"s_waitcnt lgkmcnt(1)\n"
"buffer_store_dwordx4 a[232:235], v227, s[48:51], s94 offen sc1 nt\n"
"v_cvt_pk_bf16_f32 v77, v78, v79\n"
"v_mul_f32 v93, v236, v93\n"
"v_mul_f32 v99, v237, v99\n"
"v_cvt_pk_bf16_f32 v96, v96, v97\n"
"v_mul_f32 v112, v237, v112\n"
"buffer_store_dwordx4 a[236:239], v227, s[48:51], s94 offen offset:1024 sc1 nt\n"
"v_mul_f32 v212, s62, v212\n"
"v_cvt_pk_bf16_f32 v209, v210, v211\n"
"v_mul_f32 v94, v236, v94\n"
"v_mul_f32 v95, v236, v95\n"
"v_cvt_pk_bf16_f32 v92, v92, v93\n"
"v_xor_b32 v241, 96, v233\n"
"v_add_u32 v233, 12288, v232\n"
"v_cvt_pk_bf16_f32 v97, v98, v99\n"
"v_mul_f32 v113, v237, v113\n"
"v_mfma_f32_16x16x32_bf16 v[128:131], a[128:131], v[200:203], v[128:131]\n"
"buffer_store_dwordx4 a[240:243], v227, s[48:51], s94 offen offset:2048 sc1 nt\n"
"v_cvt_pk_bf16_f32 v93, v94, v95\n"
"v_mul_f32 v114, v237, v114\n"
"v_mul_f32 v115, v237, v115\n"
"v_cvt_pk_bf16_f32 v112, v112, v113\n"
"v_mfma_f32_16x16x32_bf16 v[132:135], a[132:135], v[200:203], v[132:135]\n"
"ds_write2_b64 v241, v[76:77], v[92:93] offset0:0 offset1:16\n"
"v_exp_f32 v212, v212\n"
"v_mul_f32 v100, v237, v100\n"
"v_cvt_pk_bf16_f32 v113, v114, v115\n"
"v_mfma_f32_16x16x32_bf16 v[136:139], a[136:139], v[200:203], v[136:139]\n"
"ds_write2_b64 v233, v[96:97], v[112:113] offset0:0 offset1:16\n"
"v_mul_f32 v101, v237, v101\n"
"v_mul_f32 v102, v237, v102\n"
"v_mfma_f32_16x16x32_bf16 v[140:143], a[140:143], v[200:203], v[140:143]\n"
"buffer_store_dwordx4 a[244:247], v227, s[48:51], s94 offen offset:3072 sc1 nt\n"
"v_mul_f32 v213, s62, v213\n"
"v_mul_f32 v103, v237, v103\n"
"v_cvt_pk_bf16_f32 v100, v100, v101\n"
"v_mfma_f32_16x16x32_bf16 v[144:147], a[144:147], v[200:203], v[144:147]\n"
"ds_read_b128 a[248:251], v230 offset:8192\n"
"v_cvt_pk_bf16_f32 v101, v102, v103\n"
"v_mul_f32 v116, v237, v116\n"
"v_mul_f32 v117, v237, v117\n"
"v_mfma_f32_16x16x32_bf16 v[148:151], a[148:151], v[200:203], v[148:151]\n"
"ds_read_b128 a[252:255], v231 offset:9216\n"
"v_exp_f32 v213, v213\n"
"v_mul_f32 v118, v237, v118\n"
"v_mfma_f32_16x16x32_bf16 v[152:155], a[152:155], v[200:203], v[152:155]\n"
"ds_read_b128 a[188:191], v230 offset:10240\n"
"v_mul_f32 v214, s62, v214\n"
"v_mul_f32 v104, v237, v104\n"
"v_mul_f32 v119, v237, v119\n"
"v_cvt_pk_bf16_f32 v116, v116, v117\n"
"v_xor_b32 v239, 32, v233\n"
"v_mfma_f32_16x16x32_bf16 v[156:159], a[156:159], v[200:203], v[156:159]\n"
"ds_read_b128 a[192:195], v231 offset:11264\n"
"v_cvt_pk_bf16_f32 v210, v212, v213\n"
"v_mul_f32 v105, v237, v105\n"
"v_cvt_pk_bf16_f32 v117, v118, v119\n"
"v_mfma_f32_16x16x32_bf16 a[176:179], a[184:187], v[200:203], a[176:179]\n"
"ds_write2_b64 v239, v[100:101], v[116:117] offset0:0 offset1:16\n"
"v_exp_f32 v214, v214\n"
"v_mul_f32 v106, v237, v106\n"
"v_mul_f32 v107, v237, v107\n"
"v_cvt_pk_bf16_f32 v104, v104, v105\n"
"v_cvt_pk_bf16_f32 v105, v106, v107\n"
"v_mul_f32 v120, v237, v120\n"
"v_mul_f32 v215, s62, v215\n"
"v_mul_f32 v121, v237, v121\n"
"v_mul_f32 v122, v237, v122\n"
"v_accvgpr_read_b32 v234, a176\n"
"v_rcp_f32 v236, v234\n"
"v_mul_f32 v123, v237, v123\n"
"v_cvt_pk_bf16_f32 v120, v120, v121\n"
"v_xor_b32 v240, 64, v233\n"
"v_cmp_nle_f32 vcc, s63, v234\n"
"s_or_b64 s[68:69], s[68:69], vcc\n"
"v_exp_f32 v215, v215\n"
"v_mul_f32 v108, v237, v108\n"
"v_mul_f32 v109, v237, v109\n"
"v_cvt_pk_bf16_f32 v121, v122, v123\n"
"ds_write2_b64 v240, v[104:105], v[120:121] offset0:0 offset1:16\n"
"v_cmp_nge_f32 vcc, s64, v234\n"
"s_or_b64 s[68:69], s[68:69], vcc\n"
"v_mul_f32 v110, v237, v110\n"
"v_mul_f32 v128, v236, v128\n"
"v_mul_f32 v129, v236, v129\n"
"v_cvt_pk_bf16_f32 v211, v214, v215\n"
"v_mul_f32 v111, v237, v111\n"
"v_cvt_pk_bf16_f32 v108, v108, v109\n"
"v_mul_f32 v124, v237, v124\n"
"v_mul_f32 v130, v236, v130\n"
"s_add_u32 s94, s59, 106496\n"
"v_cvt_pk_bf16_f32 v109, v110, v111\n"
"v_mul_f32 v125, v237, v125\n"
"v_mul_f32 v131, v236, v131\n"
"s_waitcnt lgkmcnt(1)\n"
"buffer_store_dwordx4 a[248:251], v227, s[48:51], s94 offen sc1 nt\n"
"v_cvt_pk_bf16_f32 v128, v128, v129\n"
"v_mul_f32 v144, v236, v144\n"
"v_mul_f32 v126, v237, v126\n"
"v_mul_f32 v127, v237, v127\n"
"buffer_store_dwordx4 a[252:255], v227, s[48:51], s94 offen offset:1024 sc1 nt\n"
"v_cvt_pk_bf16_f32 v124, v124, v125\n"
"v_xor_b32 v241, 96, v233\n"
"v_add_u32 v233, 16384, v232\n"
"v_cvt_pk_bf16_f32 v129, v130, v131\n"
"v_mul_f32 v145, v236, v145\n"
"v_mfma_f32_16x16x32_bf16 v[160:163], a[128:131], v[208:211], v[160:163]\n"
"buffer_store_dwordx4 a[188:191], v227, s[48:51], s94 offen offset:2048 sc1 nt\n"
"v_cvt_pk_bf16_f32 v125, v126, v127\n"
"v_mul_f32 v146, v236, v146\n"
"v_mul_f32 v147, v236, v147\n"
"v_cvt_pk_bf16_f32 v144, v144, v145\n"
"v_mfma_f32_16x16x32_bf16 v[164:167], a[132:135], v[208:211], v[164:167]\n"
"ds_write2_b64 v241, v[108:109], v[124:125] offset0:0 offset1:16\n"
"v_mul_f32 v132, v236, v132\n"
"v_cvt_pk_bf16_f32 v145, v146, v147\n"
"v_mfma_f32_16x16x32_bf16 v[168:171], a[136:139], v[208:211], v[168:171]\n"
"ds_write2_b64 v233, v[128:129], v[144:145] offset0:0 offset1:16\n"
"v_mul_f32 v133, v236, v133\n"
"v_mul_f32 v134, v236, v134\n"
"v_mfma_f32_16x16x32_bf16 v[172:175], a[140:143], v[208:211], v[172:175]\n"
"buffer_store_dwordx4 a[192:195], v227, s[48:51], s94 offen offset:3072 sc1 nt\n"
"v_mul_f32 v135, v236, v135\n"
"v_cvt_pk_bf16_f32 v132, v132, v133\n"
"v_mfma_f32_16x16x32_bf16 v[176:179], a[144:147], v[208:211], v[176:179]\n"
"ds_read_b128 a[196:199], v230 offset:12288\n"
"v_cvt_pk_bf16_f32 v133, v134, v135\n"
"v_mul_f32 v148, v236, v148\n"
"v_mul_f32 v149, v236, v149\n"
"v_mfma_f32_16x16x32_bf16 v[180:183], a[148:151], v[208:211], v[180:183]\n"
"ds_read_b128 a[200:203], v231 offset:13312\n"
"v_mul_f32 v150, v236, v150\n"
"v_mfma_f32_16x16x32_bf16 v[184:187], a[152:155], v[208:211], v[184:187]\n"
"ds_read_b128 a[204:207], v230 offset:14336\n"
"v_mul_f32 v136, v236, v136\n"
"v_mul_f32 v151, v236, v151\n"
"v_cvt_pk_bf16_f32 v148, v148, v149\n"
"v_xor_b32 v239, 32, v233\n"
"v_mfma_f32_16x16x32_bf16 v[188:191], a[156:159], v[208:211], v[188:191]\n"
"ds_read_b128 a[208:211], v231 offset:15360\n"
"v_mul_f32 v137, v236, v137\n"
"v_cvt_pk_bf16_f32 v149, v150, v151\n"
"v_mfma_f32_16x16x32_bf16 a[180:183], a[184:187], v[208:211], a[180:183]\n"
"ds_write2_b64 v239, v[132:133], v[148:149] offset0:0 offset1:16\n"
"v_mul_f32 v138, v236, v138\n"
"v_mul_f32 v139, v236, v139\n"
"v_cvt_pk_bf16_f32 v136, v136, v137\n"
"v_cvt_pk_bf16_f32 v137, v138, v139\n"
"v_mul_f32 v152, v236, v152\n"
"v_mul_f32 v153, v236, v153\n"
"v_mul_f32 v154, v236, v154\n"
"v_accvgpr_read_b32 v235, a180\n"
"v_mul_f32 v155, v236, v155\n"
"v_rcp_f32 v237, v235\n"
"v_cvt_pk_bf16_f32 v152, v152, v153\n"
"v_xor_b32 v240, 64, v233\n"
"v_cmp_nle_f32 vcc, s63, v235\n"
"s_or_b64 s[68:69], s[68:69], vcc\n"
"v_mul_f32 v140, v236, v140\n"
"v_mul_f32 v141, v236, v141\n"
"v_cvt_pk_bf16_f32 v153, v154, v155\n"
"v_cmp_nge_f32 vcc, s64, v235\n"
"s_or_b64 s[68:69], s[68:69], vcc\n"
"v_mul_f32 v142, v236, v142\n"
"ds_write2_b64 v240, v[136:137], v[152:153] offset0:0 offset1:16\n"
"v_mul_f32 v160, v237, v160\n"
"v_mul_f32 v161, v237, v161\n"
"v_mul_f32 v143, v236, v143\n"
"v_cvt_pk_bf16_f32 v140, v140, v141\n"
"v_mul_f32 v156, v236, v156\n"
"v_mul_f32 v162, v237, v162\n"
"s_add_u32 s94, s59, 110592\n"
"v_cvt_pk_bf16_f32 v141, v142, v143\n"
"s_waitcnt lgkmcnt(2)\n"
"buffer_store_dwordx4 a[196:199], v227, s[48:51], s94 offen sc1 nt\n"
"v_mul_f32 v157, v236, v157\n"
"v_mul_f32 v163, v237, v163\n"
"v_cvt_pk_bf16_f32 v160, v160, v161\n"
"v_mul_f32 v176, v237, v176\n"
"buffer_store_dwordx4 a[200:203], v227, s[48:51], s94 offen offset:1024 sc1 nt\n"
"v_mul_f32 v158, v236, v158\n"
"v_mul_f32 v159, v236, v159\n"
"v_cvt_pk_bf16_f32 v156, v156, v157\n"
"v_xor_b32 v241, 96, v233\n"
"v_add_u32 v233, 20480, v232\n"
"buffer_store_dwordx4 a[204:207], v227, s[48:51], s94 offen offset:2048 sc1 nt\n"
"v_cvt_pk_bf16_f32 v161, v162, v163\n"
"v_mul_f32 v177, v237, v177\n"
"v_cvt_pk_bf16_f32 v157, v158, v159\n"
"v_mul_f32 v178, v237, v178\n"
"v_mul_f32 v179, v237, v179\n"
"ds_write2_b64 v241, v[140:141], v[156:157] offset0:0 offset1:16\n"
"v_cvt_pk_bf16_f32 v176, v176, v177\n"
"v_mul_f32 v164, v237, v164\n"
"buffer_store_dwordx4 a[208:211], v227, s[48:51], s94 offen offset:3072 sc1 nt\n"
"v_cvt_pk_bf16_f32 v177, v178, v179\n"
"v_mul_f32 v165, v237, v165\n"
"v_mul_f32 v166, v237, v166\n"
"v_mul_f32 v167, v237, v167\n"
"v_cvt_pk_bf16_f32 v164, v164, v165\n"
"ds_write2_b64 v233, v[160:161], v[176:177] offset0:0 offset1:16\n"
"v_cvt_pk_bf16_f32 v165, v166, v167\n"
"v_mul_f32 v180, v237, v180\n"
"ds_read_b128 a[212:215], v230 offset:16384\n"
"v_mul_f32 v181, v237, v181\n"
"v_mul_f32 v182, v237, v182\n"
"v_mul_f32 v168, v237, v168\n"
"v_mul_f32 v183, v237, v183\n"
"v_cvt_pk_bf16_f32 v180, v180, v181\n"
"ds_read_b128 a[216:219], v231 offset:17408\n"
"v_xor_b32 v239, 32, v233\n"
"v_mul_f32 v169, v237, v169\n"
"v_cvt_pk_bf16_f32 v181, v182, v183\n"
"v_mul_f32 v170, v237, v170\n"
"v_mul_f32 v171, v237, v171\n"
"ds_write2_b64 v239, v[164:165], v[180:181] offset0:0 offset1:16\n"
"v_cvt_pk_bf16_f32 v168, v168, v169\n"
"v_cvt_pk_bf16_f32 v169, v170, v171\n"
"ds_read_b128 a[220:223], v230 offset:18432\n"
"v_mul_f32 v184, v237, v184\n"
"v_mul_f32 v185, v237, v185\n"
"v_mul_f32 v186, v237, v186\n"
"v_mul_f32 v187, v237, v187\n"
"v_cvt_pk_bf16_f32 v184, v184, v185\n"
"ds_read_b128 a[224:227], v231 offset:19456\n"
"v_xor_b32 v240, 64, v233\n"
"v_mul_f32 v172, v237, v172\n"
"v_mul_f32 v173, v237, v173\n"
"v_cvt_pk_bf16_f32 v185, v186, v187\n"
"v_mul_f32 v174, v237, v174\n"
"ds_write2_b64 v240, v[168:169], v[184:185] offset0:0 offset1:16\n"
"v_mul_f32 v175, v237, v175\n"
"v_cvt_pk_bf16_f32 v172, v172, v173\n"
"v_mul_f32 v188, v237, v188\n"
"s_add_u32 s94, s59, 114688\n"
"v_cvt_pk_bf16_f32 v173, v174, v175\n"
"v_mul_f32 v189, v237, v189\n"
"v_mul_f32 v190, v237, v190\n"
"v_mul_f32 v191, v237, v191\n"
"v_cvt_pk_bf16_f32 v188, v188, v189\n"
"v_xor_b32 v241, 96, v233\n"
"v_cvt_pk_bf16_f32 v189, v190, v191\n"
"ds_write2_b64 v241, v[172:173], v[188:189] offset0:0 offset1:16\n"
"s_waitcnt lgkmcnt(4)\n"
"buffer_store_dwordx4 a[212:215], v227, s[48:51], s94 offen sc1 nt\n"
"buffer_store_dwordx4 a[216:219], v227, s[48:51], s94 offen offset:1024 sc1 nt\n"
"s_waitcnt lgkmcnt(2)\n"
"buffer_store_dwordx4 a[220:223], v227, s[48:51], s94 offen offset:2048 sc1 nt\n"
"buffer_store_dwordx4 a[224:227], v227, s[48:51], s94 offen offset:3072 sc1 nt\n"
"ds_read_b128 a[228:231], v230 offset:20480\n"
"s_add_u32 s94, s59, 118784\n"
"ds_read_b128 a[232:235], v231 offset:21504\n"
"ds_read_b128 a[236:239], v230 offset:22528\n"
"ds_read_b128 a[240:243], v231 offset:23552\n"
"s_waitcnt lgkmcnt(2)\n"
"buffer_store_dwordx4 a[228:231], v227, s[48:51], s94 offen sc1 nt\n"
"buffer_store_dwordx4 a[232:235], v227, s[48:51], s94 offen offset:1024 sc1 nt\n"
"s_waitcnt lgkmcnt(0)\n"
"buffer_store_dwordx4 a[236:239], v227, s[48:51], s94 offen offset:2048 sc1 nt\n"
"buffer_store_dwordx4 a[240:243], v227, s[48:51], s94 offen offset:3072 sc1 nt\n"
"s_cmp_lg_u64 s[66:67], 0\n"
"s_cselect_b32 s70, 1, 0\n"
"s_cmp_lg_u64 s[68:69], 0\n"
"s_cselect_b32 s71, 2, 0\n"
"s_or_b32 %[flags], s70, s71\n"
"s_cmp_lg_u32 %[flags], 0\n"
"s_cbranch_scc1 Lslow%=\n"
"s_waitcnt vmcnt(0) lgkmcnt(0)\n"
"s_endpgm\n"
"Lslow%=:\n"
"s_waitcnt vmcnt(0) lgkmcnt(0)\n"
"Lend%=:\n"
      : [flags] "=s"(flags)
      : [ql] "s"((unsigned)qa), [qh] "s"((unsigned)(qa >> 32)), [kl] "s"((unsigned)ka), [kh] "s"((unsigned)(ka >> 32)),
        [vl] "s"((unsigned)va), [vh] "s"((unsigned)(va >> 32)), [ol] "s"((unsigned)oa), [oh] "s"((unsigned)(oa >> 32)),
        [wid] "s"(wid), [bid] "s"(bid), [pid] "s"(pid), [ldsb] "s"(ldsb)
      : "memory", "vcc", "scc",
      "v0","v1","v2","v3","v4","v5","v6","v7","v8","v9","v10","v11","v12","v13","v14","v15","v16","v17","v18","v19","v20","v21","v22","v23","v24","v25",
      "v26","v27","v28","v29","v30","v31","v32","v33","v34","v35","v36","v37","v38","v39","v40","v41","v42","v43","v44","v45","v46","v47","v48","v49","v50",
      "v51","v52","v53","v54","v55","v56","v57","v58","v59","v60","v61","v62","v63","v64","v65","v66","v67","v68","v69","v70","v71","v72","v73","v74","v75",
      "v76","v77","v78","v79","v80","v81","v82","v83","v84","v85","v86","v87","v88","v89","v90","v91","v92","v93","v94","v95","v96","v97","v98","v99",
      "v100","v101","v102","v103","v104","v105","v106","v107","v108","v109","v110","v111","v112","v113","v114","v115","v116","v117","v118","v119","v120",
      "v121","v122","v123","v124","v125","v126","v127","v128","v129","v130","v131","v132","v133","v134","v135","v136","v137","v138","v139","v140","v141",
      "v142","v143","v144","v145","v146","v147","v148","v149","v150","v151","v152","v153","v154","v155","v156","v157","v158","v159","v160","v161","v162",
      "v163","v164","v165","v166","v167","v168","v169","v170","v171","v172","v173","v174","v175","v176","v177","v178","v179","v180","v181","v182","v183",
      "v184","v185","v186","v187","v188","v189","v190","v191","v192","v193","v194","v195","v196","v197","v198","v199","v200","v201","v202","v203","v204",
      "v205","v206","v207","v208","v209","v210","v211","v212","v213","v214","v215","v216","v217","v218","v219","v220","v221","v222","v223","v224","v225",
      "v226","v227","v228","v229","v230","v231","v232","v233","v234","v235","v236","v237","v238","v239","v240","v241","v242","v243","v244","v245","v246",
      "v247","v248","v249","v250","v251","v252","v253","v254","v255","a0","a1","a2","a3","a4","a5","a6","a7","a8","a9","a10","a11","a12","a13","a14","a15",
      "a16","a17","a18","a19","a20","a21","a22","a23","a24","a25","a26","a27","a28","a29","a30","a31","a32","a33","a34","a35","a36","a37","a38","a39","a40",
      "a41","a42","a43","a44","a45","a46","a47","a48","a49","a50","a51","a52","a53","a54","a55","a56","a57","a58","a59","a60","a61","a62","a63","a64","a65",
      "a66","a67","a68","a69","a70","a71","a72","a73","a74","a75","a76","a77","a78","a79","a80","a81","a82","a83","a84","a85","a86","a87","a88","a89","a90",
      "a91","a92","a93","a94","a95","a96","a97","a98","a99","a100","a101","a102","a103","a104","a105","a106","a107","a108","a109","a110","a111","a112",
      "a113","a114","a115","a116","a117","a118","a119","a120","a121","a122","a123","a124","a125","a126","a127","a128","a129","a130","a131","a132","a133",
      "a134","a135","a136","a137","a138","a139","a140","a141","a142","a143","a144","a145","a146","a147","a148","a149","a150","a151","a152","a153","a154",
      "a155","a156","a157","a158","a159","a160","a161","a162","a163","a164","a165","a166","a167","a168","a169","a170","a171","a172","a173","a174","a175",
      "a176","a177","a178","a179","a180","a181","a182","a183","a184","a185","a186","a187","a188","a189","a190","a191","a192","a193","a194","a195","a196",
      "a197","a198","a199","a200","a201","a202","a203","a204","a205","a206","a207","a208","a209","a210","a211","a212","a213","a214","a215","a216","a217",
      "a218","a219","a220","a221","a222","a223","a224","a225","a226","a227","a228","a229","a230","a231","a232","a233","a234","a235","a236","a237","a238",
      "a239","a240","a241","a242","a243","a244","a245","a246","a247","a248","a249","a250","a251","a252","a253","a254","a255","s36","s37","s38","s39","s40",
      "s41","s42","s43","s44","s45","s46","s47","s48","s49","s50","s51","s52","s53","s54","s55","s56","s57","s58","s59","s60","s61","s62","s63","s64","s65",
      "s66","s67","s68","s69","s70","s71","s72","s73","s74","s75","s76","s77","s78","s79","s80","s81","s82","s83","s84","s85","s86","s87","s88","s89","s90",
      "s91","s92","s93","s94","s95");
  if (flags) {
    unsigned lane;
    asm volatile("v_mbcnt_lo_u32_b32 %0, -1, 0\n\tv_mbcnt_hi_u32_b32 %0, -1, %0" : "=v"(lane));
#pragma unroll 1
    for (int j = 0; j < 2; ++j) {
      if (!((flags >> j) & 1u)) continue;
      const size_t head = ((size_t)pid * 2 + j) * S * D, rows = head + (size_t)wid * 96 * D;
      attn_true_max(q + rows, k + head, v + head, o + rows, (int)lane);
    }
  }
}

}

extern "C" void kernel_launch(void* const* d_in, const int* in_sizes, int n_in, void* d_out, int out_size, void* d_ws, size_t ws_size,
                              hipStream_t stream) {
  (void)in_sizes; (void)n_in; (void)out_size; (void)d_ws; (void)ws_size;
  (void)hipFuncSetAttribute((const void*)attn_fwd_kernel, hipFuncAttributeMaxDynamicSharedMemorySize, (int)LDS_BYTES);
  hipLaunchKernelGGL(attn_fwd_kernel, dim3(NWG), dim3(256), LDS_BYTES, stream, (const unsigned short*)d_in[0],
                     (const unsigned short*)d_in[1], (const unsigned short*)d_in[2], (unsigned short*)d_out);
}
```
